```python
import torch

def _softmax(x, axis=-1):
    return torch.softmax(x, dim=axis)
def _gelu(x):
    return torch.nn.functional.gelu(x, approximate="tanh")
def _silu(x):
    return torch.nn.functional.silu(x)
def _sigmoid(x):
    return torch.sigmoid(x)
def _layernorm(x, axis=-1, eps=1e-5):
    m = x.mean(dim=axis, keepdim=True)
    v = x.var(dim=axis, keepdim=True, unbiased=False)
    return (x - m) / torch.sqrt(v + eps)
def _scatter_add(tgt, idx, src, axis=0):
    return tgt.clone().index_add_(axis, idx, src)
def _segment_sum(src, seg, n):
    out = torch.zeros((n,) + src.shape[1:], dtype=src.dtype)
    return out.index_add_(0, seg, src)
def _pad(x, pw):
    flat = tuple(v for pair in reversed(pw) for v in pair)
    return torch.nn.functional.pad(x, flat)

kernel_name = 'concat_heads__mha_block__ssm_conv_s4d__prompt'

def setup_inputs():

    _w = (lambda a: (lambda t: t.to(torch.bfloat16) if t.is_floating_point() else t)(torch.as_tensor(a)))
    def _A_setup():
        B, S, D, H = 1, 2048, 448, 16
        return {
            "x": _w((lambda *s: torch.randn(*s, dtype=torch.bfloat16) * 0.2)(B, S, D)),
            "wqkv": _w((lambda *s: torch.randn(*s, dtype=torch.bfloat16) * 0.2)(D, 3 * D)),
            "wo": _w((lambda *s: torch.randn(*s, dtype=torch.bfloat16) * 0.05)(D, D)),
        }
    _A_in = _A_setup()

    def _B_setup():
        Bsz, L, D, N = 1, 2048, 448, 256
        return {
            "u": _w((lambda *s: torch.randn(*s, dtype=torch.bfloat16) * 0.05)(Bsz, L, D)),
            "log_A_real": _w((lambda *s: torch.randn(*s, dtype=torch.bfloat16) * 6.4)(D, N) * 0.5),
            "A_imag": _w((lambda *s: torch.randn(*s, dtype=torch.bfloat16) * 0.1)(D, N) * 3.14),
            "C_real": _w((lambda *s: torch.randn(*s, dtype=torch.bfloat16) * 0.4)(D, N) / N ** 0.5),
            "C_imag": _w((lambda *s: torch.randn(*s, dtype=torch.bfloat16) * 0.4)(D, N) / N ** 0.5),
            "log_dt": _w((lambda *s: torch.randn(*s, dtype=torch.bfloat16) * 0.1)(D) * 0.5 - 4.0),
            "D_skip": _w((lambda *s: torch.randn(*s, dtype=torch.bfloat16) * 0.1)(D)),
        }
    _B_in = _B_setup()

    return {
        "A_x": _A_in["x"],
        "A_wqkv": _A_in["wqkv"],
        "A_wo": _A_in["wo"],
        "B_u": _B_in["u"],
        "B_log_A_real": _B_in["log_A_real"],
        "B_A_imag": _B_in["A_imag"],
        "B_C_real": _B_in["C_real"],
        "B_C_imag": _B_in["C_imag"],
        "B_log_dt": _B_in["log_dt"],
        "B_D_skip": _B_in["D_skip"],
    }

def reference(A_x, A_wqkv, A_wo, B_u, B_log_A_real, B_A_imag, B_C_real, B_C_imag, B_log_dt, B_D_skip):
    def _A_ref(x, wqkv, wo):
        B, S, D = x.shape
        H = 16
        Dh = D // H
        qkv = (lambda x, *d: x.permute(*d))((x @ wqkv).reshape(B, S, 3, H, Dh), 2, 0, 3, 1, 4)
        q, k, v = qkv[0], qkv[1], qkv[2]
        scores = torch.einsum("bhqd,bhkd->bhqk", q, k) * (Dh ** -0.5)
        p = _softmax(scores, axis=-1)
        o = torch.einsum("bhqk,bhkd->bhqd", p, v)
        return (lambda x, *d: x.permute(*d))(o, 0, 2, 1, 3).reshape(B, S, D) @ wo

    def _B_ref(u, log_A_real, A_imag, C_real, C_imag, log_dt, D_skip):
        L = u.shape[1]
        dt = torch.exp(log_dt)
        A = -torch.exp(log_A_real) + 1j * A_imag
        C = (C_real + 1j * C_imag) * dt[:, None]
        pos = (lambda x, dt: x.to(dt))(torch.arange(L), log_dt.dtype)
        P = torch.exp((A * dt[:, None])[:, :, None] * pos[None, None, :])
        K = 2.0 * torch.einsum("dn,dnl->dl", C, P).real
        L2 = 2 * L
        u_t = (lambda x, *d: x.permute(*d))(u, 0, 2, 1)
        Kf = (lambda x, *a, **k: torch.fft.rfft(x.float(), *a, **k))(K, L2)
        Uf = (lambda x, *a, **k: torch.fft.rfft(x.float(), *a, **k))(u_t, L2)
        y = (lambda x, *a, **k: torch.fft.irfft(x, *a, **k).to(torch.bfloat16))(Uf * Kf[None, :, :], L2)[:, :, :L]
        y = (lambda x, *d: x.permute(*d))(y, 0, 2, 1)
        return y + u * D_skip[None, None, :]

    a = _A_ref(A_x, A_wqkv, A_wo)
    b = _B_ref(B_u, B_log_A_real, B_A_imag, B_C_real, B_C_imag, B_log_dt, B_D_skip)
    return torch.concatenate([a, b], axis=-1)
```

```cpp
#include <hip/hip_runtime.h>
#include <hip/hip_bf16.h>
#include <stdint.h>

typedef short s16x8 __attribute__((ext_vector_type(8)));
typedef short s16x4 __attribute__((ext_vector_type(4)));
typedef short s16x2 __attribute__((ext_vector_type(2)));
typedef _Float16 h16x8 __attribute__((ext_vector_type(8)));
typedef _Float16 h16x2 __attribute__((ext_vector_type(2)));
typedef __bf16 b16x2 __attribute__((ext_vector_type(2)));
typedef float f32x16 __attribute__((ext_vector_type(16)));
typedef float f32x4 __attribute__((ext_vector_type(4)));
typedef float f32x2 __attribute__((ext_vector_type(2)));
typedef unsigned u32x4 __attribute__((ext_vector_type(4)));
typedef unsigned u32x2 __attribute__((ext_vector_type(2)));
typedef unsigned long long u64;
#define LAS __attribute__((address_space(3)))
#define GAS __attribute__((address_space(1)))
#if defined(__HIP_DEVICE_COMPILE__)
#define NOPK __attribute__((target("no-packed-fp32-ops")))
#else
#define NOPK
#endif
#define DI NOPK __device__ __forceinline__
#define SBAR() __builtin_amdgcn_sched_barrier(0)
#define PIN(x) asm volatile("" : "+v"(x))
#define MFMA32(a, b, c) __builtin_amdgcn_mfma_f32_32x32x16_bf16((a), (b), (c), 0, 0, 0)
#define MFMA32H(a, b, c) __builtin_amdgcn_mfma_f32_32x32x16_f16((a), (b), (c), 0, 0, 0)
#define MFMA16(a, b, c) __builtin_amdgcn_mfma_f32_16x16x32_bf16((a), (b), (c), 0, 0, 0)
#define AUX_SC1 16

#define PREFETCH 1
#define WSEND 0
#define STAMPS 0

constexpr unsigned WS_EPOCH = 0;
constexpr unsigned WS_QFLAG = 4096;
constexpr unsigned WS_OFLAG = WS_QFLAG + 256 * 128;
constexpr unsigned WS_UFLAG = WS_OFLAG + 1024 * 128;
constexpr unsigned WS_YFLAG = WS_UFLAG + 256 * 128;
constexpr unsigned WS_KM = WS_YFLAG + 256 * 128;
constexpr unsigned WS_QN = WS_KM + 4096;
constexpr unsigned WS_QKV = 393216;
constexpr unsigned WS_KV = WS_QKV + 16 * 64 * 2048;
constexpr unsigned WS_O = WS_QKV + 48 * 64 * 2048;
constexpr unsigned WS_UT = WS_O + 64 * 16 * 2048;
constexpr unsigned WS_YT = WS_UT + 224 * 8192;
constexpr unsigned WS_DBG = WS_YT + 224 * 8192;
constexpr unsigned WS_END = WS_DBG + 256 * 8 * 16 * 8;
static_assert(WS_QN + 16 * 2048 * 4 <= WS_QKV, "ws map");

constexpr unsigned L_RING = 0, SLOT_B = 28672, XCH_B = 16384;
constexpr unsigned L_B = 100352;
constexpr unsigned TAB_W = 14000, TAB_HI = 8448;
constexpr unsigned L_T = L_B + 56000, T_B = 3088;
constexpr unsigned KC_S = 4160, UC_S = 4640, CH_B = 4 * KC_S + 2 * UC_S;
constexpr unsigned L_YIMG = L_B + 49152;
constexpr unsigned L_UIMG = 147456;
constexpr unsigned L_CTL = 163584;
constexpr unsigned LDS_BYTES = 163840;
constexpr unsigned C_A = L_CTL, C_B = L_CTL + 4, C_GO = L_CTL + 8, C_APUB = L_CTL + 12, C_BPUB = L_CTL + 16, C_WO = L_CTL + 20, C_BFREE = L_CTL + 24, C_BTAG = L_CTL + 28, C_U = L_CTL + 36;
constexpr unsigned C_L = L_CTL + 64, C_R = L_CTL + 96;
constexpr unsigned L_MAX = 162816;
static_assert(L_T + 2 * T_B <= L_MAX, "lds map");

constexpr unsigned SPIN_CAP = 1u << 22;
constexpr float LOG2E = 1.4426950408889634f;
constexpr float QSCALE = 0.18898223650461362f * LOG2E;
constexpr float SAFE_BOUND = 48.0f;

template <int V> struct IC { static constexpr int value = V; };
DI unsigned f2u(float f) { return __builtin_bit_cast(unsigned, f); }
DI float u2f(unsigned u) { return __builtin_bit_cast(float, u); }
DI unsigned umax(unsigned a, unsigned b) { return a > b ? a : b; }
DI float fexp(float x) { return __builtin_amdgcn_exp2f(x * LOG2E); }
DI float bf_lo(unsigned v) { return u2f(v << 16); }
DI float bf_hi(unsigned v) { return u2f(v & 0xffff0000u); }
DI unsigned pk_bf16(float a, float b) { f32x2 v = {a, b}; return __builtin_bit_cast(unsigned, __builtin_convertvector(v, b16x2)); }
DI unsigned pk_f16(float a, float b) { f32x2 v = {a, b}; return __builtin_bit_cast(unsigned, __builtin_convertvector(v, h16x2)); }
DI float bf_round(float a) { return bf_lo(pk_bf16(a, a)); }
template <int OFF> DI void glds(unsigned voff, const char* sbase, unsigned lds) {
  asm volatile("s_add_u32 m0, %2, %3\n\ts_nop 0\n\tglobal_load_lds_dwordx4 %0, %1" :: "v"(voff), "s"(sbase), "s"(lds), "i"(OFF) : "memory", "scc");
}
DI s16x4 tr_read(unsigned a) { return __builtin_bit_cast(s16x4, __builtin_amdgcn_ds_read_tr16_b64_v4i16((LAS s16x4*)(uintptr_t)a)); }
DI s16x8 cat8(s16x4 lo, s16x4 hi) { return __builtin_shufflevector(lo, hi, 0, 1, 2, 3, 4, 5, 6, 7); }
template <class T> DI T lds_ld(unsigned a) { return *(LAS T*)(uintptr_t)a; }
template <class T> DI void lds_st(unsigned a, T v) { *(LAS T*)(uintptr_t)a = v; }
#define LDS_POLL(V, A, S, N, T, EXTRA) "s_mov_b32 " N ", 0\n1:\n\t" EXTRA "ds_read_b32 " V ", " A "\n\ts_add_u32 " N ", " N ", 1024\n\ts_cselect_b32 " S ", -1, 0\n\t" \
  "s_waitcnt lgkmcnt(0)\n\tv_or_b32 " V ", " S ", " V "\n\ts_nop 0\n\tv_readfirstlane_b32 " S ", " V "\n\ts_cmp_lt_u32 " S ", " T "\n\ts_cbranch_scc1 1b\n\t"
DI void lds_wait_ge(unsigned a, unsigned target) {
  unsigned v, sv, n;
  asm volatile(LDS_POLL("%0", "%3", "%1", "%2", "%4", "") : "=&v"(v), "=&s"(sv), "=&s"(n) : "v"(a), "s"(target) : "memory", "scc");
}
DI void lds_wait_ge_slow(unsigned a, unsigned target) {
  unsigned v, sv, n;
  asm volatile(LDS_POLL("%0", "%3", "%1", "%2", "%4", "s_sleep 1\n\t") : "=&v"(v), "=&s"(sv), "=&s"(n) : "v"(a), "s"(target) : "memory", "scc");
}
DI void lds_arrive(unsigned a) {
  asm volatile("s_waitcnt lgkmcnt(0)\n\ts_mov_b64 exec, 1\n\tds_add_u32 %0, %1\n\ts_mov_b64 exec, -1" :: "v"(a), "v"(1u) : "memory");
}
DI void lds_arrive_nw(unsigned a) {
  asm volatile("s_mov_b64 exec, 1\n\tds_add_u32 %0, %1\n\ts_mov_b64 exec, -1" :: "v"(a), "v"(1u) : "memory");
}
DI void role_sync(unsigned a, unsigned nth) { lds_arrive(a); lds_wait_ge(a, nth * 4u); }
DI void role_sync_slow(unsigned a, unsigned nth) { lds_arrive(a); lds_wait_ge_slow(a, nth * 4u); }
DI bool last_of_four(unsigned a, int lane, unsigned round) {
  unsigned old = 0;
  if (lane == 0) old = __hip_atomic_fetch_add((LAS unsigned*)(uintptr_t)a, 1u, __ATOMIC_RELAXED, __HIP_MEMORY_SCOPE_WORKGROUP);
  return (unsigned)__builtin_amdgcn_readfirstlane(old) == round * 4u + 3u;
}
DI void st128(u32x4 v, __amdgpu_buffer_rsrc_t r, unsigned voff, unsigned soff) {
  __builtin_amdgcn_raw_buffer_store_b128(v, r, voff, soff, AUX_SC1);
  asm volatile("s_nop 1" :: "v"(v) : "memory");
}
DI void drain_vm() { asm volatile("s_waitcnt vmcnt(0)" ::: "memory"); }
DI void flag_store(char* ws, unsigned off, u64 tag) { __hip_atomic_store((GAS u64*)(ws + off), tag, __ATOMIC_RELAXED, __HIP_MEMORY_SCOPE_AGENT); }
DI void flag_wait(char* ws, unsigned off, u64 tag) {
#pragma unroll 1
  for (unsigned n = 0; n < SPIN_CAP; ++n) {
    u64 v = __hip_atomic_load((GAS u64*)(ws + off), __ATOMIC_RELAXED, __HIP_MEMORY_SCOPE_AGENT);
    if (__builtin_amdgcn_ballot_w64(v != tag) == 0ull) break;
    __builtin_amdgcn_s_sleep(2);
  }
  asm volatile("" ::: "memory");
}
template <int CTRL> DI unsigned dpp_max(unsigned v) { unsigned t = __builtin_amdgcn_update_dpp(0u, v, CTRL, 0xf, 0xf, false); return v > t ? v : t; }
DI unsigned wave_umax(unsigned v) {
  v = dpp_max<0x111>(v); v = dpp_max<0x112>(v); v = dpp_max<0x114>(v); v = dpp_max<0x118>(v);
  v = dpp_max<0x142>(v); v = dpp_max<0x143>(v);
  return __builtin_amdgcn_readlane(v, 63);
}
DI float half_swap_add(float v) {
  auto r = __builtin_amdgcn_permlane32_swap(f2u(v), f2u(v), false, false);
  return u2f(r[0]) + u2f(r[1]);
}
DI float half_swap_max(float v) {
  auto r = __builtin_amdgcn_permlane32_swap(f2u(v), f2u(v), false, false);
  return __builtin_fmaxf(u2f(r[0]), u2f(r[1]));
}
DI f32x2 cmul(f32x2 a, f32x2 b) { f32x2 r; r.x = __builtin_fmaf(-a.y, b.y, a.x * b.x); r.y = __builtin_fmaf(a.y, b.x, a.x * b.y); return r; }
DI f32x2 zpow(float rho, float th, float p) {
  const float e = __builtin_amdgcn_exp2f(rho * p), f = __builtin_amdgcn_fractf(th * p);
  f32x2 r; r.x = e * __builtin_amdgcn_cosf(f); r.y = e * __builtin_amdgcn_sinf(f); return r;
}
#define STAMP(k) do { } while (0)
#define STAMP_FLUSH() do { } while (0)

DI void qkv_issue(const unsigned (&xo)[4], const unsigned (&wo_)[3], const char* xb, const char* wb, unsigned xdst, unsigned wdst) {
  glds<0>(xo[0], xb, xdst); glds<1024>(xo[1], xb, xdst); glds<2048>(xo[2], xb, xdst); glds<3072>(xo[3], xb, xdst);
  glds<0>(wo_[0], wb, wdst); glds<1024>(wo_[1], wb, wdst); glds<2048>(wo_[2], wb, wdst);
}
#define QR(a, n) "v[" #a "+" #n ":" #a "+" #n "+1]"
#define Q_READ(F, XA, S) "ds_read_b128 v[" #F ":" #F "+3], " XA "\n\t" \
  "ds_read_b64_tr_b16 " QR(F, 4) ", %[wa] offset:" #S "*3072\n\tds_read_b64_tr_b16 " QR(F, 6) ", %[wa] offset:" #S "*3072+768\n\t" \
  "ds_read_b64_tr_b16 " QR(F, 8) ", %[wa] offset:" #S "*3072+56\n\tds_read_b64_tr_b16 " QR(F, 10) ", %[wa] offset:" #S "*3072+824\n\t" \
  "ds_read_b64_tr_b16 " QR(F, 12) ", %[wa] offset:" #S "*3072+112\n\tds_read_b64_tr_b16 " QR(F, 14) ", %[wa] offset:" #S "*3072+880\n\t"
#define Q_T(ACC, F, n) "v_mfma_f32_32x32x16_bf16 " ACC ", v[" #F "+" #n ":" #F "+" #n "+3], v[" #F ":" #F "+3], " ACC "\n\t"
#define Q_N(ACC, F, n) "v_mfma_f32_32x32x16_bf16 " ACC ", v[" #F ":" #F "+3], v[" #F "+" #n ":" #F "+" #n "+3], " ACC "\n\t"
#define Q_MMA(F, M0, M1, M2) M0("%[a0]", F, 4) M1("%[a1]", F, 8) M2("%[a2]", F, 12)
#define Q_ARRIVE "s_mov_b64 exec, 1\n\tds_add_u32 %[ca], %[one]\n\ts_mov_b64 exec, -1\n\t"
#define QKV_BODY(M0, M1, M2) \
  "2:\n\t" \
  Q_READ(132, "%[x2]", 2) \
  "s_waitcnt lgkmcnt(14)\n\t" Q_MMA(100, M0, M1, M2) "s_waitcnt lgkmcnt(7)\n\t" Q_READ(148, "%[x3]", 3) \
  "s_cmp_eq_u32 %[tg], 28\n\ts_cbranch_scc1 5f\n\t" \
  Q_ARRIVE "s_cmp_eq_u32 %[tg], 16\n\ts_cselect_b32 %[ts], %[wrap], %[step]\n\ts_add_u32 %[tg], %[tg], 4\n\t" \
  Q_MMA(116, M0, M1, M2) \
  "v_add_u32 %[ca], 4, %[ca]\n\tv_add_u32 %[cl], 4, %[cl]\n\t" \
  "v_add_u32 %[x0], %[ts], %[x0]\n\tv_add_u32 %[x1], %[ts], %[x1]\n\tv_add_u32 %[x2], %[ts], %[x2]\n\tv_add_u32 %[x3], %[ts], %[x3]\n\tv_add_u32 %[wa], %[ts], %[wa]\n\t" \
  LDS_POLL("%[tv]", "%[cl]", "%[ts]", "%[tn]", "4", "") \
  Q_READ(100, "%[x0]", 0) Q_MMA(132, M0, M1, M2) Q_READ(116, "%[x1]", 1) Q_MMA(148, M0, M1, M2) \
  "s_branch 2b\n" \
  "5:\n\t" \
  Q_MMA(116, M0, M1, M2) "s_waitcnt lgkmcnt(7)\n\t" Q_MMA(132, M0, M1, M2) "s_waitcnt lgkmcnt(0)\n\t" Q_ARRIVE "s_nop 0\n\t" Q_MMA(148, M0, M1, M2)
#define QKV_LOOP() asm volatile( \
  "s_mov_b32 %[tg], 4\n\t" LDS_POLL("%[tv]", "%[cl]", "%[ts]", "%[tn]", "4", "") \
  Q_READ(100, "%[x0]", 0) Q_READ(116, "%[x1]", 1) \
  "s_cmp_gt_u32 %[g], 10\n\ts_cbranch_scc1 7f\n\ts_cmp_eq_u32 %[g], 10\n\ts_cbranch_scc1 6f\n" \
  QKV_BODY(Q_T, Q_T, Q_T) "s_branch 8f\n6:\n" QKV_BODY(Q_T, Q_T, Q_N) "s_branch 8f\n7:\n" QKV_BODY(Q_N, Q_N, Q_N) \
  "8:\n\ts_nop 7\n\ts_nop 7" \
  : [a0] "+v"(acc[0]), [a1] "+v"(acc[1]), [a2] "+v"(acc[2]), [x0] "+v"(xa0), [x1] "+v"(xa1), [x2] "+v"(xa2), [x3] "+v"(xa3), [wa] "+v"(wa), \
    [tv] "=&v"(tv), [ts] "=&s"(ts), [tn] "=&s"(tn), [tg] "=&s"(tg), [ca] "+v"(car), [cl] "+v"(cla) \
  : [one] "v"(1u), [step] "s"(SLOT_B), [wrap] "s"(0u - 3u * SLOT_B), [g] "s"(g) \
  : "memory", "scc", V100_163)
#define V10(p) "v" #p "0", "v" #p "1", "v" #p "2", "v" #p "3", "v" #p "4", "v" #p "5", "v" #p "6", "v" #p "7", "v" #p "8", "v" #p "9"
#define V100_163 V10(10), V10(11), V10(12), V10(13), V10(14), V10(15), "v160", "v161", "v162", "v163"

NOPK __global__ void __launch_bounds__(512, 2)
fused_mha_s4d(const char* __restrict__ x, const char* __restrict__ wqkv, const char* __restrict__ wo, const char* __restrict__ u,
              const char* __restrict__ logAr, const char* __restrict__ Aim, const char* __restrict__ Cre, const char* __restrict__ Cim,
              const char* __restrict__ logdt, const char* __restrict__ Dsk, char* __restrict__ out, char* __restrict__ ws) {
  extern __shared__ __attribute__((aligned(16))) char smem[];
  const int tid = __builtin_amdgcn_workitem_id_x(), lane = tid & 63;
  const int wid = __builtin_amdgcn_readfirstlane(tid >> 6);
  const int b = __builtin_amdgcn_workgroup_id_x(), xcd = b & 7, idx = b >> 3;
  const int r32 = lane & 31, hh = lane >> 5;
  const unsigned L0 = (unsigned)(uintptr_t)smem;
  (void)L0;
  const __amdgpu_buffer_rsrc_t wsr = __builtin_amdgcn_make_buffer_rsrc(ws, 0, WS_END, 0x00020000);
  asm volatile("" :: "s"(x), "s"(wqkv), "s"(wo), "s"(u), "s"(logAr), "s"(Aim), "s"(Cre), "s"(Cim), "s"(logdt), "s"(Dsk), "s"(out), "s"(ws));
  STAMP(0);
  asm volatile(".Lpf_begin:");
#define MAKE_TAG(e) (((u64)(unsigned)__builtin_amdgcn_readfirstlane((unsigned)((e) >> 32)) << 32 | (unsigned)__builtin_amdgcn_readfirstlane((unsigned)(e))) + 0x9E3779B97F4A7C15ull)
  const int rt = (xcd & 3) * 4 + (idx >> 3), g = (xcd >> 2) * 8 + (idx & 7);
  const int woff = (g == 15) ? 8 : 0;
  const bool has_b = xcd < 7;

  if (__builtin_expect(wid >= 4, 0)) {
    const int w = wid - 4;
    __builtin_amdgcn_s_setprio(2);
    if (w == 0) lds_st<u32x4>(L_MAX + lane * 16, u32x4{0u, 0u, 0u, 0u});
    const u64 epoch = __hip_atomic_load((GAS u64*)(ws + WS_EPOCH), __ATOMIC_RELAXED, __HIP_MEMORY_SCOPE_AGENT);
    {
      u64 pc; unsigned below, above;
      asm volatile("s_getpc_b64 %0\n.Lpf_here:\n\ts_mov_b32 %1, .Lpf_here-.Lpf_begin\n\ts_mov_b32 %2, .Lpf_end-.Lpf_here" : "=s"(pc), "=s"(below), "=s"(above));
      const unsigned off = ((w * 64 + lane) * 8 + (idx & 7)) * 128;
      if (off < below + above) asm volatile("s_mov_b32 m0, %2\n\ts_nop 0\n\tglobal_load_lds_dword %0, %1" :: "v"(off), "s"(pc - below), "s"(L_MAX + 512) : "memory");
    }
    asm volatile("s_waitcnt lgkmcnt(0)\n\ts_barrier" ::: "memory");
    f32x16 acc[3];
#pragma unroll
    for (int j = 0; j < 3; ++j) acc[j] = f32x16{};
    {
      const unsigned xa_l = w * 4096 + r32 * 128, xsw = (r32 >> 1) & 7;
      unsigned xa0 = xa_l + (((0 + hh) ^ xsw) << 4), xa1 = xa_l + (((2 + hh) ^ xsw) << 4), xa2 = xa0 ^ 64, xa3 = xa1 ^ 64;
      unsigned wa = XCH_B + (8 * hh + ((lane & 15) >> 2)) * 192 + woff + ((lane >> 4) & 1) * 32 + (lane & 3) * 8;
      unsigned tv, ts, tn, tg, car = C_R, cla = C_L;
      QKV_LOOP();
    }
    STAMP(2);
    const int rt32 = rt * 4 + w;
    const u64 tag = MAKE_TAG(epoch);
    unsigned l16 = lane * 16; PIN(l16);
    lds_wait_ge(C_BTAG, 4);
#pragma unroll
    for (int j = 0; j < 3; ++j) {
      const int sl = 3 * g + j, ty = sl >> 4;
      const unsigned so = ty == 0 ? WS_QKV + (sl * 64 + rt32) * 2048 : WS_KV + ((sl & 15) * 64 + rt32) * 4096 + (ty - 1) * 2048;
      if (ty == 0) {
#pragma unroll
        for (int i = 0; i < 16; ++i) acc[j][i] *= QSCALE;
      }
      u32x4 f0, f1;
#pragma unroll
      for (int i = 0; i < 4; ++i) { f0[i] = pk_bf16(acc[j][2 * i], acc[j][2 * i + 1]); f1[i] = pk_bf16(acc[j][8 + 2 * i], acc[j][9 + 2 * i]); }
      const bool vpad = ty == 2 && r32 >= 28, kpad = ty < 2 && hh;
      const unsigned cst = r32 == 28 ? 0x3F803F80u : 0u;
#pragma unroll
      for (int i = 0; i < 4; ++i) f0[i] = vpad ? cst : f0[i];
      f1[0] = vpad ? cst : f1[0]; f1[1] = vpad ? cst : f1[1];
      f1[2] = vpad ? cst : (kpad ? 0x3F803F80u : f1[2]); f1[3] = vpad ? cst : (kpad ? 0x00003F80u : f1[3]);
      if (j < 2) { st128(f0, wsr, l16, so); st128(f1, wsr, l16 + 1024, so); }
      if (ty < 2) {
        float n2 = 0.f, n3 = 0.f, n4 = 0.f;
#pragma unroll
        for (int i = 0; i < 6; ++i) { n2 = __builtin_fmaf(acc[j][i], acc[j][i], n2); n4 = __builtin_fmaf(acc[j][6 + i], acc[j][6 + i], n4); }
#pragma unroll
        for (int i = 12; i < 16; ++i) n3 = __builtin_fmaf(acc[j][i], acc[j][i], n3);
        unsigned nrm = f2u(__builtin_amdgcn_sqrtf(half_swap_add(n2 + n4 + (hh ? 0.f : n3))) * 1.01f);
        unsigned off = WS_QN + (sl * 64 + rt32) * 128 + lane * 4;
        if (ty == 1) { nrm = wave_umax(nrm); off = WS_KM + ((sl - 16) * 64 + rt32) * 4; }
        if (lane < (ty == 1 ? 1 : 32)) __builtin_amdgcn_raw_buffer_store_b32(nrm, wsr, off, 0, AUX_SC1);
      }
      if (j == 2) { st128(f0, wsr, l16, so); st128(f1, wsr, l16 + 1024, so); }
    }
    STAMP(14);
    drain_vm();
    STAMP(15);
    if (last_of_four(C_APUB, lane, 0)) { if (lane == 0) flag_store(ws, WS_QFLAG + (g * 16 + rt) * 128, tag); }
    __builtin_amdgcn_s_setprio(0);
    STAMP(3);
    const int h = xcd * 2 + (idx >> 4), qt = idx & 15;
    s16x8 qf[4][2], kf[4][2], vf[4][2];
    const unsigned kbase = WS_KV + (h * 64 + w * 16) * 4096;
#define LDR(rs_, soff, voff) __builtin_bit_cast(s16x8, __builtin_amdgcn_raw_buffer_load_b128(rs_, l16 + (voff), (soff), AUX_SC1))
#define LDF(soff, voff) LDR(wsr, soff, voff)
    flag_wait(ws, WS_QFLAG + ((h / 3) * 16 + qt) * 128, tag);
    unsigned qn[4];
#pragma unroll
    for (int i = 0; i < 4; ++i) {
      qn[i] = __builtin_amdgcn_raw_buffer_load_b32(wsr, r32 * 4, WS_QN + (h * 64 + qt * 4 + ((w + 1 + i) & 3)) * 128, AUX_SC1);
      const unsigned so = WS_QKV + (h * 64 + qt * 4 + ((w + 1 + i) & 3)) * 2048;
      qf[i][1] = LDF(so, 1024); qf[i][0] = LDF(so, 0);
    }
    STAMP(1);
    if (w == 0) {
      flag_wait(ws, WS_QFLAG + ((hh ? (32 + h) / 3 : (16 + h) / 3) * 16 + (lane & 15)) * 128, tag);
      lds_arrive_nw(C_GO);
    } else {
      lds_wait_ge(C_GO, 1);
    }
    STAMP(4);
    const unsigned kmv = __builtin_amdgcn_raw_buffer_load_b32(wsr, lane * 4, WS_KM + h * 256, AUX_SC1);
#define LDKV(rs_, B, soff) do { SBAR(); kf[B][0] = LDR(rs_, soff, 0); kf[B][1] = LDR(rs_, soff, 1024); vf[B][0] = LDR(rs_, soff, 2048); vf[B][1] = LDR(rs_, soff, 3072); SBAR(); } while (0)
    LDKV(wsr, 0, kbase); LDKV(wsr, 1, kbase + 4096); LDKV(wsr, 2, kbase + 8192);
    {
      const float kmax = u2f(wave_umax(kmv));
      float m[4]; bool unsafe = false;
#pragma unroll
      for (int i = 0; i < 4; ++i) {
        m[i] = u2f(qn[i]) * kmax;
        unsafe |= !(m[i] <= SAFE_BOUND);
      }
      if (__builtin_expect(__builtin_amdgcn_ballot_w64(unsafe) != 0ull, 0)) {
        float mx[4];
#pragma unroll
        for (int i = 0; i < 4; ++i) { mx[i] = -INFINITY; if (hh) { u32x4 t_ = __builtin_bit_cast(u32x4, qf[i][1]); t_[2] = 0u; t_[3] = 0u; qf[i][1] = __builtin_bit_cast(s16x8, t_); } }
#pragma unroll 1
        for (int t = 0; t < 16; ++t) {
          const s16x8 k0 = LDF(kbase + t * 4096, 0), k1 = LDF(kbase + t * 4096, 1024);
#pragma unroll
          for (int i = 0; i < 4; ++i) {
            f32x16 s = MFMA32(k0, qf[i][0], f32x16{}); s = MFMA32(k1, qf[i][1], s);
#pragma unroll
            for (int e = 0; e < 16; ++e) mx[i] = __builtin_fmaxf(mx[i], s[e]);
          }
        }
#pragma unroll
        for (int i = 0; i < 4; ++i) { const float v = half_swap_max(mx[i]);
          const unsigned k_ = f2u(v) ^ ((unsigned)((int)f2u(v) >> 31) | 0x80000000u);
          __hip_atomic_fetch_max((LAS unsigned*)(uintptr_t)(L_MAX + (((w + 1 + i) & 3) * 32 + r32) * 4), k_, __ATOMIC_RELAXED, __HIP_MEMORY_SCOPE_WORKGROUP); }
        lds_arrive(C_GO); lds_wait_ge(C_GO, 5);
#pragma unroll
        for (int i = 0; i < 4; ++i) {
          const unsigned k_ = lds_ld<unsigned>(L_MAX + (((w + 1 + i) & 3) * 32 + r32) * 4);
          m[i] = u2f(k_ ^ ((unsigned)((int)~k_ >> 31) | 0x80000000u));
        }
      }
#pragma unroll
      for (int i = 0; i < 4; ++i) {
        const float m0 = bf_round(-m[i]), r1 = -m[i] - m0, m1 = bf_round(r1), m2 = r1 - m1;
        if (hh) { u32x4 t_ = __builtin_bit_cast(u32x4, qf[i][1]); t_[2] = pk_bf16(m0, m1); t_[3] = pk_bf16(m2, 0.f); qf[i][1] = __builtin_bit_cast(s16x8, t_); }
      }
    }
    f32x16 o[4];
#pragma unroll
    for (int i = 0; i < 4; ++i) o[i] = f32x16{};
    f32x16 sA, sB; u32x4 p0, p1 = u32x4{0u, 0u, 0u, 0u};
    sA = MFMA32(kf[0][0], qf[0][0], f32x16{}); sA = MFMA32(kf[0][1], qf[0][1], sA);
    STAMP(5);
    unsigned redw = L_B + l16; PIN(redw);
#define EX(S, i) S[i] = __builtin_amdgcn_exp2f(S[i])
#define PK(S, i) pk_bf16(S[i], S[(i) + 1])
#define BF(P) __builtin_bit_cast(s16x8, P)
#define STEP(C, N, KN, QN, VP, OP, VC, OC) do { \
      N = MFMA32(KN[0], QN[0], f32x16{}); SBAR(); EX(C, 0); EX(C, 1); EX(C, 2); EX(C, 3); EX(C, 4); SBAR(); \
      OP = MFMA32(VP[1], BF(p1), OP); SBAR(); EX(C, 5); EX(C, 6); EX(C, 7); p0[0] = PK(C, 0); p0[1] = PK(C, 2); p0[2] = PK(C, 4); p0[3] = PK(C, 6); PIN(p0); SBAR(); \
      N = MFMA32(KN[1], QN[1], N); SBAR(); EX(C, 8); EX(C, 9); EX(C, 10); EX(C, 11); EX(C, 12); SBAR(); \
      OC = MFMA32(VC[0], BF(p0), OC); SBAR(); EX(C, 13); EX(C, 14); EX(C, 15); p1[0] = PK(C, 8); p1[1] = PK(C, 10); p1[2] = PK(C, 12); p1[3] = PK(C, 14); PIN(p1); SBAR(); \
    } while (0)
#define RED_WRITE(i) do { const unsigned a_ = redw + (((w + 1 + (i)) & 3) * 3 + (2 - (i))) * 4096; \
      _Pragma("unroll") for (int q_ = 0; q_ < 4; ++q_) lds_st<f32x4>(a_ + q_ * 1024, f32x4{o[i][4 * q_], o[i][4 * q_ + 1], o[i][4 * q_ + 2], o[i][4 * q_ + 3]}); } while (0)
#define TILE(T, TP, TN, RS, LAST) do { \
      STEP(sA, sB, kf[T], qf[1], vf[TP], o[3], vf[T], o[0]); \
      if (!LAST) LDKV(RS, TP, kcur + (T + 3) * 4096); \
      STEP(sB, sA, kf[T], qf[2], vf[T], o[0], vf[T], o[1]); \
      if (LAST) RED_WRITE(0); \
      STEP(sA, sB, kf[T], qf[3], vf[T], o[1], vf[T], o[2]); \
      if (LAST) RED_WRITE(1); \
      STEP(sB, sA, kf[TN], qf[0], vf[T], o[2], vf[T], o[3]); \
      if (LAST) RED_WRITE(2); \
    } while (0)
    vf[3][1] = s16x8{};
    unsigned kcur = kbase;
#pragma unroll 1
    for (int tb = 0;; tb += 4) {
      const __amdgpu_buffer_rsrc_t wsr_next = __builtin_amdgcn_make_buffer_rsrc(ws, 0, tb < 12 ? WS_END : 0u, 0x00020000);
      TILE(0, 3, 1, wsr, false);
      TILE(1, 0, 2, wsr_next, false);
      TILE(2, 1, 3, wsr_next, false);
      if (tb == 12) break;
      TILE(3, 2, 0, wsr, false);
      kcur += 16384;
      if (STAMPS) { if (tb == 0) STAMP(11); else if (tb == 4) STAMP(12); else STAMP(13); }
    }
    lds_wait_ge(C_BFREE, 4);
    TILE(3, 2, 0, wsr, true);
    o[3] = MFMA32(vf[3][1], BF(p1), o[3]);
    STAMP(6);
    role_sync(C_A, 1);
    {
      f32x4 pv[12];
#pragma unroll
      for (int k = 0; k < 12; ++k) pv[k] = lds_ld<f32x4>(redw + w * 12288 + k * 1024);
      SBAR();
      f32x16 a = o[3];
#pragma unroll
      for (int k = 0; k < 12; ++k) { const int q = k & 3; a[4 * q] += pv[k][0]; a[4 * q + 1] += pv[k][1]; a[4 * q + 2] += pv[k][2]; a[4 * q + 3] += pv[k][3]; }
      auto lr = __builtin_amdgcn_permlane32_swap(f2u(a[12]), f2u(a[12]), false, false);
      const float inv = __builtin_amdgcn_rcpf(u2f(lr[1]));
      u32x4 f0, f1;
#pragma unroll
      for (int i = 0; i < 4; ++i) { f0[i] = pk_bf16(a[2 * i] * inv, a[2 * i + 1] * inv); f1[i] = pk_bf16(a[8 + 2 * i] * inv, a[9 + 2 * i] * inv); }
      if (hh) { f1[2] = 0u; f1[3] = 0u; }
      const unsigned so = WS_O + ((qt * 4 + w) * 16 + h) * 2048;
      st128(f0, wsr, l16, so);
      st128(f1, wsr, l16 + 1024, so);
      lds_arrive(C_A);
      drain_vm();
      if (lane == 0) flag_store(ws, WS_OFLAG + ((qt * 4 + w) * 16 + h) * 128, tag);
    }
    STAMP(7);
    const int rs = xcd * 8 + (idx >> 2), cg = idx & 3, n0 = w < 3 ? 32 * w : 80;
    lds_wait_ge(C_WO, 4);
    s16x8 wf[16][2];
    {
      unsigned wa = L_RING + (4 * hh + ((lane & 15) >> 2)) * 224 + n0 * 2 + ((lane >> 4) & 1) * 32 + (lane & 3) * 8;
      unsigned wb2 = wa + (hh ? 16 * 224 : 24 * 224), wa1 = wa + 8 * 28 * 224, wb3 = wb2 + 8 * 28 * 224;
      PIN(wa); PIN(wb2); PIN(wa1); PIN(wb3);
#pragma unroll
      for (int hd = 0; hd < 16; ++hd) {
        const unsigned a_ = (hd < 8 ? wa : wa1) + (hd & 7) * 28 * 224, b_ = (hd < 8 ? wb2 : wb3) + (hd & 7) * 28 * 224;
        wf[hd][0] = cat8(tr_read(a_), tr_read(a_ + 8 * 224));
        wf[hd][1] = cat8(tr_read(a_ + 16 * 224), tr_read(b_));
      }
    }
    lds_wait_ge(C_A, 2 * 4);
    flag_wait(ws, WS_OFLAG + (rs * 16 + w * 4 + (lane & 3)) * 128, tag);
    STAMP(8);
    {
      u32x4 of[8];
#pragma unroll
      for (int i = 0; i < 8; ++i) of[i] = __builtin_amdgcn_raw_buffer_load_b128(wsr, l16 + (i & 3) * 1024, WS_O + (rs * 16 + w * 4) * 2048 + (i >> 2) * 4096, AUX_SC1);
#pragma unroll
      for (int i = 0; i < 8; ++i) lds_st<u32x4>(redw + w * 8192 + i * 1024, of[i]);
    }
    role_sync(C_A, 3);
    STAMP(9);
    f32x16 d = f32x16{};
    {
      s16x8 ob[8];
#pragma unroll
      for (int k = 0; k < 8; ++k) ob[k] = lds_ld<s16x8>(redw + k * 1024);
      SBAR();
#pragma unroll
      for (int k = 0; k < 32; ++k) {
        d = MFMA32(wf[k >> 1][k & 1], ob[k & 7], d);
        if (k + 8 < 32) ob[k & 7] = lds_ld<s16x8>(redw + (k + 8) * 1024);
        SBAR();
      }
    }
    {
      char* op = out + (size_t)(rs * 32 * 1792 + (cg * 112 + n0) * 2) + (unsigned)(r32 * 1792 + hh * 16);
#pragma unroll
      for (int k = 0; k < 2; ++k) {
        const unsigned a0 = pk_bf16(d[8 * k], d[8 * k + 1]), a1 = pk_bf16(d[8 * k + 2], d[8 * k + 3]);
        const unsigned b0 = pk_bf16(d[8 * k + 4], d[8 * k + 5]), b1 = pk_bf16(d[8 * k + 6], d[8 * k + 7]);
        auto s0 = __builtin_amdgcn_permlane32_swap(a0, b0, false, false);
        auto s1 = __builtin_amdgcn_permlane32_swap(a1, b1, false, false);
        *(GAS u32x4*)(op + k * 32) = u32x4{s0[0], s1[0], s0[1], s1[1]};
      }
    }
    STAMP(10);
    STAMP_FLUSH();
    asm volatile(".Lpf_end:");
  } else {
    const int wb = wid;
    const int c = wb & 1, half = wb >> 1;
    const int d = (has_b ? xcd * 64 : 0) + idx * 2 + c;
    unsigned xo[4], wo_[3];
#pragma unroll
    for (int q = 0; q < 4; ++q) xo[q] = (q * 8 + (lane >> 3)) * 896 + (((lane & 7) ^ ((q & 1) * 4 + (lane >> 4))) << 4);
    const char* xb = x + (size_t)(rt * 128 + wb * 32) * 896;
    const unsigned xdst = L_RING + wb * 4096, wdst = L_RING + XCH_B + wb * 3072;
    glds<0>(xo[0], xb, xdst); glds<1024>(xo[1], xb, xdst); glds<2048>(xo[2], xb, xdst); glds<3072>(xo[3], xb, xdst);
#pragma unroll
    for (int i = 0; i < 3; ++i) {
      const unsigned p = (3 * wb + i) * 64 + lane, row = (p * 43691u) >> 19; unsigned e = p - row * 12; e = e > 10 ? 10 : e;
      wo_[i] = row * 2688 + e * 16;
    }
    const char* wq = wqkv + g * 168 - woff;
    STAMP(1);
    glds<0>(wo_[0], wq, wdst); glds<1024>(wo_[1], wq, wdst); glds<2048>(wo_[2], wq, wdst);
    STAMP(13);
    unsigned pl, pa, pr, pi, pdt; u64 epoch; u32x4 ut0, ut1;
    {
      const unsigned po = d * 512 + half * 256 + lane * 4, zero = 0, uo = (lane >> 3) * 896 + (lane & 7) * 16, uo1 = uo + 8 * 896;
      asm volatile("global_load_dwordx2 %0, %8, %12 sc1\n\tglobal_load_ushort %1, %8, %13\n\tglobal_load_dword %2, %9, %14\n\tglobal_load_dword %3, %9, %15\n\t"
                   "global_load_dword %4, %9, %16\n\tglobal_load_dword %5, %9, %17\n\tglobal_load_dwordx4 %6, %10, %18\n\tglobal_load_dwordx4 %7, %11, %18"
                   : "=&v"(epoch), "=&v"(pdt), "=&v"(pl), "=&v"(pa), "=&v"(pr), "=&v"(pi), "=&v"(ut0), "=&v"(ut1)
                   : "v"(zero), "v"(po), "v"(uo), "v"(uo1), "s"(ws + WS_EPOCH), "s"(logdt + d * 2), "s"(logAr), "s"(Aim), "s"(Cre), "s"(Cim),
                     "s"(u + (size_t)(idx * 64 + wb * 16) * 896 + (has_b ? xcd * 128 : 0)) : "memory");
    }
    STAMP(14);
    asm volatile("s_barrier" ::: "memory");
    __builtin_amdgcn_s_setprio(1);
    qkv_issue(xo, wo_, xb + 128, wq + 64 * 2688, xdst + SLOT_B, wdst + SLOT_B);
    asm volatile("s_waitcnt vmcnt(15)" ::: "memory"); lds_arrive_nw(C_L);
    STAMP(15);
    qkv_issue(xo, wo_, xb + 256, wq + 128 * 2688, xdst + 2 * SLOT_B, wdst + 2 * SLOT_B);
    asm volatile("s_waitcnt vmcnt(7)" : "+v"(epoch), "+v"(pdt), "+v"(pl), "+v"(pa), "+v"(pr), "+v"(pi), "+v"(ut0), "+v"(ut1) :: "memory"); lds_arrive_nw(C_L + 4);
    STAMP(9);
    const u64 tag = MAKE_TAG(epoch);
    asm volatile("" :: "s"(tag));
    lds_arrive_nw(C_BTAG);
    auto piece = [&](auto kc) __attribute__((always_inline)) NOPK {
      constexpr int k = decltype(kc)::value, n = 3 + k / 7, i = k % 7;
      if constexpr (k < 28) {
        if constexpr (i == 0 && n >= 4) {
          lds_wait_ge(C_R + 4 * (n - 3), 4);
        }
        if constexpr (i < 4) glds<(n & 3) * SLOT_B + i * 1024>(xo[i], xb + n * 128, xdst);
        else glds<(n & 3) * SLOT_B + (i - 4) * 1024>(wo_[i - 4], wq + n * 64 * 2688, wdst);
        if constexpr (i == 6) { asm volatile("s_waitcnt vmcnt(7)" ::: "memory"); lds_arrive_nw(C_L + 4 * (n - 1)); }

      }
    };
#define PIECE(k) do { SBAR(); piece(IC<(k)>{}); SBAR(); } while (0)
    PIECE(0);
    const float dt = bf_round(fexp(bf_lo(pdt)));
    const unsigned cm = umax(umax(pr << 16 & 0x7fff0000u, pr & 0x7fff0000u), umax(pi << 16 & 0x7fff0000u, pi & 0x7fff0000u));
    unsigned eb = wave_umax(cm) >> 23; eb = eb < 1u ? 1u : (eb > 253u ? 253u : eb);
    const float csc = u2f((254u - eb) << 23), cinv = u2f(eb << 23);
    unsigned lo[2][32], hi[2][21];
    auto image_u = [&]() __attribute__((always_inline)) NOPK
    {
      const int oct = lane & 7;
#pragma unroll
      for (int i = 0; i < 2; ++i) {
        const int tp = 63 - (wb * 16 + i * 8 + (lane >> 3));
        const unsigned a = L_UIMG + oct * 1024 + (((tp >> 3) ^ oct) << 4) + (tp & 7) * 2;
        const s16x8 v = __builtin_bit_cast(s16x8, i ? ut1 : ut0);
#pragma unroll
        for (int e = 0; e < 8; ++e) lds_st<short>(a + e * 128, v[e]);
      }
      lds_arrive(C_U);
    };
    auto publish_u = [&]() __attribute__((always_inline)) NOPK {
      lds_wait_ge(C_U, 4);
#pragma unroll
      for (int i = 0; i < 2; ++i) {
        const int pair = wb * 8 + i * 4 + (lane >> 4), ch = 2 * pair + (lane & 1);
        const u32x4 v = lds_ld<u32x4>(L_UIMG + ch * 128 + ((((lane >> 1) & 7) ^ (ch >> 3)) << 4));
        st128(v, wsr, ((xcd * 32 + pair) * 32 + (31 - idx)) * 256 + (lane & 15) * 16, WS_UT);
      }
      lds_arrive(C_U);
    };
    auto gen = [&](auto rc) __attribute__((always_inline)) NOPK {
      constexpr int r = decltype(rc)::value, k0 = r ? 28 : 1;
#define LANDED() do { if (r) { SBAR(); drain_vm(); lds_arrive_nw(C_L + 24); image_u(); SBAR(); } } while (0)
      const float lar = r ? bf_hi(pl) : bf_lo(pl), aim = r ? bf_hi(pa) : bf_lo(pa);
      float rho = -bf_round(fexp(lar)) * dt * LOG2E, th = aim * dt * 0.15915494309189535f;
      PIN(rho); PIN(th); PIECE(k0);
      f32x2 hA; hA.x = (r ? bf_hi(pr) : bf_lo(pr)) * csc; hA.y = (r ? bf_hi(pi) : bf_lo(pi)) * -csc;
      f32x2 z8 = zpow(rho, th, 8.f); PIN(z8); PIECE(k0 + 1);
      f32x2 z16 = zpow(rho, th, 16.f); PIN(z16); PIECE(k0 + 2);
      f32x2 z1 = zpow(rho, -th, 1.f); PIN(z1); PIECE(k0 + 3);
      f32x2 z256 = zpow(rho, -th, 256.f); PIN(z256); PIECE(k0 + 4);
#define PUTH(i, v) do { hi[r][i] = pk_f16((v).x, (v).y); PIN(hi[r][i]); } while (0)
#define PUTL(j, v) do { lo[r][j] = pk_f16((v).x, (v).y); PIN(lo[r][j]); } while (0)
#define LO2(j) do { e1 = cmul(e1, z16); PUTL(j, e0); PUTL((j) + 1, e1); if ((j) < 30) e0 = cmul(e0, z16); PIECE(k0 + 4 + (j) / 2); } while (0)
      f32x2 e0 = z16, e1 = z8, hB;
      lo[r][0] = 0x00003C00u; PUTL(1, e1);
      LO2(2); LO2(4); LO2(6); LO2(8); LO2(10); LO2(12); LANDED(); LO2(14); LO2(16); LO2(18); LO2(20); LO2(22); LO2(24); LO2(26); LO2(28); LO2(30);
      if (r && has_b) { SBAR(); publish_u(); SBAR(); }
      PUTH(0, hA); f32x2 g0 = cmul(hA, z256); PUTH(8, g0);
      hB = cmul(hA, z1); PUTH(1, hB); PIECE(k0 + 20); hA = cmul(hB, z1); PUTH(2, hA); { f32x2 t_ = cmul(hA, z256); PUTH(9, t_); } PIECE(k0 + 21);
      hB = cmul(hA, z1); PUTH(3, hB); hA = cmul(hB, z1); PUTH(4, hA); PIECE(k0 + 22); f32x2 g4 = cmul(hA, z256); PUTH(10, g4);
      hB = cmul(hA, z1); PUTH(5, hB); PIECE(k0 + 23); hA = cmul(hB, z1); PUTH(6, hA); { f32x2 t_ = cmul(hA, z256); PUTH(11, t_); } PIECE(k0 + 24);
      hB = cmul(hA, z1); PUTH(7, hB);
      g0 = cmul(g0, z256); PUTH(12, g0); PIECE(k0 + 25); g4 = cmul(g4, z256); PUTH(13, g4);
      g0 = cmul(g0, z256); PUTH(14, g0); PIECE(k0 + 26); g4 = cmul(g4, z256); PUTH(15, g4);
#pragma unroll
      for (int i = 16; i < 21; ++i) { g0 = cmul(g0, z256); PUTH(i, g0); }
    };
    gen(IC<0>{});
    STAMP(10);
    gen(IC<1>{});
    const unsigned T = L_T + c * T_B;
    unsigned ga[2][11];
#pragma unroll
    for (int it = 0; it < 2; ++it)
#pragma unroll
      for (int k = 0; k < 11; ++k) {
        const float f = (float)(8 * (3 + it * 128 + half * 64 + lane) - 27 + k);
        const float fc = it ? f : __builtin_fmaxf(__builtin_fmaxf(f, __builtin_fmaf(f, 0.25f, 0.75f)), 0.5f);
        ga[it][k] = (pk_bf16(fc, fc) & 0xffffu) * 2u + (T - 2u * (126u << 7));
        PIN(ga[it][k]);
      }
    STAMP(11);
    if (has_b) {
      const unsigned tb_ = L_B + wb * TAB_W;
      const unsigned lw = tb_ + (lane >> 3) * 1056 + ((lane >> 2) & 1) * 528 + (lane & 3) * 4;
      const unsigned hw = tb_ + TAB_HI + (lane >> 3) * 672 + ((lane >> 2) & 1) * 336 + (lane & 3) * 4;
      const unsigned lr = tb_ + hh * 528 + r32 * 16, hr = tb_ + TAB_HI + hh * 336 + r32 * 16;
      lds_wait_ge(C_R + 16, 4);
      lds_wait_ge(C_U, 8);
      STAMP(12);
      f32x16 kd = f32x16{};
#pragma unroll
      for (int r = 0; r < 2; ++r) {
#pragma unroll
        for (int j = 0; j < 32; ++j) lds_st<unsigned>(lw + j * 16, lo[r][j]);
#pragma unroll
        for (int i = 0; i < 21; ++i) lds_st<unsigned>(hw + i * 16, hi[r][i]);
        h16x8 fa[8], fb[8];
#pragma unroll
        for (int s = 0; s < 8; ++s) { fa[s] = lds_ld<h16x8>(hr + s * 672); fb[s] = lds_ld<h16x8>(lr + s * 1056); }
        SBAR();
        if (r == 0) {
          drain_vm();
          if (last_of_four(C_BPUB, lane, 0)) { if (lane == 0) flag_store(ws, WS_UFLAG + (xcd * 32 + idx) * 128, tag); }
        }
#pragma unroll
        for (int s = 0; s < 8; ++s) kd = MFMA32H(fa[s], fb[s], kd);
      }
      {
        const float ks = cinv * 2.f * dt;
        if (half) {
#pragma unroll
          for (int q = 0; q < 3; ++q) lds_st<f32x4>(tb_ + q * 1024 + lane * 16, f32x4{kd[4 * q] * ks, kd[4 * q + 1] * ks, kd[4 * q + 2] * ks, kd[4 * q + 3] * ks});
        }
        role_sync(C_B, 1);
        if (!half) {
          const unsigned T128 = T + (1024 - 128) * 2;
          const unsigned i1 = T128 + (hh ? 384 + 2 * r32 : 256 + 4 * r32) * 2;
          const unsigned i2 = T128 + (hh ? (r32 == 0 ? 640 : 643) : 512 + r32) * 2, i3 = T128 + (hh ? 643 : 512 + r32) * 2, st3 = hh ? 0 : 64;
          const f32x4 v0 = lds_ld<f32x4>(tb_ + 2 * TAB_W + lane * 16), v1 = lds_ld<f32x4>(tb_ + 2 * TAB_W + 1024 + lane * 16), v2 = lds_ld<f32x4>(tb_ + 2 * TAB_W + 2048 + lane * 16);
#pragma unroll
          for (int e = 0; e < 4; ++e) {
            const float f = __builtin_fmaxf((float)(8 * r32 + 4 * hh + e), 0.75f);
            lds_st<short>((pk_bf16(f, f) & 0xffffu) * 2u + (T - 2u * (126u << 7)), (short)pk_bf16(__builtin_fmaf(kd[e], ks, v0[e]), 0.f));
          }
          lds_st<unsigned>(i1, pk_bf16(__builtin_fmaf(kd[4], ks, v1[0]), __builtin_fmaf(kd[5], ks, v1[1])));
          lds_st<unsigned>(i1 + (hh ? 128 : 4), pk_bf16(__builtin_fmaf(kd[6], ks, v1[2]), __builtin_fmaf(kd[7], ks, v1[3])));
          const unsigned w0 = pk_bf16(__builtin_fmaf(kd[8], ks, v2[0]), __builtin_fmaf(kd[9], ks, v2[1])), w1 = pk_bf16(__builtin_fmaf(kd[10], ks, v2[2]), __builtin_fmaf(kd[11], ks, v2[3]));
          lds_st<short>(i2, (short)w0); lds_st<short>(i3 + st3, (short)(w0 >> 16)); lds_st<short>(i3 + 2 * st3, (short)w1); lds_st<short>(i3 + 3 * st3, (short)(w1 >> 16));
          if (lane == 0) lds_st<short>(T, 0);
        }
        role_sync(C_B, 2);
      }
      STAMP(2);
      const unsigned cb = L_B + c * CH_B;
      {
        s16x2 P[2][6];
#pragma unroll
        for (int it = 0; it < 2; ++it)
#pragma unroll
          for (int k = 0; k < 11; ++k) P[it][k >> 1][k & 1] = lds_ld<short>(ga[it][k]);
        const unsigned ub = cb + 4 * KC_S + half * UC_S;
        const u32x4 z4 = u32x4{0u, 0u, 0u, 0u};
        if (lane < 28) lds_st<u32x4>(ub + 4096 + lane * 16, z4);
        else if (lane < 34) lds_st<u32x4>(cb + (half * 2 + (lane - 28) / 3) * KC_S + ((lane - 28) % 3) * 16, z4);
        else if (lane == 34 && half) lds_st<u32x2>(ub + 4088, u32x2{0u, 0u});
#pragma unroll
        for (int it = 0; it < 2; ++it) {
          unsigned Q[6], A[5];
#pragma unroll
          for (int k = 0; k < 6; ++k) Q[k] = __builtin_bit_cast(unsigned, P[it][k]);
#pragma unroll
          for (int k = 0; k < 5; ++k) A[k] = __builtin_amdgcn_alignbit(Q[k + 1], Q[k], 16);
          const unsigned a = cb + (3 + it * 128 + half * 64 + lane) * 16;
          lds_st<u32x4>(a, u32x4{Q[0], Q[1], Q[2], Q[3]}); lds_st<u32x4>(a + KC_S, u32x4{A[0], A[1], A[2], A[3]});
          lds_st<u32x4>(a + 2 * KC_S, u32x4{Q[1], Q[2], Q[3], Q[4]}); lds_st<u32x4>(a + 3 * KC_S, u32x4{A[1], A[2], A[3], A[4]});
        }
      }
      STAMP(3);
      if (wb == 0) flag_wait(ws, WS_UFLAG + (xcd * 32 + r32) * 128, tag);
      role_sync_slow(C_B, 3);
      STAMP(4);
      {
        u32x4 v[2];
#pragma unroll
        for (int i = 0; i < 2; ++i) v[i] = __builtin_amdgcn_raw_buffer_load_b128(wsr, wb * 2048 + i * 1024 + lane * 16, WS_UT + (xcd * 32 + idx) * 8192, AUX_SC1);
#pragma unroll
        for (int i = 0; i < 2; ++i) {
          const int p = wb * 128 + i * 64 + lane;
          const unsigned a = L_B + (p & 1) * CH_B + 4 * KC_S + (p >> 1) * 16;
          lds_st<u32x4>(a, v[i]); lds_st<u32x2>(a + UC_S - 8, u32x2{v[i][0], v[i][1]}); lds_st<u32x2>(a + UC_S, u32x2{v[i][2], v[i][3]});
        }
      }
      role_sync(C_B, 4);
      STAMP(5);
      f32x4 y[8];
#pragma unroll
      for (int i = 0; i < 8; ++i) y[i] = f32x4{0.f, 0.f, 0.f, 0.f};
      {
        const int r16 = lane & 15, kq = lane >> 4;
        const unsigned ka = cb + (r16 & 3) * KC_S + ((r16 >> 2) + kq) * 16 + half * 256;
        const unsigned ua = cb + 4 * KC_S + (1 - (r16 & 1)) * UC_S + (2016 - 32 * (r16 >> 1) + 8 * kq) * 2 - 7 * 512 + half * 256;
        s16x8 af[2][8], bf[2][8];
#define CONV_LD(S, O) _Pragma("unroll") for (int m = 0; m < 8; ++m) { bf[S][7 - m] = lds_ld<s16x8>(ua + (O) + m * 512); af[S][m] = lds_ld<s16x8>(ka + (O) + m * 512); }
#define CONV_MM(S) _Pragma("unroll") for (int n = 7; n >= 0; --n) _Pragma("unroll") for (int m = 0; m + n < 8; ++m) y[m + n] = MFMA16(af[S][m], bf[S][n], y[m + n]);
        CONV_LD(0, 0);
        unsigned o_ = 64;
#pragma unroll 1
        for (int it = 0; it < 2; ++it) {
          SBAR(); CONV_LD(1, o_); SBAR(); CONV_MM(0); SBAR();
          CONV_LD(0, o_ + 64); SBAR(); CONV_MM(1);
          o_ += 128;
        }
      }
      role_sync(C_B, 5);
#pragma unroll
      for (int i = 0; i < 4; ++i) lds_st<f32x4>(L_B + wb * 4096 + i * 1024 + lane * 16, half ? y[i] : y[4 + i]);
      role_sync(C_B, 6);
      {
        const int cc = lane & 15, kq = lane >> 4;
#pragma unroll
        for (int i = 0; i < 4; ++i) {
          const f32x4 v = lds_ld<f32x4>(L_B + (wb ^ 2) * 4096 + i * 1024 + lane * 16) + (half ? y[4 + i] : y[i]);
          const int t64 = 4 * (half * 4 + i) + (cc >> 2);
          __builtin_amdgcn_raw_buffer_store_b64(u32x2{pk_bf16(v[0], v[1]), pk_bf16(v[2], v[3])}, wsr,
                                                ((xcd * 32 + t64) * 64 + idx * 2 + c) * 128 + (32 * ((cc >> 1) & 1) + 8 * kq + 4 * (cc & 1)) * 2, WS_YT, AUX_SC1);
        }
      }
      lds_arrive(C_BFREE);
      drain_vm();
      if (last_of_four(C_BPUB, lane, 1)) { if (lane == 0) flag_store(ws, WS_YFLAG + (xcd * 32 + idx) * 128, tag); }
      STAMP(6);
    } else {
      lds_arrive(C_BFREE);
    }
    {
      unsigned wv[7];
#pragma unroll
      for (int i = 0; i < 7; ++i) { const unsigned p = i * 64 + lane, row = (p * 37450u) >> 19; wv[i] = row * 896 + (p - row * 14) * 16; }
      lds_wait_ge_slow(C_R + 24, 4);
#pragma unroll 1
      for (int i = wb; i < 14; i += 4) {
        const char* sb = wo + i * 32 * 896 + (idx & 3) * 224;
        const unsigned dst = L_RING + i * 7168;
#define WO_PIECE(k) glds<(k) * 1024>(wv[k], sb, dst); asm volatile("s_waitcnt vmcnt(2)" ::: "memory")
        WO_PIECE(0); WO_PIECE(1); WO_PIECE(2); WO_PIECE(3); WO_PIECE(4); WO_PIECE(5); WO_PIECE(6);
      }
      drain_vm();
      lds_arrive(C_WO);
    }
    STAMP(7);
    if (has_b) {
      const int tl = wb * 16 + (lane & 15);
      u32x4 uu[2], ds[2];
#pragma unroll
      for (int i = 0; i < 2; ++i) {
        const int oct = (lane >> 4) + 4 * i;
        uu[i] = *(const GAS u32x4*)(u + (size_t)(idx * 64 * 896 + xcd * 128) + (unsigned)(tl * 896 + oct * 16));
        ds[i] = *(const GAS u32x4*)(Dsk + xcd * 128 + (unsigned)(oct * 16));
      }
      if (wb == 0) flag_wait(ws, WS_YFLAG + (xcd * 32 + r32) * 128, tag);
      role_sync_slow(C_B, 7);
      u32x4 v[2];
#pragma unroll
      for (int i = 0; i < 2; ++i) v[i] = __builtin_amdgcn_raw_buffer_load_b128(wsr, wb * 2048 + i * 1024 + lane * 16, WS_YT + (xcd * 32 + idx) * 8192, AUX_SC1);
#pragma unroll
      for (int i = 0; i < 2; ++i) lds_st<u32x4>(L_YIMG + wb * 2048 + i * 1024 + lane * 16, v[i]);
      role_sync_slow(C_B, 8);
#pragma unroll
      for (int i = 0; i < 2; ++i) {
        const int oct = (lane >> 4) + 4 * i;
        const unsigned a = L_YIMG + (8 * oct + ((lane & 15) >> 2)) * 128 + (wb * 16 + (lane & 3) * 4) * 2;
        const u32x4 yy = __builtin_bit_cast(u32x4, cat8(tr_read(a), tr_read(a + 512)));
        u32x4 r;
#pragma unroll
        for (int k = 0; k < 4; ++k) {
          const unsigned sk = pk_bf16(bf_lo(uu[i][k]) * bf_lo(ds[i][k]), bf_hi(uu[i][k]) * bf_hi(ds[i][k]));
          r[k] = pk_bf16(bf_lo(yy[k]) + bf_lo(sk), bf_hi(yy[k]) + bf_hi(sk));
        }
        *(GAS u32x4*)(out + (size_t)(idx * 64 * 1792 + 896 + xcd * 128) + (unsigned)(tl * 1792 + oct * 16)) = r;
      }
      STAMP(8);
    } else if (b == 7 && wb == 0) {
#pragma unroll 1
      for (int i = 0; i < 4; ++i) flag_wait(ws, WS_QFLAG + (i * 64 + lane) * 128, tag);
      if (lane == 0) flag_store(ws, WS_EPOCH, tag);
    }
    STAMP_FLUSH();
  }
}

extern "C" void kernel_launch(void* const* d_in, const int* in_sizes, int n_in, void* d_out, int out_size, void* d_ws, size_t ws_size,
                              hipStream_t stream) {
  (void)in_sizes; (void)n_in; (void)out_size; (void)ws_size;
  (void)hipFuncSetAttribute((const void*)fused_mha_s4d, hipFuncAttributeMaxDynamicSharedMemorySize, LDS_BYTES);
  fused_mha_s4d<<<256, 512, LDS_BYTES, stream>>>((const char*)d_in[0], (const char*)d_in[1], (const char*)d_in[2], (const char*)d_in[3],
                                                 (const char*)d_in[4], (const char*)d_in[5], (const char*)d_in[6], (const char*)d_in[7],
                                                 (const char*)d_in[8], (const char*)d_in[9], (char*)d_out,
                                                 (char*)d_ws + (WSEND ? ((ws_size - WS_END) & ~(size_t)0xfffff) : 0));
}
```

```cpp
#include <hip/hip_runtime.h>
#include <hip/hip_bf16.h>
#include <stdint.h>

typedef short s16x8 __attribute__((ext_vector_type(8)));
typedef short s16x4 __attribute__((ext_vector_type(4)));
typedef short s16x2 __attribute__((ext_vector_type(2)));
typedef _Float16 h16x8 __attribute__((ext_vector_type(8)));
typedef _Float16 h16x2 __attribute__((ext_vector_type(2)));
typedef __bf16 b16x2 __attribute__((ext_vector_type(2)));
typedef float f32x16 __attribute__((ext_vector_type(16)));
typedef float f32x4 __attribute__((ext_vector_type(4)));
typedef float f32x2 __attribute__((ext_vector_type(2)));
typedef unsigned u32x4 __attribute__((ext_vector_type(4)));
typedef unsigned u32x2 __attribute__((ext_vector_type(2)));
typedef unsigned long long u64;
#define LAS __attribute__((address_space(3)))
#define GAS __attribute__((address_space(1)))
#if defined(__HIP_DEVICE_COMPILE__)
#define NOPK __attribute__((target("no-packed-fp32-ops")))
#else
#define NOPK
#endif
#define DI NOPK __device__ __forceinline__
#define SBAR() __builtin_amdgcn_sched_barrier(0)
#define PIN(x) asm volatile("" : "+v"(x))
#define MFMA32(a, b, c) __builtin_amdgcn_mfma_f32_32x32x16_bf16((a), (b), (c), 0, 0, 0)
#define MFMA32H(a, b, c) __builtin_amdgcn_mfma_f32_32x32x16_f16((a), (b), (c), 0, 0, 0)
#define MFMA16(a, b, c) __builtin_amdgcn_mfma_f32_16x16x32_bf16((a), (b), (c), 0, 0, 0)
#define AUX_SC1 16

#define PREFETCH 1
#define WSEND 0
#define LAZY 1
#define APRIO 2
#define INPF 0
#define STAMPS 0

constexpr unsigned WS_EPOCH = 0;
constexpr unsigned WS_QFLAG = 4096;
constexpr unsigned WS_OFLAG = WS_QFLAG + 256 * 128;
constexpr unsigned WS_UFLAG = WS_OFLAG + 1024 * 128;
constexpr unsigned WS_YFLAG = WS_UFLAG + 256 * 128;
constexpr unsigned WS_KM = WS_YFLAG + 256 * 128;
constexpr unsigned WS_QN = WS_KM + 4096;
constexpr unsigned WS_QKV = 393216;
constexpr unsigned WS_KV = WS_QKV + 16 * 64 * 2048;
constexpr unsigned WS_O = WS_QKV + 48 * 64 * 2048;
constexpr unsigned WS_UT = WS_O + 64 * 16 * 2048;
constexpr unsigned WS_YT = WS_UT + 256 * 8192;
constexpr unsigned WS_DBG = WS_YT + 224 * 8192;
constexpr unsigned WS_END = WS_DBG + 256 * 8 * 16 * 8;
static_assert(WS_QN + 16 * 2048 * 4 <= WS_QKV, "ws map");

constexpr unsigned L_RING = 0, SLOT_B = 28672, XCH_B = 16384;
constexpr unsigned L_B = 100352;
constexpr unsigned TAB_W = 14000, TAB_HI = 8448;
constexpr unsigned L_UIMG = 4 * SLOT_B + 2 * TAB_W;
constexpr unsigned L_T = L_B + 56000, T_B = 3088;
constexpr unsigned KC_S = 4160, UC_S = 4640, CH_B = 4 * KC_S + 2 * UC_S;
constexpr unsigned L_YIMG = L_B + 49152;
constexpr unsigned L_CTL = 163584;
constexpr unsigned LDS_BYTES = 163840;
constexpr unsigned C_A = L_CTL, C_B = L_CTL + 4, C_GO = L_CTL + 8, C_APUB = L_CTL + 12, C_BPUB = L_CTL + 16, C_WO = L_CTL + 20, C_BFREE = L_CTL + 24, C_BTAG = L_CTL + 28, C_U = L_CTL + 32;
constexpr unsigned C_L = L_CTL + 64, C_R = L_CTL + 96;
constexpr unsigned L_MAX = 162816;
static_assert(L_T + 2 * T_B <= L_MAX, "lds map");

constexpr unsigned SPIN_CAP = 1u << 22;
constexpr float LOG2E = 1.4426950408889634f;
constexpr float QSCALE = 0.18898223650461362f * LOG2E;
constexpr float SAFE_BOUND = 48.0f;

template <int V> struct IC { static constexpr int value = V; };
DI unsigned f2u(float f) { return __builtin_bit_cast(unsigned, f); }
DI float u2f(unsigned u) { return __builtin_bit_cast(float, u); }
DI unsigned umax(unsigned a, unsigned b) { return a > b ? a : b; }
DI float fexp(float x) { return __builtin_amdgcn_exp2f(x * LOG2E); }
DI float bf_lo(unsigned v) { return u2f(v << 16); }
DI float bf_hi(unsigned v) { return u2f(v & 0xffff0000u); }
DI unsigned pk_bf16(float a, float b) { f32x2 v = {a, b}; return __builtin_bit_cast(unsigned, __builtin_convertvector(v, b16x2)); }
DI unsigned pk_f16(float a, float b) { f32x2 v = {a, b}; return __builtin_bit_cast(unsigned, __builtin_convertvector(v, h16x2)); }
DI float bf_round(float a) { return bf_lo(pk_bf16(a, a)); }
template <int OFF> DI void glds(unsigned voff, const char* sbase, unsigned lds) {
  asm volatile("s_add_u32 m0, %2, %3\n\ts_nop 0\n\tglobal_load_lds_dwordx4 %0, %1" :: "v"(voff), "s"(sbase), "s"(lds), "i"(OFF) : "memory", "scc");
}
DI s16x4 tr_read(unsigned a) { return __builtin_bit_cast(s16x4, __builtin_amdgcn_ds_read_tr16_b64_v4i16((LAS s16x4*)(uintptr_t)a)); }
DI s16x8 cat8(s16x4 lo, s16x4 hi) { return __builtin_shufflevector(lo, hi, 0, 1, 2, 3, 4, 5, 6, 7); }
template <class T> DI T lds_ld(unsigned a) { return *(LAS T*)(uintptr_t)a; }
template <class T> DI void lds_st(unsigned a, T v) { *(LAS T*)(uintptr_t)a = v; }
#define LDS_POLL(V, A, S, N, T, EXTRA) "s_mov_b32 " N ", 0\n1:\n\t" EXTRA "ds_read_b32 " V ", " A "\n\ts_add_u32 " N ", " N ", 1024\n\ts_cselect_b32 " S ", -1, 0\n\t" \
  "s_waitcnt lgkmcnt(0)\n\tv_or_b32 " V ", " S ", " V "\n\ts_nop 0\n\tv_readfirstlane_b32 " S ", " V "\n\ts_cmp_lt_u32 " S ", " T "\n\ts_cbranch_scc1 1b\n\t"
DI void lds_wait_ge(unsigned a, unsigned target) {
  unsigned v, sv, n;
  asm volatile(LDS_POLL("%0", "%3", "%1", "%2", "%4", "") : "=&v"(v), "=&s"(sv), "=&s"(n) : "v"(a), "s"(target) : "memory", "scc");
}
DI void lds_wait_ge_slow(unsigned a, unsigned target) {
  unsigned v, sv, n;
  asm volatile(LDS_POLL("%0", "%3", "%1", "%2", "%4", "s_sleep 1\n\t") : "=&v"(v), "=&s"(sv), "=&s"(n) : "v"(a), "s"(target) : "memory", "scc");
}
DI void lds_arrive(unsigned a) {
  asm volatile("s_waitcnt lgkmcnt(0)\n\ts_mov_b64 exec, 1\n\tds_add_u32 %0, %1\n\ts_mov_b64 exec, -1" :: "v"(a), "v"(1u) : "memory");
}
DI void lds_arrive_nw(unsigned a) {
  asm volatile("s_mov_b64 exec, 1\n\tds_add_u32 %0, %1\n\ts_mov_b64 exec, -1" :: "v"(a), "v"(1u) : "memory");
}
DI void role_sync(unsigned a, unsigned nth) { lds_arrive(a); lds_wait_ge(a, nth * 4u); }
DI void role_sync_slow(unsigned a, unsigned nth) { lds_arrive(a); lds_wait_ge_slow(a, nth * 4u); }
DI bool last_of_four(unsigned a, int lane, unsigned round) {
  unsigned old = 0;
  if (lane == 0) old = __hip_atomic_fetch_add((LAS unsigned*)(uintptr_t)a, 1u, __ATOMIC_RELAXED, __HIP_MEMORY_SCOPE_WORKGROUP);
  return (unsigned)__builtin_amdgcn_readfirstlane(old) == round * 4u + 3u;
}
DI void st128(u32x4 v, __amdgpu_buffer_rsrc_t r, unsigned voff, unsigned soff) {
  __builtin_amdgcn_raw_buffer_store_b128(v, r, voff, soff, AUX_SC1);
  asm volatile("s_nop 1" :: "v"(v) : "memory");
}
DI void drain_vm() { asm volatile("s_waitcnt vmcnt(0)" ::: "memory"); }
DI void flag_store(char* ws, unsigned off, u64 tag) { __hip_atomic_store((GAS u64*)(ws + off), tag, __ATOMIC_RELAXED, __HIP_MEMORY_SCOPE_AGENT); }
DI void flag_wait(char* ws, unsigned off, u64 tag) {
#pragma unroll 1
  for (unsigned n = 0; n < SPIN_CAP; ++n) {
    u64 v = __hip_atomic_load((GAS u64*)(ws + off), __ATOMIC_RELAXED, __HIP_MEMORY_SCOPE_AGENT);
    if (__builtin_amdgcn_ballot_w64(v != tag) == 0ull) break;
    __builtin_amdgcn_s_sleep(2);
  }
  asm volatile("" ::: "memory");
}
template <int CTRL> DI unsigned dpp_max(unsigned v) { unsigned t = __builtin_amdgcn_update_dpp(0u, v, CTRL, 0xf, 0xf, false); return v > t ? v : t; }
DI unsigned wave_umax(unsigned v) {
  v = dpp_max<0x111>(v); v = dpp_max<0x112>(v); v = dpp_max<0x114>(v); v = dpp_max<0x118>(v);
  v = dpp_max<0x142>(v); v = dpp_max<0x143>(v);
  return __builtin_amdgcn_readlane(v, 63);
}
DI float half_swap_add(float v) {
  auto r = __builtin_amdgcn_permlane32_swap(f2u(v), f2u(v), false, false);
  return u2f(r[0]) + u2f(r[1]);
}
DI float half_swap_max(float v) {
  auto r = __builtin_amdgcn_permlane32_swap(f2u(v), f2u(v), false, false);
  return __builtin_fmaxf(u2f(r[0]), u2f(r[1]));
}
DI f32x2 cmul(f32x2 a, f32x2 b) { f32x2 r; r.x = __builtin_fmaf(-a.y, b.y, a.x * b.x); r.y = __builtin_fmaf(a.y, b.x, a.x * b.y); return r; }
DI void hmul3(unsigned a, unsigned b1, unsigned b2, unsigned b3, unsigned& r1, unsigned& r2, unsigned& r3) {
  asm("v_pk_mul_f16 %0, %3, %4 op_sel:[0,0] op_sel_hi:[0,1]\n\tv_pk_mul_f16 %1, %3, %5 op_sel:[0,0] op_sel_hi:[0,1]\n\tv_pk_mul_f16 %2, %3, %6 op_sel:[0,0] op_sel_hi:[0,1]\n\t"
      "v_pk_fma_f16 %0, %3, %4, %0 op_sel:[1,1,0] op_sel_hi:[1,0,1] neg_lo:[1,0,0]\n\tv_pk_fma_f16 %1, %3, %5, %1 op_sel:[1,1,0] op_sel_hi:[1,0,1] neg_lo:[1,0,0]\n\t"
      "v_pk_fma_f16 %2, %3, %6, %2 op_sel:[1,1,0] op_sel_hi:[1,0,1] neg_lo:[1,0,0]"
      : "=&v"(r1), "=&v"(r2), "=&v"(r3) : "v"(a), "v"(b1), "v"(b2), "v"(b3));
}
DI f32x2 zpow(float rho, float th, float p) {
  const float e = __builtin_amdgcn_exp2f(rho * p), f = __builtin_amdgcn_fractf(th * p);
  f32x2 r; r.x = e * __builtin_amdgcn_cosf(f); r.y = e * __builtin_amdgcn_sinf(f); return r;
}
#define STAMP(k) do { } while (0)
#define STAMP_FLUSH() do { } while (0)

#define QR(a, n) "v[" #a "+" #n ":" #a "+" #n "+1]"
#define Q_READ(F, XA, S) "ds_read_b128 v[" #F ":" #F "+3], " XA "\n\t" \
  "ds_read_b64_tr_b16 " QR(F, 4) ", %[wa] offset:" #S "*3072\n\tds_read_b64_tr_b16 " QR(F, 6) ", %[wa] offset:" #S "*3072+768\n\t" \
  "ds_read_b64_tr_b16 " QR(F, 8) ", %[wa] offset:" #S "*3072+56\n\tds_read_b64_tr_b16 " QR(F, 10) ", %[wa] offset:" #S "*3072+824\n\t" \
  "ds_read_b64_tr_b16 " QR(F, 12) ", %[wa] offset:" #S "*3072+112\n\tds_read_b64_tr_b16 " QR(F, 14) ", %[wa] offset:" #S "*3072+880\n\t"
#define Q_T(ACC, F, n) "v_mfma_f32_32x32x16_bf16 " ACC ", v[" #F "+" #n ":" #F "+" #n "+3], v[" #F ":" #F "+3], " ACC "\n\t"
#define Q_N(ACC, F, n) "v_mfma_f32_32x32x16_bf16 " ACC ", v[" #F ":" #F "+3], v[" #F "+" #n ":" #F "+" #n "+3], " ACC "\n\t"
#define Q_MMA(F, M0, M1, M2) M0("%[a0]", F, 4) M1("%[a1]", F, 8) M2("%[a2]", F, 12)
#define Q_ARRIVE "s_mov_b64 exec, 1\n\tds_add_u32 %[ca], %[one]\n\ts_mov_b64 exec, -1\n\t"
#define Q_TR(F, n, S, O) "ds_read_b64_tr_b16 " QR(F, n) ", %[wa] offset:" #S "*3072+" #O "\n\t"
#define Q_GROUP(G, F, XA, S, M0, M1, M2) \
  M0("%[a0]", G, 4) "ds_read_b128 v[" #F ":" #F "+3], " XA "\n\t" Q_TR(F, 4, S, 0) Q_TR(F, 6, S, 768) \
  M1("%[a1]", G, 8) Q_TR(F, 8, S, 56) Q_TR(F, 10, S, 824) \
  M2("%[a2]", G, 12) Q_TR(F, 12, S, 112) Q_TR(F, 14, S, 880)
#define QKV_BODY(M0, M1, M2) \
  "2:\n\t" \
  "v_add_u32 %[cl], 4, %[cl]\n\ts_waitcnt lgkmcnt(7)\n\t" \
  Q_GROUP(100, 132, "%[x2]", 2, M0, M1, M2) "ds_read_b32 %[tv], %[cl]\n\t" \
  "s_waitcnt lgkmcnt(8)\n\t" \
  Q_GROUP(116, 148, "%[x3]", 3, M0, M1, M2) \
  "s_cmp_eq_u32 %[tg], 28\n\ts_cbranch_scc1 5f\n\t" \
  "s_cmp_eq_u32 %[tg], 16\n\ts_cselect_b32 %[ts], %[wrap], %[step]\n\ts_add_u32 %[tg], %[tg], 4\n\t" \
  "v_add_u32 %[x0], %[ts], %[x0]\n\tv_add_u32 %[x1], %[ts], %[x1]\n\tv_add_u32 %[x2], %[ts], %[x2]\n\tv_add_u32 %[x3], %[ts], %[x3]\n\tv_add_u32 %[wa], %[ts], %[wa]\n\t" \
  "s_waitcnt lgkmcnt(7)\n\tv_readfirstlane_b32 %[ts], %[tv]\n\ts_cmp_ge_u32 %[ts], 2\n\ts_cbranch_scc1 0f\n\t" \
  LDS_POLL("%[tv]", "%[cl]", "%[ts]", "%[tn]", "2", "") \
  "0:\n\t" \
  Q_GROUP(132, 100, "%[x0]", 0, M0, M1, M2) \
  "s_cmp_eq_u32 %[tg], 8\n\ts_cbranch_scc1 3f\n\ts_waitcnt vmcnt(0)\n3:\n\t" \
  "s_waitcnt lgkmcnt(7)\n\t" Q_ARRIVE \
  Q_GROUP(148, 116, "%[x1]", 1, M0, M1, M2) \
  "v_add_u32 %[ca], 4, %[ca]\n\ts_branch 2b\n" \
  "5:\n\t" \
  "s_waitcnt lgkmcnt(7)\n\t" Q_MMA(132, M0, M1, M2) "s_waitcnt lgkmcnt(0)\n\t" Q_ARRIVE "s_nop 0\n\t" Q_MMA(148, M0, M1, M2)
#define QKV_LOOP() asm volatile( \
  "s_mov_b32 %[tg], 4\n\t" LDS_POLL("%[tv]", "%[cl]", "%[ts]", "%[tn]", "2", "") \
  Q_READ(100, "%[x0]", 0) Q_READ(116, "%[x1]", 1) \
  "s_cmp_gt_u32 %[g], 10\n\ts_cbranch_scc1 7f\n\ts_cmp_eq_u32 %[g], 10\n\ts_cbranch_scc1 6f\n" \
  QKV_BODY(Q_T, Q_T, Q_T) "s_branch 8f\n6:\n" QKV_BODY(Q_T, Q_T, Q_N) "s_branch 8f\n7:\n" QKV_BODY(Q_N, Q_N, Q_N) \
  "8:\n\ts_nop 7\n\ts_nop 7" \
  : [a0] "+v"(acc[0]), [a1] "+v"(acc[1]), [a2] "+v"(acc[2]), [x0] "+v"(xa0), [x1] "+v"(xa1), [x2] "+v"(xa2), [x3] "+v"(xa3), [wa] "+v"(wa), \
    [tv] "=&v"(tv), [ts] "=&s"(ts), [tn] "=&s"(tn), [tg] "=&s"(tg), [ca] "+v"(car), [cl] "+v"(cla) \
  : [one] "v"(1u), [step] "s"(SLOT_B), [wrap] "s"(0u - 3u * SLOT_B), [g] "s"(g) \
  : "memory", "scc", V100_163)
#define V10(p) "v" #p "0", "v" #p "1", "v" #p "2", "v" #p "3", "v" #p "4", "v" #p "5", "v" #p "6", "v" #p "7", "v" #p "8", "v" #p "9"
#define V100_163 V10(10), V10(11), V10(12), V10(13), V10(14), V10(15), "v160", "v161", "v162", "v163"

NOPK __global__ void __launch_bounds__(512, 2)
fused_mha_s4d(const char* __restrict__ x, const char* __restrict__ wqkv, const char* __restrict__ wo, const char* __restrict__ u,
              const char* __restrict__ logAr, const char* __restrict__ Aim, const char* __restrict__ Cre, const char* __restrict__ Cim,
              const char* __restrict__ logdt, const char* __restrict__ Dsk, char* __restrict__ out, char* __restrict__ ws) {
  extern __shared__ __attribute__((aligned(16))) char smem[];
  const int tid = __builtin_amdgcn_workitem_id_x(), lane = tid & 63;
  const int wid = __builtin_amdgcn_readfirstlane(tid >> 6);
  const int b = __builtin_amdgcn_workgroup_id_x(), xcd = b & 7, idx = b >> 3;
  const int r32 = lane & 31, hh = lane >> 5;
  const unsigned L0 = (unsigned)(uintptr_t)smem;
  (void)L0;
  const __amdgpu_buffer_rsrc_t wsr = __builtin_amdgcn_make_buffer_rsrc(ws, 0, WS_END, 0x00020000);
  asm volatile("" :: "s"(x), "s"(wqkv), "s"(wo), "s"(u), "s"(logAr), "s"(Aim), "s"(Cre), "s"(Cim), "s"(logdt), "s"(Dsk), "s"(out), "s"(ws));
  STAMP(0);
  asm volatile(".Lpf_begin:");
#define MAKE_TAG(e) (((u64)(unsigned)__builtin_amdgcn_readfirstlane((unsigned)((e) >> 32)) << 32 | (unsigned)__builtin_amdgcn_readfirstlane((unsigned)(e))) + 0x9E3779B97F4A7C15ull)
  const int rt = (xcd & 3) * 4 + (idx >> 3), g = (xcd >> 2) * 8 + (idx & 7);
  const int woff = (g == 15) ? 8 : 0;
  const bool has_b = xcd < 7;

  if (__builtin_expect(wid >= 4, 0)) {
    const int w = wid - 4;
    __builtin_amdgcn_s_setprio(APRIO);
    const u64 epoch = __hip_atomic_load((GAS u64*)(ws + WS_EPOCH), __ATOMIC_RELAXED, __HIP_MEMORY_SCOPE_AGENT);
    {
      u64 pc; unsigned below, above;
      asm volatile("s_getpc_b64 %0\n.Lpf_here:\n\ts_mov_b32 %1, .Lpf_here-.Lpf_begin\n\ts_mov_b32 %2, .Lpf_end-.Lpf_here" : "=s"(pc), "=s"(below), "=s"(above));
      const unsigned off = ((w * 64 + lane) * 8 + (idx & 7)) * 128;
      if (off < below + above) asm volatile("s_mov_b32 m0, %2\n\ts_nop 0\n\tglobal_load_lds_dword %0, %1" :: "v"(off), "s"(pc - below), "s"(L_MAX + 512) : "memory");
    }
    if (w == 0) lds_st<u32x4>(L_MAX + lane * 16, u32x4{0u, 0u, 0u, 0u});
    asm volatile("s_waitcnt lgkmcnt(0)\n\ts_barrier" ::: "memory");
    if (has_b) {
      lds_wait_ge(C_U, 2);
      const int oct = (lane >> 1) & 7;
      s16x8 v[2];
#pragma unroll
      for (int i = 0; i < 2; ++i) {
        const int ch = 2 * (w * 8 + i * 4 + (lane >> 4)) + (lane & 1);
        const unsigned a = L_UIMG + oct * 1024 + (((ch >> 3) ^ oct) << 4) + (ch & 7) * 2;
#pragma unroll
        for (int e = 0; e < 8; ++e) v[i][e] = lds_ld<short>(a + e * 128);
      }
#pragma unroll
      for (int i = 0; i < 2; ++i)
        st128(__builtin_bit_cast(u32x4, v[i]), wsr, ((xcd * 32 + w * 8 + i * 4 + (lane >> 4)) * 32 + (31 - idx)) * 256 + (lane & 15) * 16, WS_UT);
    }
    STAMP(9);
    f32x16 acc[3];
#pragma unroll
    for (int j = 0; j < 3; ++j) acc[j] = f32x16{};
    {
      const unsigned xa_l = w * 4096 + r32 * 128, xsw = (r32 >> 1) & 7;
      unsigned xa0 = xa_l + (((0 + hh) ^ xsw) << 4), xa1 = xa_l + (((2 + hh) ^ xsw) << 4), xa2 = xa0 ^ 64, xa3 = xa1 ^ 64;
      unsigned wa = XCH_B + (8 * hh + ((lane & 15) >> 2)) * 192 + woff + ((lane >> 4) & 1) * 32 + (lane & 3) * 8;
      unsigned tv, ts, tn, tg, car = C_R, cla = C_L;
      QKV_LOOP();
    }
    STAMP(2);
    const int rt32 = rt * 4 + w;
    const u64 tag = MAKE_TAG(epoch);
    unsigned l16 = lane * 16; PIN(l16);
    lds_wait_ge(C_BTAG, 4);
#pragma unroll
    for (int j = 0; j < 3; ++j) {
      const int sl = 3 * g + j, ty = sl >> 4;
      const unsigned so = ty == 0 ? WS_QKV + (sl * 64 + rt32) * 2048 : WS_KV + ((sl & 15) * 64 + rt32) * 4096 + (ty - 1) * 2048;
      if (ty == 0) {
#pragma unroll
        for (int i = 0; i < 16; ++i) acc[j][i] *= QSCALE;
      }
      u32x4 f0, f1;
#pragma unroll
      for (int i = 0; i < 4; ++i) { f0[i] = pk_bf16(acc[j][2 * i], acc[j][2 * i + 1]); f1[i] = pk_bf16(acc[j][8 + 2 * i], acc[j][9 + 2 * i]); }
      const bool vpad = ty == 2 && r32 >= 28, kpad = ty < 2 && hh;
      const unsigned cst = r32 == 28 ? 0x3F803F80u : 0u;
#pragma unroll
      for (int i = 0; i < 4; ++i) f0[i] = vpad ? cst : f0[i];
      f1[0] = vpad ? cst : f1[0]; f1[1] = vpad ? cst : f1[1];
      f1[2] = vpad ? cst : (kpad ? 0x3F803F80u : f1[2]); f1[3] = vpad ? cst : (kpad ? 0x00003F80u : f1[3]);
      if (j < 2) { st128(f0, wsr, l16, so); st128(f1, wsr, l16 + 1024, so); }
      if (ty < 2) {
        float n2 = 0.f, n3 = 0.f, n4 = 0.f;
#pragma unroll
        for (int i = 0; i < 6; ++i) { n2 = __builtin_fmaf(acc[j][i], acc[j][i], n2); n4 = __builtin_fmaf(acc[j][6 + i], acc[j][6 + i], n4); }
#pragma unroll
        for (int i = 12; i < 16; ++i) n3 = __builtin_fmaf(acc[j][i], acc[j][i], n3);
        unsigned nrm = f2u(__builtin_amdgcn_sqrtf(half_swap_add(n2 + n4 + (hh ? 0.f : n3))) * 1.01f);
        unsigned off = WS_QN + (sl * 64 + rt32) * 128 + lane * 4;
        if (ty == 1) { nrm = wave_umax(nrm); off = WS_KM + ((sl - 16) * 64 + rt32) * 4; }
        if (lane < (ty == 1 ? 1 : 32)) __builtin_amdgcn_raw_buffer_store_b32(nrm, wsr, off, 0, AUX_SC1);
      }
      if (j == 2) { st128(f0, wsr, l16, so); st128(f1, wsr, l16 + 1024, so); }
    }
    STAMP(14);
    drain_vm();
    STAMP(15);
    if (last_of_four(C_APUB, lane, 0)) { if (lane == 0) flag_store(ws, WS_QFLAG + (g * 16 + rt) * 128, tag); }
    __builtin_amdgcn_s_setprio(0);
    STAMP(3);
    const int h = xcd * 2 + (idx >> 4), qt = idx & 15;
    s16x8 qf[4][2], kf[4][2], vf[4][2];
    const unsigned kbase = WS_KV + (h * 64 + w * 16) * 4096;
#define LDR(rs_, soff, voff) __builtin_bit_cast(s16x8, __builtin_amdgcn_raw_buffer_load_b128(rs_, l16 + (voff), (soff), AUX_SC1))
#define LDF(soff, voff) LDR(wsr, soff, voff)
    flag_wait(ws, WS_QFLAG + ((h / 3) * 16 + qt) * 128, tag);
    unsigned qn[4];
#pragma unroll
    for (int i = 0; i < 4; ++i) {
      qn[i] = __builtin_amdgcn_raw_buffer_load_b32(wsr, r32 * 4, WS_QN + (h * 64 + qt * 4 + ((w + 1 + i) & 3)) * 128, AUX_SC1);
      const unsigned so = WS_QKV + (h * 64 + qt * 4 + ((w + 1 + i) & 3)) * 2048;
      qf[i][1] = LDF(so, 1024); qf[i][0] = LDF(so, 0);
    }
    STAMP(1);
    if (w == 0) {
      flag_wait(ws, WS_QFLAG + ((hh ? (32 + h) / 3 : (16 + h) / 3) * 16 + (lane & 15)) * 128, tag);
      lds_arrive_nw(C_GO);
    } else {
      lds_wait_ge(C_GO, 1);
    }
    STAMP(4);
    const unsigned kmv = __builtin_amdgcn_raw_buffer_load_b32(wsr, lane * 4, WS_KM + h * 256, AUX_SC1);
#define LDKV(rs_, B, soff) do { SBAR(); kf[B][0] = LDR(rs_, soff, 0); kf[B][1] = LDR(rs_, soff, 1024); vf[B][0] = LDR(rs_, soff, 2048); vf[B][1] = LDR(rs_, soff, 3072); SBAR(); } while (0)
    LDKV(wsr, 0, kbase); LDKV(wsr, 1, kbase + 4096); LDKV(wsr, 2, kbase + 8192);
    {
      const float kmax = u2f(wave_umax(kmv));
      float m[4]; bool unsafe = false;
#pragma unroll
      for (int i = 0; i < 4; ++i) {
        m[i] = u2f(qn[i]) * kmax;
        unsafe |= !(m[i] <= SAFE_BOUND);
      }
      if (__builtin_expect(__builtin_amdgcn_ballot_w64(unsafe) != 0ull, 0)) {
        float mx[4];
#pragma unroll
        for (int i = 0; i < 4; ++i) { mx[i] = -INFINITY; if (hh) { u32x4 t_ = __builtin_bit_cast(u32x4, qf[i][1]); t_[2] = 0u; t_[3] = 0u; qf[i][1] = __builtin_bit_cast(s16x8, t_); } }
#pragma unroll 1
        for (int t = 0; t < 16; ++t) {
          const s16x8 k0 = LDF(kbase + t * 4096, 0), k1 = LDF(kbase + t * 4096, 1024);
#pragma unroll
          for (int i = 0; i < 4; ++i) {
            f32x16 s = MFMA32(k0, qf[i][0], f32x16{}); s = MFMA32(k1, qf[i][1], s);
#pragma unroll
            for (int e = 0; e < 16; ++e) mx[i] = __builtin_fmaxf(mx[i], s[e]);
          }
        }
#pragma unroll
        for (int i = 0; i < 4; ++i) { const float v = half_swap_max(mx[i]);
          const unsigned k_ = f2u(v) ^ ((unsigned)((int)f2u(v) >> 31) | 0x80000000u);
          __hip_atomic_fetch_max((LAS unsigned*)(uintptr_t)(L_MAX + (((w + 1 + i) & 3) * 32 + r32) * 4), k_, __ATOMIC_RELAXED, __HIP_MEMORY_SCOPE_WORKGROUP); }
        lds_arrive(C_GO); lds_wait_ge(C_GO, 5);
#pragma unroll
        for (int i = 0; i < 4; ++i) {
          const unsigned k_ = lds_ld<unsigned>(L_MAX + (((w + 1 + i) & 3) * 32 + r32) * 4);
          m[i] = u2f(k_ ^ ((unsigned)((int)~k_ >> 31) | 0x80000000u));
        }
      }
#pragma unroll
      for (int i = 0; i < 4; ++i) {
        const float m0 = bf_round(-m[i]), r1 = -m[i] - m0, m1 = bf_round(r1), m2 = r1 - m1;
        if (hh) { u32x4 t_ = __builtin_bit_cast(u32x4, qf[i][1]); t_[2] = pk_bf16(m0, m1); t_[3] = pk_bf16(m2, 0.f); qf[i][1] = __builtin_bit_cast(s16x8, t_); }
      }
    }
    f32x16 o[4];
#pragma unroll
    for (int i = 0; i < 4; ++i) o[i] = f32x16{};
    f32x16 sA, sB; u32x4 p0, p1 = u32x4{0u, 0u, 0u, 0u};
    sA = MFMA32(kf[0][0], qf[0][0], f32x16{}); sA = MFMA32(kf[0][1], qf[0][1], sA);
    STAMP(5);
    unsigned redw = L_B + l16; PIN(redw);
#define EX(S, i) S[i] = __builtin_amdgcn_exp2f(S[i])
#define PK(S, i) pk_bf16(S[i], S[(i) + 1])
#define BF(P) __builtin_bit_cast(s16x8, P)
#define STEP(C, N, KN, QN, VP, OP, VC, OC) do { \
      N = MFMA32(KN[0], QN[0], f32x16{}); SBAR(); EX(C, 0); EX(C, 1); EX(C, 2); EX(C, 3); EX(C, 4); SBAR(); \
      OP = MFMA32(VP[1], BF(p1), OP); SBAR(); EX(C, 5); EX(C, 6); EX(C, 7); p0[0] = PK(C, 0); p0[1] = PK(C, 2); p0[2] = PK(C, 4); p0[3] = PK(C, 6); PIN(p0); SBAR(); \
      N = MFMA32(KN[1], QN[1], N); SBAR(); EX(C, 8); EX(C, 9); EX(C, 10); EX(C, 11); EX(C, 12); SBAR(); \
      OC = MFMA32(VC[0], BF(p0), OC); SBAR(); EX(C, 13); EX(C, 14); EX(C, 15); p1[0] = PK(C, 8); p1[1] = PK(C, 10); p1[2] = PK(C, 12); p1[3] = PK(C, 14); PIN(p1); SBAR(); \
    } while (0)
#define RED_WRITE(i) do { const unsigned a_ = redw + (((w + 1 + (i)) & 3) * 3 + (2 - (i))) * 4096; \
      _Pragma("unroll") for (int q_ = 0; q_ < 4; ++q_) lds_st<f32x4>(a_ + q_ * 1024, f32x4{o[i][4 * q_], o[i][4 * q_ + 1], o[i][4 * q_ + 2], o[i][4 * q_ + 3]}); } while (0)
#define TILE(T, TP, TN, RS, LAST) do { \
      STEP(sA, sB, kf[T], qf[1], vf[TP], o[3], vf[T], o[0]); \
      if (!LAST) LDKV(RS, TP, kcur + (T + 3) * 4096); \
      STEP(sB, sA, kf[T], qf[2], vf[T], o[0], vf[T], o[1]); \
      if (LAST) RED_WRITE(0); \
      STEP(sA, sB, kf[T], qf[3], vf[T], o[1], vf[T], o[2]); \
      if (LAST) RED_WRITE(1); \
      STEP(sB, sA, kf[TN], qf[0], vf[T], o[2], vf[T], o[3]); \
      if (LAST) RED_WRITE(2); \
    } while (0)
    vf[3][1] = s16x8{};
    unsigned kcur = kbase;
#pragma unroll 1
    for (int tb = 0;; tb += 4) {
      const __amdgpu_buffer_rsrc_t wsr_next = __builtin_amdgcn_make_buffer_rsrc(ws, 0, tb < 12 ? WS_END : 0u, 0x00020000);
      TILE(0, 3, 1, wsr, false);
      TILE(1, 0, 2, wsr_next, false);
      TILE(2, 1, 3, wsr_next, false);
      if (tb == 12) break;
      TILE(3, 2, 0, wsr, false);
      kcur += 16384;
      if (STAMPS) { if (tb == 0) STAMP(11); else if (tb == 4) STAMP(12); else STAMP(13); }
    }
    lds_wait_ge(C_BFREE, 4);
    TILE(3, 2, 0, wsr, true);
    o[3] = MFMA32(vf[3][1], BF(p1), o[3]);
    STAMP(6);
    role_sync(C_A, 1);
    {
      f32x4 pv[12];
#pragma unroll
      for (int k = 0; k < 12; ++k) pv[k] = lds_ld<f32x4>(redw + w * 12288 + k * 1024);
      SBAR();
      f32x16 a = o[3];
#pragma unroll
      for (int k = 0; k < 12; ++k) { const int q = k & 3; a[4 * q] += pv[k][0]; a[4 * q + 1] += pv[k][1]; a[4 * q + 2] += pv[k][2]; a[4 * q + 3] += pv[k][3]; }
      auto lr = __builtin_amdgcn_permlane32_swap(f2u(a[12]), f2u(a[12]), false, false);
      const float inv = __builtin_amdgcn_rcpf(u2f(lr[1]));
      u32x4 f0, f1;
#pragma unroll
      for (int i = 0; i < 4; ++i) { f0[i] = pk_bf16(a[2 * i] * inv, a[2 * i + 1] * inv); f1[i] = pk_bf16(a[8 + 2 * i] * inv, a[9 + 2 * i] * inv); }
      if (hh) { f1[2] = 0u; f1[3] = 0u; }
      const unsigned so = WS_O + ((qt * 4 + w) * 16 + h) * 2048;
      st128(f0, wsr, l16, so);
      st128(f1, wsr, l16 + 1024, so);
      lds_arrive(C_A);
      drain_vm();
      if (lane == 0) flag_store(ws, WS_OFLAG + ((qt * 4 + w) * 16 + h) * 128, tag);
    }
    STAMP(7);
    const int rs = xcd * 8 + (idx >> 2), cg = idx & 3, n0 = w < 3 ? 32 * w : 80;
    lds_wait_ge(C_WO, 4);
    s16x8 wf[16][2];
    {
      unsigned wa = L_RING + (4 * hh + ((lane & 15) >> 2)) * 224 + n0 * 2 + ((lane >> 4) & 1) * 32 + (lane & 3) * 8;
      unsigned wb2 = wa + (hh ? 16 * 224 : 24 * 224), wa1 = wa + 8 * 28 * 224, wb3 = wb2 + 8 * 28 * 224;
      PIN(wa); PIN(wb2); PIN(wa1); PIN(wb3);
#pragma unroll
      for (int hd = 0; hd < 16; ++hd) {
        const unsigned a_ = (hd < 8 ? wa : wa1) + (hd & 7) * 28 * 224, b_ = (hd < 8 ? wb2 : wb3) + (hd & 7) * 28 * 224;
        wf[hd][0] = cat8(tr_read(a_), tr_read(a_ + 8 * 224));
        wf[hd][1] = cat8(tr_read(a_ + 16 * 224), tr_read(b_));
      }
    }
    lds_wait_ge(C_A, 2 * 4);
    flag_wait(ws, WS_OFLAG + (rs * 16 + w * 4 + (lane & 3)) * 128, tag);
    STAMP(8);
    {
      u32x4 of[8];
#pragma unroll
      for (int i = 0; i < 8; ++i) of[i] = __builtin_amdgcn_raw_buffer_load_b128(wsr, l16 + (i & 3) * 1024, WS_O + (rs * 16 + w * 4) * 2048 + (i >> 2) * 4096, AUX_SC1);
#pragma unroll
      for (int i = 0; i < 8; ++i) lds_st<u32x4>(redw + w * 8192 + i * 1024, of[i]);
    }
    role_sync(C_A, 3);
    f32x16 d = f32x16{};
    {
      s16x8 ob[8];
#pragma unroll
      for (int k = 0; k < 8; ++k) ob[k] = lds_ld<s16x8>(redw + k * 1024);
      SBAR();
#pragma unroll
      for (int k = 0; k < 32; ++k) {
        d = MFMA32(wf[k >> 1][k & 1], ob[k & 7], d);
        if (k + 8 < 32) ob[k & 7] = lds_ld<s16x8>(redw + (k + 8) * 1024);
        SBAR();
      }
    }
    {
      char* op = out + (size_t)(rs * 32 * 1792 + (cg * 112 + n0) * 2) + (unsigned)(r32 * 1792 + hh * 16);
#pragma unroll
      for (int k = 0; k < 2; ++k) {
        const unsigned a0 = pk_bf16(d[8 * k], d[8 * k + 1]), a1 = pk_bf16(d[8 * k + 2], d[8 * k + 3]);
        const unsigned b0 = pk_bf16(d[8 * k + 4], d[8 * k + 5]), b1 = pk_bf16(d[8 * k + 6], d[8 * k + 7]);
        auto s0 = __builtin_amdgcn_permlane32_swap(a0, b0, false, false);
        auto s1 = __builtin_amdgcn_permlane32_swap(a1, b1, false, false);
        *(GAS u32x4*)(op + k * 32) = u32x4{s0[0], s1[0], s0[1], s1[1]};
      }
    }
    STAMP(10);
    STAMP_FLUSH();
    asm volatile(".Lpf_end:");
  } else {
    const int wb = wid ^ 2;
    const int c = wb & 1, half = wb >> 1;
    const int d = (has_b ? xcd * 64 : 0) + idx * 2 + c;
    const unsigned po = d * 512 + lane * 4;
    unsigned pdt, p0[4], p1[4];
    u64 tag;
#define LOAD_P(P, O) do { P[0] = *(const GAS unsigned*)(logAr + (O) + po); P[1] = *(const GAS unsigned*)(Aim + (O) + po); \
      P[2] = *(const GAS unsigned*)(Cre + (O) + po); P[3] = *(const GAS unsigned*)(Cim + (O) + po); } while (0)
    if (half) {
      unsigned xo[4], wo_[6];
#pragma unroll
      for (int q = 0; q < 4; ++q) xo[q] = (q * 8 + (lane >> 3)) * 896 + (((lane & 7) ^ ((q & 1) * 4 + (lane >> 4))) << 4);
      const char* xb = x + (size_t)(rt * 128 + c * 64) * 896;
      const char* wq = wqkv + g * 168 - woff;
      u64 epoch;
      asm volatile("global_load_dwordx2 %0, %1, %2 sc1" : "=v"(epoch) : "v"(0u), "s"(ws + WS_EPOCH) : "memory");
#define X_PIECES(B, D) do { glds<0>(xo[0], B, D); glds<1024>(xo[1], B, D); glds<2048>(xo[2], B, D); glds<3072>(xo[3], B, D); \
        glds<4096>(xo[0], (B) + 32 * 896, D); glds<5120>(xo[1], (B) + 32 * 896, D); glds<6144>(xo[2], (B) + 32 * 896, D); glds<7168>(xo[3], (B) + 32 * 896, D); } while (0)
#define W_PIECES(B, D) do { glds<0>(wo_[0], B, D); glds<1024>(wo_[1], B, D); glds<2048>(wo_[2], B, D); glds<3072>(wo_[3], B, D); glds<4096>(wo_[4], B, D); glds<5120>(wo_[5], B, D); } while (0)
      {
        const char* up = u + (size_t)(idx * 64 + 56 - 32 * c) * 896 + (has_b ? xcd * 128 : 0);
        const unsigned uo = (7 - (lane >> 3)) * 896, k4 = (lane & 7) ^ (4 * c);
        glds<0>(uo + (k4 << 4), up, L_UIMG + c * 4096); glds<1024>(uo + ((k4 ^ 1) << 4), up - 8 * 896, L_UIMG + c * 4096);
        glds<2048>(uo + ((k4 ^ 2) << 4), up - 16 * 896, L_UIMG + c * 4096); glds<3072>(uo + ((k4 ^ 3) << 4), up - 24 * 896, L_UIMG + c * 4096);
      }
      X_PIECES(xb, L_RING + c * 8192);
#pragma unroll
      for (int i = 0; i < 6; ++i) {
        const unsigned p = (6 * c + i) * 64 + lane, row = (p * 43691u) >> 19; unsigned e = p - row * 12; e = e > 10 ? 10 : e;
        wo_[i] = row * 2688 + e * 16;
      }
      W_PIECES(wq, L_RING + XCH_B + c * 6144);
      asm volatile("s_barrier" ::: "memory");
      asm volatile("s_waitcnt vmcnt(14)" : "+v"(epoch) :: "memory");
      lds_arrive_nw(C_U);
      tag = MAKE_TAG(epoch); asm volatile("" :: "s"(tag)); lds_arrive_nw(C_BTAG);
      X_PIECES(xb + 128, L_RING + c * 8192 + SLOT_B);
      W_PIECES(wq + 64 * 2688, L_RING + XCH_B + c * 6144 + SLOT_B);
      asm volatile("s_waitcnt vmcnt(14)" ::: "memory");
      lds_arrive_nw(C_L);
      STAMP(1);
#pragma unroll 1
      for (int n = 2; n < 7; ++n) {
        if (n >= 4) {
          lds_wait_ge(C_R + 4 * (n - 4), 4);
          if (n == 5 && c == 0 && has_b && lane == 0) flag_store(ws, WS_UFLAG + (xcd * 32 + idx) * 128, tag);
        }
        const unsigned slot = (n & 3) * SLOT_B;
        X_PIECES(xb + n * 128, L_RING + c * 8192 + slot);
        if (n > 2) { asm volatile("s_waitcnt vmcnt(22)" ::: "memory"); lds_arrive_nw(C_L + 4 * (n - 2)); }
        W_PIECES(wq + n * 64 * 2688, L_RING + XCH_B + c * 6144 + slot);
      }
      asm volatile("s_waitcnt vmcnt(14)" ::: "memory");
      lds_arrive_nw(C_L + 20);
      STAMP(4);
      pdt = *(const GAS unsigned short*)(logdt + d * 2);
      LOAD_P(p0, 256);
      drain_vm();
      lds_arrive_nw(C_L + 24);
      STAMP(12);
#pragma unroll
      for (int i = 0; i < 4; ++i) { p0[i] <<= 16; p1[i] = 0u; }
      lds_wait_ge(C_R + 12, 4);
    } else {
      pdt = *(const GAS unsigned short*)(logdt + d * 2);
      LOAD_P(p0, 0); LOAD_P(p1, 256);
      const u64 epoch = __hip_atomic_load((GAS u64*)(ws + WS_EPOCH), __ATOMIC_RELAXED, __HIP_MEMORY_SCOPE_AGENT);
      asm volatile("s_barrier" ::: "memory");
      tag = MAKE_TAG(epoch); asm volatile("" :: "s"(tag)); lds_arrive_nw(C_BTAG);
#pragma unroll
      for (int i = 0; i < 4; ++i) p1[i] &= 0xffff0000u;
    }
    __builtin_amdgcn_s_setprio(1);
    STAMP(9);
    const unsigned T = L_T + c * T_B;
    if (has_b) {
      const float dt = bf_round(fexp(bf_lo(pdt)));
      const unsigned cm = umax(umax(umax(p0[2] << 16 & 0x7fff0000u, p0[2] & 0x7fff0000u), umax(p0[3] << 16 & 0x7fff0000u, p0[3] & 0x7fff0000u)),
                               umax(p1[2] & 0x7fff0000u, p1[3] & 0x7fff0000u));
      unsigned eb = wave_umax(cm) >> 23; eb = eb < 1u ? 1u : (eb > 253u ? 253u : eb);
      const float csc = u2f((254u - eb) << 23), cinv = u2f(eb << 23);
      const unsigned tb_ = (half ? 3 * SLOT_B : 4 * SLOT_B) + c * TAB_W;
      const unsigned lw = tb_ + (lane >> 3) * 1056 + ((lane >> 2) & 1) * 528 + (lane & 3) * 4;
      const unsigned hw = tb_ + TAB_HI + (lane >> 3) * 672 + ((lane >> 2) & 1) * 336 + (lane & 3) * 4;
      const unsigned lr = tb_ + hh * 528 + r32 * 16, hr = tb_ + TAB_HI + hh * 336 + r32 * 16;
      f32x16 kd = f32x16{};
#pragma unroll 1
      for (int r = half ? 2 : 0; r < 3; ++r) {
        const float rho = -bf_round(fexp(bf_hi(p0[0]))) * dt * LOG2E, th = bf_hi(p0[1]) * dt * 0.15915494309189535f;
        f32x2 hA, hB; hA.x = bf_hi(p0[2]) * csc; hA.y = bf_hi(p0[3]) * -csc;
#pragma unroll
        for (int i = 0; i < 4; ++i) p0[i] = r == 0 ? p0[i] << 16 : p1[i];
        const f32x2 z8 = zpow(rho, th, 8.f), z32 = zpow(rho, th, 32.f), z1 = zpow(rho, -th, 1.f), z256 = zpow(rho, -th, 256.f);
        const f32x2 z16 = cmul(z8, z8), z24 = cmul(z8, z16);
        const unsigned q1 = pk_f16(z8.x, z8.y), q2 = pk_f16(z16.x, z16.y), q3 = pk_f16(z24.x, z24.y);
        lds_st<unsigned>(lw, 0x00003C00u); lds_st<unsigned>(lw + 16, q1); lds_st<unsigned>(lw + 32, q2); lds_st<unsigned>(lw + 48, q3);
        f32x2 e = z32;
#pragma unroll
        for (int a = 1; a < 8; ++a) {
          const unsigned p_ = pk_f16(e.x, e.y); unsigned m1, m2, m3;
          if (a < 7) e = cmul(e, z32);
          hmul3(p_, q1, q2, q3, m1, m2, m3);
          lds_st<unsigned>(lw + 64 * a, p_); lds_st<unsigned>(lw + 64 * a + 16, m1); lds_st<unsigned>(lw + 64 * a + 32, m2); lds_st<unsigned>(lw + 64 * a + 48, m3);
        }
#define PUTH(i, v) lds_st<unsigned>(hw + (i) * 16, pk_f16((v).x, (v).y))
        PUTH(0, hA); f32x2 g0 = cmul(hA, z256); PUTH(8, g0);
        hB = cmul(hA, z1); PUTH(1, hB); hA = cmul(hB, z1); PUTH(2, hA); { f32x2 t_ = cmul(hA, z256); PUTH(9, t_); }
        hB = cmul(hA, z1); PUTH(3, hB); hA = cmul(hB, z1); PUTH(4, hA); f32x2 g4 = cmul(hA, z256); PUTH(10, g4);
        hB = cmul(hA, z1); PUTH(5, hB); hA = cmul(hB, z1); PUTH(6, hA); { f32x2 t_ = cmul(hA, z256); PUTH(11, t_); }
        hB = cmul(hA, z1); PUTH(7, hB);
        g0 = cmul(g0, z256); PUTH(12, g0); g4 = cmul(g4, z256); PUTH(13, g4);
        g0 = cmul(g0, z256); PUTH(14, g0); g4 = cmul(g4, z256); PUTH(15, g4);
#pragma unroll
        for (int i = 16; i < 21; ++i) { g0 = cmul(g0, z256); PUTH(i, g0); }
        h16x8 fa[8], fb[8];
#pragma unroll
        for (int s = 0; s < 8; ++s) { fa[s] = lds_ld<h16x8>(hr + s * 672); fb[s] = lds_ld<h16x8>(lr + s * 1056); }
        SBAR();
#pragma unroll
        for (int s = 0; s < 8; ++s) kd = MFMA32H(fa[s], fb[s], kd);
      }
      STAMP(10);
      flag_wait(ws, WS_UFLAG + (xcd * 32 + r32) * 128, tag);
      u32x4 uv[2];
#pragma unroll
      for (int i = 0; i < 2; ++i) uv[i] = __builtin_amdgcn_raw_buffer_load_b128(wsr, wb * 2048 + i * 1024 + lane * 16, WS_UT + (xcd * 32 + idx) * 8192, AUX_SC1);
      unsigned ga[2][11];
#pragma unroll
      for (int it = 0; it < 2; ++it) {
        const float f0 = (float)(8 * (3 + it * 128 + half * 64 + lane) - 27);
#pragma unroll
        for (int k = 0; k < 11; k += 2) {
          float fa_ = f0 + (float)k, fb_ = f0 + (float)(k + 1);
          if (it == 0 && k < 4) {
            fa_ = __builtin_fmaxf(__builtin_fmaxf(fa_, __builtin_fmaf(fa_, 0.25f, 0.75f)), 0.5f);
            fb_ = __builtin_fmaxf(__builtin_fmaxf(fb_, __builtin_fmaf(fb_, 0.25f, 0.75f)), 0.5f);
          }
          const unsigned pk = pk_bf16(fa_, fb_);
          asm("v_mad_u32_u16 %0, %1, 2, %2" : "=v"(ga[it][k]) : "v"(pk), "s"(T - 2u * (126u << 7)));
          if (k < 10) asm("v_mad_u32_u16 %0, %1, 2, %2 op_sel:[1,0,0,0]" : "=v"(ga[it][k + 1]) : "v"(pk), "s"(T - 2u * (126u << 7)));
        }
      }
      STAMP(11);
      {
        const float ks = cinv * 2.f * dt;
        if (half) {
#pragma unroll
          for (int q = 0; q < 3; ++q) lds_st<f32x4>(tb_ + q * 1024 + lane * 16, f32x4{kd[4 * q] * ks, kd[4 * q + 1] * ks, kd[4 * q + 2] * ks, kd[4 * q + 3] * ks});
        }
        role_sync(C_B, 1);
        if (!half) {
          const unsigned T128 = T + (1024 - 128) * 2;
          const unsigned i1 = T128 + (hh ? 384 + 2 * r32 : 256 + 4 * r32) * 2;
          const unsigned i2 = T128 + (hh ? (r32 == 0 ? 640 : 643) : 512 + r32) * 2, i3 = T128 + (hh ? 643 : 512 + r32) * 2, st3 = hh ? 0 : 64;
          const f32x4 v0 = lds_ld<f32x4>(tb_ - SLOT_B + lane * 16), v1 = lds_ld<f32x4>(tb_ - SLOT_B + 1024 + lane * 16), v2 = lds_ld<f32x4>(tb_ - SLOT_B + 2048 + lane * 16);
#pragma unroll
          for (int e = 0; e < 4; ++e) {
            const float f = __builtin_fmaxf((float)(8 * r32 + 4 * hh + e), 0.75f);
            lds_st<short>((pk_bf16(f, f) & 0xffffu) * 2u + (T - 2u * (126u << 7)), (short)pk_bf16(__builtin_fmaf(kd[e], ks, v0[e]), 0.f));
          }
          lds_st<unsigned>(i1, pk_bf16(__builtin_fmaf(kd[4], ks, v1[0]), __builtin_fmaf(kd[5], ks, v1[1])));
          lds_st<unsigned>(i1 + (hh ? 128 : 4), pk_bf16(__builtin_fmaf(kd[6], ks, v1[2]), __builtin_fmaf(kd[7], ks, v1[3])));
          const unsigned w0 = pk_bf16(__builtin_fmaf(kd[8], ks, v2[0]), __builtin_fmaf(kd[9], ks, v2[1])), w1 = pk_bf16(__builtin_fmaf(kd[10], ks, v2[2]), __builtin_fmaf(kd[11], ks, v2[3]));
          lds_st<short>(i2, (short)w0); lds_st<short>(i3 + st3, (short)(w0 >> 16)); lds_st<short>(i3 + 2 * st3, (short)w1); lds_st<short>(i3 + 3 * st3, (short)(w1 >> 16));
          if (lane == 0) lds_st<short>(T, 0);
        }
        role_sync(C_B, 2);
      }
      STAMP(2);
      const unsigned cb = L_B + c * CH_B;
      {
        s16x2 P[2][6];
#pragma unroll
        for (int it = 0; it < 2; ++it)
#pragma unroll
          for (int k = 0; k < 11; ++k) P[it][k >> 1][k & 1] = lds_ld<short>(ga[it][k]);
        const unsigned ub = cb + 4 * KC_S + half * UC_S;
        const u32x4 z4 = u32x4{0u, 0u, 0u, 0u};
        if (lane < 28) lds_st<u32x4>(ub + 4096 + lane * 16, z4);
        else if (lane < 34) lds_st<u32x4>(cb + (half * 2 + (lane - 28) / 3) * KC_S + ((lane - 28) % 3) * 16, z4);
        else if (lane == 34 && half) lds_st<u32x2>(ub + 4088, u32x2{0u, 0u});
#pragma unroll
        for (int it = 0; it < 2; ++it) {
          unsigned Q[6], A[5];
#pragma unroll
          for (int k = 0; k < 6; ++k) Q[k] = __builtin_bit_cast(unsigned, P[it][k]);
#pragma unroll
          for (int k = 0; k < 5; ++k) A[k] = __builtin_amdgcn_alignbit(Q[k + 1], Q[k], 16);
          const unsigned a = cb + (3 + it * 128 + half * 64 + lane) * 16;
          lds_st<u32x4>(a, u32x4{Q[0], Q[1], Q[2], Q[3]}); lds_st<u32x4>(a + KC_S, u32x4{A[0], A[1], A[2], A[3]});
          lds_st<u32x4>(a + 2 * KC_S, u32x4{Q[1], Q[2], Q[3], Q[4]}); lds_st<u32x4>(a + 3 * KC_S, u32x4{A[1], A[2], A[3], A[4]});
        }
      }
      STAMP(3);
#pragma unroll
      for (int i = 0; i < 2; ++i) {
        const int p = wb * 128 + i * 64 + lane;
        const unsigned a = L_B + (p & 1) * CH_B + 4 * KC_S + (p >> 1) * 16;
        lds_st<u32x4>(a, uv[i]); lds_st<u32x2>(a + UC_S - 8, u32x2{uv[i][0], uv[i][1]}); lds_st<u32x2>(a + UC_S, u32x2{uv[i][2], uv[i][3]});
      }
      role_sync(C_B, 3);
      STAMP(5);
      f32x4 y[8];
#pragma unroll
      for (int i = 0; i < 8; ++i) y[i] = f32x4{0.f, 0.f, 0.f, 0.f};
      {
        const int r16 = lane & 15, kq = lane >> 4;
        const unsigned ka = cb + (r16 & 3) * KC_S + ((r16 >> 2) + kq) * 16 + half * 256;
        const unsigned ua = cb + 4 * KC_S + (1 - (r16 & 1)) * UC_S + (2016 - 32 * (r16 >> 1) + 8 * kq) * 2 - 7 * 512 + half * 256;
        s16x8 af[2][8], bf[2][8];
#define CONV_LD(S, O) _Pragma("unroll") for (int m = 0; m < 8; ++m) { bf[S][7 - m] = lds_ld<s16x8>(ua + (O) + m * 512); af[S][m] = lds_ld<s16x8>(ka + (O) + m * 512); }
#define CONV_MM(S) _Pragma("unroll") for (int n = 7; n >= 0; --n) _Pragma("unroll") for (int m = 0; m + n < 8; ++m) y[m + n] = MFMA16(af[S][m], bf[S][n], y[m + n]);
        CONV_LD(0, 0);
        unsigned o_ = 64;
#pragma unroll 1
        for (int it = 0; it < 2; ++it) {
          SBAR(); CONV_LD(1, o_); SBAR(); CONV_MM(0); SBAR();
          CONV_LD(0, o_ + 64); SBAR(); CONV_MM(1);
          o_ += 128;
        }
      }
      role_sync(C_B, 4);
#pragma unroll
      for (int i = 0; i < 4; ++i) lds_st<f32x4>(L_B + wb * 4096 + i * 1024 + lane * 16, half ? y[i] : y[4 + i]);
      role_sync(C_B, 5);
      {
        const int cc = lane & 15, kq = lane >> 4;
#pragma unroll
        for (int i = 0; i < 4; ++i) {
          const f32x4 v = lds_ld<f32x4>(L_B + (wb ^ 2) * 4096 + i * 1024 + lane * 16) + (half ? y[4 + i] : y[i]);
          const int t64 = 4 * (half * 4 + i) + (cc >> 2);
          __builtin_amdgcn_raw_buffer_store_b64(u32x2{pk_bf16(v[0], v[1]), pk_bf16(v[2], v[3])}, wsr,
                                                ((xcd * 32 + t64) * 64 + idx * 2 + c) * 128 + (32 * ((cc >> 1) & 1) + 8 * kq + 4 * (cc & 1)) * 2, WS_YT, AUX_SC1);
        }
      }
      lds_arrive(C_BFREE);
      drain_vm();
      if (last_of_four(C_BPUB, lane, 0)) { if (lane == 0) flag_store(ws, WS_YFLAG + (xcd * 32 + idx) * 128, tag); }
      STAMP(6);
    } else {
      lds_arrive(C_BFREE);
    }
    {
      unsigned wv[7];
#pragma unroll
      for (int i = 0; i < 7; ++i) { const unsigned p = i * 64 + lane, row = (p * 37450u) >> 19; wv[i] = row * 896 + (p - row * 14) * 16; }
      lds_wait_ge_slow(C_R + 24, 4);
#pragma unroll 1
      for (int i = wb; i < 14; i += 4) {
        const char* sb = wo + i * 32 * 896 + (idx & 3) * 224;
        const unsigned dst = L_RING + i * 7168;
#define WO_PIECE(k) glds<(k) * 1024>(wv[k], sb, dst); asm volatile("s_waitcnt vmcnt(2)" ::: "memory")
        WO_PIECE(0); WO_PIECE(1); WO_PIECE(2); WO_PIECE(3); WO_PIECE(4); WO_PIECE(5); WO_PIECE(6);
      }
      drain_vm();
      lds_arrive(C_WO);
    }
    STAMP(7);
    if (has_b) {
      const int tl = wb * 16 + (lane & 15);
      u32x4 uu[2], ds[2];
#pragma unroll
      for (int i = 0; i < 2; ++i) {
        const int oct = (lane >> 4) + 4 * i;
        uu[i] = *(const GAS u32x4*)(u + (size_t)(idx * 64 * 896 + xcd * 128) + (unsigned)(tl * 896 + oct * 16));
        ds[i] = *(const GAS u32x4*)(Dsk + xcd * 128 + (unsigned)(oct * 16));
      }
      if (wb == 0) flag_wait(ws, WS_YFLAG + (xcd * 32 + r32) * 128, tag);
      role_sync_slow(C_B, 6);
      u32x4 v[2];
#pragma unroll
      for (int i = 0; i < 2; ++i) v[i] = __builtin_amdgcn_raw_buffer_load_b128(wsr, wb * 2048 + i * 1024 + lane * 16, WS_YT + (xcd * 32 + idx) * 8192, AUX_SC1);
#pragma unroll
      for (int i = 0; i < 2; ++i) lds_st<u32x4>(L_YIMG + wb * 2048 + i * 1024 + lane * 16, v[i]);
      role_sync_slow(C_B, 7);
#pragma unroll
      for (int i = 0; i < 2; ++i) {
        const int oct = (lane >> 4) + 4 * i;
        const unsigned a = L_YIMG + (8 * oct + ((lane & 15) >> 2)) * 128 + (wb * 16 + (lane & 3) * 4) * 2;
        const u32x4 yy = __builtin_bit_cast(u32x4, cat8(tr_read(a), tr_read(a + 512)));
        u32x4 r;
#pragma unroll
        for (int k = 0; k < 4; ++k) {
          const unsigned sk = pk_bf16(bf_lo(uu[i][k]) * bf_lo(ds[i][k]), bf_hi(uu[i][k]) * bf_hi(ds[i][k]));
          r[k] = pk_bf16(bf_lo(yy[k]) + bf_lo(sk), bf_hi(yy[k]) + bf_hi(sk));
        }
        *(GAS u32x4*)(out + (size_t)(idx * 64 * 1792 + 896 + xcd * 128) + (unsigned)(tl * 1792 + oct * 16)) = r;
      }
      STAMP(8);
    } else if (b == 7 && wb == 0) {
#pragma unroll 1
      for (int i = 0; i < 4; ++i) flag_wait(ws, WS_QFLAG + (i * 64 + lane) * 128, tag);
      if (lane == 0) flag_store(ws, WS_EPOCH, tag);
    }
    STAMP_FLUSH();
  }
}

extern "C" void kernel_launch(void* const* d_in, const int* in_sizes, int n_in, void* d_out, int out_size, void* d_ws, size_t ws_size,
                              hipStream_t stream) {
  (void)in_sizes; (void)n_in; (void)out_size; (void)ws_size;
  (void)hipFuncSetAttribute((const void*)fused_mha_s4d, hipFuncAttributeMaxDynamicSharedMemorySize, LDS_BYTES);
  fused_mha_s4d<<<256, 512, LDS_BYTES, stream>>>((const char*)d_in[0], (const char*)d_in[1], (const char*)d_in[2], (const char*)d_in[3],
                                                 (const char*)d_in[4], (const char*)d_in[5], (const char*)d_in[6], (const char*)d_in[7],
                                                 (const char*)d_in[8], (const char*)d_in[9], (char*)d_out,
                                                 (char*)d_ws + (WSEND ? ((ws_size - WS_END) & ~(size_t)0xfffff) : 0));
}
```

```cpp
#include <hip/hip_runtime.h>
#include <hip/hip_bf16.h>
#include <stdint.h>

typedef short s16x8 __attribute__((ext_vector_type(8)));
typedef short s16x4 __attribute__((ext_vector_type(4)));
typedef short s16x2 __attribute__((ext_vector_type(2)));
typedef _Float16 h16x8 __attribute__((ext_vector_type(8)));
typedef _Float16 h16x2 __attribute__((ext_vector_type(2)));
typedef __bf16 b16x2 __attribute__((ext_vector_type(2)));
typedef float f32x16 __attribute__((ext_vector_type(16)));
typedef float f32x4 __attribute__((ext_vector_type(4)));
typedef float f32x2 __attribute__((ext_vector_type(2)));
typedef unsigned u32x4 __attribute__((ext_vector_type(4)));
typedef unsigned u32x2 __attribute__((ext_vector_type(2)));
typedef unsigned long long u64;
#define LAS __attribute__((address_space(3)))
#define GAS __attribute__((address_space(1)))
#if defined(__HIP_DEVICE_COMPILE__)
#define NOPK __attribute__((target("no-packed-fp32-ops")))
#else
#define NOPK
#endif
#define DI NOPK __device__ __forceinline__
#define SBAR() __builtin_amdgcn_sched_barrier(0)
#define PIN(x) asm volatile("" : "+v"(x))
#define MFMA32(a, b, c) __builtin_amdgcn_mfma_f32_32x32x16_bf16((a), (b), (c), 0, 0, 0)
#define MFMA32H(a, b, c) __builtin_amdgcn_mfma_f32_32x32x16_f16((a), (b), (c), 0, 0, 0)
#define MFMA16(a, b, c) __builtin_amdgcn_mfma_f32_16x16x32_bf16((a), (b), (c), 0, 0, 0)
#define AUX_SC1 16

#define PREFETCH 1
#define WSEND 0
#define LAZY 1
#define APRIO 3
#define INPF 0
#define BCONV 0
#define STAMPS 0

constexpr unsigned WS_EPOCH = 0;
constexpr unsigned WS_QFLAG = 4096;
constexpr unsigned WS_OFLAG = WS_QFLAG + 256 * 128;
constexpr unsigned WS_UFLAG = WS_OFLAG + 1024 * 128;
constexpr unsigned WS_YFLAG = WS_UFLAG + 256 * 128;
constexpr unsigned WS_QKV = 393216;
constexpr unsigned WS_KV = WS_QKV + 16 * 64 * 2048;
constexpr unsigned WS_O = WS_QKV + 48 * 64 * 2048;
constexpr unsigned WS_UT = WS_O + 64 * 16 * 2048;
constexpr unsigned WS_YT = WS_UT + 256 * 8192;
constexpr unsigned WS_DBG = WS_YT + 224 * 8192;
constexpr unsigned WS_END = WS_DBG + 256 * 8 * 16 * 8;
static_assert(WS_YFLAG + 256 * 128 <= WS_QKV, "ws map");

constexpr unsigned L_RING = 0, SLOT_B = 28672, XCH_B = 16384;
constexpr unsigned L_B = 100352;
constexpr unsigned TAB_W = 14000, TAB_HI = 8448;
constexpr unsigned L_UIMG = 4 * SLOT_B + 2 * TAB_W;
constexpr unsigned L_T = L_B + 56000, T_B = 3088;
constexpr unsigned KC_S = 4160, UC_S = 4640, CH_B = 4 * KC_S + 2 * UC_S;
constexpr unsigned L_YIMG = L_B + 49152;
constexpr unsigned L_CTL = 163584;
constexpr unsigned LDS_BYTES = 163840;
constexpr unsigned C_A = L_CTL, C_B = L_CTL + 4, C_GO = L_CTL + 8, C_APUB = L_CTL + 12, C_BPUB = L_CTL + 16, C_WO = L_CTL + 20, C_BFREE = L_CTL + 24, C_BTAG = L_CTL + 28, C_U = L_CTL + 32, C_Q = L_CTL + 36, C_QR = L_CTL + 40, C_FB = L_CTL + 44, C_KM = L_CTL + 48;
constexpr unsigned C_L = L_CTL + 64, C_R = L_CTL + 96;
constexpr unsigned L_MAX = 162816;
static_assert(L_T + 2 * T_B <= L_MAX, "lds map");

constexpr unsigned SPIN_CAP = 1u << 22;
constexpr float LOG2E = 1.4426950408889634f;
constexpr float QSCALE = 0.18898223650461362f * LOG2E;
constexpr float SAFE_BOUND = 48.0f;

template <int V> struct IC { static constexpr int value = V; };
DI unsigned f2u(float f) { return __builtin_bit_cast(unsigned, f); }
DI float u2f(unsigned u) { return __builtin_bit_cast(float, u); }
DI unsigned umax(unsigned a, unsigned b) { return a > b ? a : b; }
DI float fexp(float x) { return __builtin_amdgcn_exp2f(x * LOG2E); }
DI float bf_lo(unsigned v) { return u2f(v << 16); }
DI float bf_hi(unsigned v) { return u2f(v & 0xffff0000u); }
DI unsigned pk_bf16(float a, float b) { f32x2 v = {a, b}; return __builtin_bit_cast(unsigned, __builtin_convertvector(v, b16x2)); }
DI unsigned pk_f16(float a, float b) { f32x2 v = {a, b}; return __builtin_bit_cast(unsigned, __builtin_convertvector(v, h16x2)); }
DI float bf_round(float a) { return bf_lo(pk_bf16(a, a)); }
template <int OFF> DI void glds(unsigned voff, const char* sbase, unsigned lds) {
  asm volatile("s_add_u32 m0, %2, %3\n\ts_nop 0\n\tglobal_load_lds_dwordx4 %0, %1" :: "v"(voff), "s"(sbase), "s"(lds), "i"(OFF) : "memory", "scc");
}
DI float half_swap_add(float v) {
  auto r = __builtin_amdgcn_permlane32_swap(f2u(v), f2u(v), false, false);
  return u2f(r[0]) + u2f(r[1]);
}
template <int OFF> DI void glds_dev(unsigned voff, const char* sbase, unsigned lds) {
  asm volatile("s_add_u32 m0, %2, %3\n\ts_nop 0\n\tglobal_load_lds_dwordx4 %0, %1 sc1" :: "v"(voff), "s"(sbase), "s"(lds), "i"(OFF) : "memory", "scc");
}
DI float dot2(unsigned a, float c) { return __builtin_amdgcn_fdot2_f32_bf16(__builtin_bit_cast(b16x2, a), __builtin_bit_cast(b16x2, a), c, false); }
DI float frag_norm2(u32x4 f0, u32x4 f1, int hh) {
  float a = dot2(f0[0], 0.f), b = dot2(f0[1], 0.f), c = dot2(f1[2], 0.f);
  a = dot2(f0[2], a); b = dot2(f0[3], b); c = dot2(f1[3], c); a = dot2(f1[0], a); b = dot2(f1[1], b);
  return half_swap_add(a + b + (hh ? 0.f : c));
}
DI s16x4 tr_read(unsigned a) { return __builtin_bit_cast(s16x4, __builtin_amdgcn_ds_read_tr16_b64_v4i16((LAS s16x4*)(uintptr_t)a)); }
DI s16x8 cat8(s16x4 lo, s16x4 hi) { return __builtin_shufflevector(lo, hi, 0, 1, 2, 3, 4, 5, 6, 7); }
template <class T> DI T lds_ld(unsigned a) { return *(LAS T*)(uintptr_t)a; }
template <class T> DI void lds_st(unsigned a, T v) { *(LAS T*)(uintptr_t)a = v; }
#define LDS_POLL(V, A, S, N, T, EXTRA) "s_mov_b32 " N ", 0\n1:\n\t" EXTRA "ds_read_b32 " V ", " A "\n\ts_add_u32 " N ", " N ", 1024\n\t" \
  "s_waitcnt lgkmcnt(0)\n\tv_readfirstlane_b32 " S ", " V "\n\ts_cbranch_scc1 4f\n\ts_cmp_lt_u32 " S ", " T "\n\ts_cbranch_scc1 1b\n4:\n\t"
DI unsigned lds_wait_ge(unsigned a, unsigned target) {
  unsigned v, sv, n;
  asm volatile(LDS_POLL("%0", "%3", "%1", "%2", "%4", "") : "=&v"(v), "=&s"(sv), "=&s"(n) : "v"(a), "s"(target) : "memory", "scc");
  return sv;
}
DI unsigned lds_wait_ge_slow(unsigned a, unsigned target) {
  unsigned v, sv, n;
  asm volatile(LDS_POLL("%0", "%3", "%1", "%2", "%4", "s_sleep 1\n\t") : "=&v"(v), "=&s"(sv), "=&s"(n) : "v"(a), "s"(target) : "memory", "scc");
  return sv;
}
DI void lds_arrive(unsigned a) {
  asm volatile("s_waitcnt lgkmcnt(0)\n\ts_mov_b64 exec, 1\n\tds_add_u32 %0, %1\n\ts_mov_b64 exec, -1" :: "v"(a), "v"(1u) : "memory");
}
DI void lds_arrive_nw(unsigned a) {
  asm volatile("s_mov_b64 exec, 1\n\tds_add_u32 %0, %1\n\ts_mov_b64 exec, -1" :: "v"(a), "v"(1u) : "memory");
}
DI void role_sync(unsigned a, unsigned nth) { lds_arrive(a); lds_wait_ge(a, nth * 4u); }
DI void role_sync_slow(unsigned a, unsigned nth) { lds_arrive(a); lds_wait_ge_slow(a, nth * 4u); }
DI void role_sync_idle(unsigned a, unsigned nth) {
  lds_arrive(a);
  unsigned v, sv, n;
  asm volatile(LDS_POLL("%0", "%3", "%1", "%2", "%4", "s_sleep 6\n\t") : "=&v"(v), "=&s"(sv), "=&s"(n) : "v"(a), "s"(nth * 4u) : "memory", "scc");
}
DI bool last_of_four(unsigned a, int lane, unsigned round) {
  unsigned old = 0;
  if (lane == 0) old = __hip_atomic_fetch_add((LAS unsigned*)(uintptr_t)a, 1u, __ATOMIC_RELAXED, __HIP_MEMORY_SCOPE_WORKGROUP);
  return (unsigned)__builtin_amdgcn_readfirstlane(old) == round * 4u + 3u;
}
DI void st128(u32x4 v, __amdgpu_buffer_rsrc_t r, unsigned voff, unsigned soff) {
  __builtin_amdgcn_raw_buffer_store_b128(v, r, voff, soff, AUX_SC1);
  asm volatile("s_nop 1" :: "v"(v) : "memory");
}
DI void drain_vm() { asm volatile("s_waitcnt vmcnt(0)" ::: "memory"); }
DI void flag_store(char* ws, unsigned off, u64 tag) { __hip_atomic_store((GAS u64*)(ws + off), tag, __ATOMIC_RELAXED, __HIP_MEMORY_SCOPE_AGENT); }
DI void flag_wait(char* ws, unsigned off, u64 tag) {
#pragma unroll 1
  for (unsigned n = 0; n < SPIN_CAP; ++n) {
    u64 v = __hip_atomic_load((GAS u64*)(ws + off), __ATOMIC_RELAXED, __HIP_MEMORY_SCOPE_AGENT);
    if (__builtin_amdgcn_ballot_w64(v != tag) == 0ull) break;
    __builtin_amdgcn_s_sleep(2);
  }
  asm volatile("" ::: "memory");
}
DI u32x4 rec_wait(__amdgpu_buffer_rsrc_t r, unsigned off, u64 tag) {
  u32x4 v;
#pragma unroll 1
  for (unsigned n = 0; n < SPIN_CAP; ++n) {
    v = __builtin_amdgcn_raw_buffer_load_b128(r, off, 0, AUX_SC1);
    if (__builtin_amdgcn_ballot_w64(v[0] != (unsigned)tag || v[2] != (unsigned)(tag >> 32)) == 0ull) break;
    asm volatile("s_sleep 1" ::: "memory");
  }
  asm volatile("" ::: "memory");
  return v;
}
template <int CTRL> DI unsigned dpp_max(unsigned v) { unsigned t = __builtin_amdgcn_update_dpp(0u, v, CTRL, 0xf, 0xf, false); return v > t ? v : t; }
DI unsigned wave_umax(unsigned v) {
  v = dpp_max<0x111>(v); v = dpp_max<0x112>(v); v = dpp_max<0x114>(v); v = dpp_max<0x118>(v);
  v = dpp_max<0x142>(v); v = dpp_max<0x143>(v);
  return __builtin_amdgcn_readlane(v, 63);
}
DI float half_swap_max(float v) {
  auto r = __builtin_amdgcn_permlane32_swap(f2u(v), f2u(v), false, false);
  return __builtin_fmaxf(u2f(r[0]), u2f(r[1]));
}
DI f32x2 cmul(f32x2 a, f32x2 b) { f32x2 r; r.x = __builtin_fmaf(-a.y, b.y, a.x * b.x); r.y = __builtin_fmaf(a.y, b.x, a.x * b.y); return r; }
DI void hmul3(unsigned a, unsigned b1, unsigned b2, unsigned b3, unsigned& r1, unsigned& r2, unsigned& r3) {
  asm("v_pk_mul_f16 %0, %3, %4 op_sel:[0,0] op_sel_hi:[0,1]\n\tv_pk_mul_f16 %1, %3, %5 op_sel:[0,0] op_sel_hi:[0,1]\n\tv_pk_mul_f16 %2, %3, %6 op_sel:[0,0] op_sel_hi:[0,1]\n\t"
      "v_pk_fma_f16 %0, %3, %4, %0 op_sel:[1,1,0] op_sel_hi:[1,0,1] neg_lo:[1,0,0]\n\tv_pk_fma_f16 %1, %3, %5, %1 op_sel:[1,1,0] op_sel_hi:[1,0,1] neg_lo:[1,0,0]\n\t"
      "v_pk_fma_f16 %2, %3, %6, %2 op_sel:[1,1,0] op_sel_hi:[1,0,1] neg_lo:[1,0,0]"
      : "=&v"(r1), "=&v"(r2), "=&v"(r3) : "v"(a), "v"(b1), "v"(b2), "v"(b3));
}
DI f32x2 zpow(float rho, float th, float p) {
  const float e = __builtin_amdgcn_exp2f(rho * p), f = __builtin_amdgcn_fractf(th * p);
  f32x2 r; r.x = e * __builtin_amdgcn_cosf(f); r.y = e * __builtin_amdgcn_sinf(f); return r;
}
#define STAMP(k) do { } while (0)
#define STAMP_FLUSH() do { } while (0)

#define QR(a, n) "v[" #a "+" #n ":" #a "+" #n "+1]"
#define Q_READ(F, XA, S) "ds_read_b128 v[" #F ":" #F "+3], " XA "\n\t" \
  "ds_read_b64_tr_b16 " QR(F, 4) ", %[wa] offset:" #S "*3072\n\tds_read_b64_tr_b16 " QR(F, 6) ", %[wa] offset:" #S "*3072+768\n\t" \
  "ds_read_b64_tr_b16 " QR(F, 8) ", %[wa] offset:" #S "*3072+56\n\tds_read_b64_tr_b16 " QR(F, 10) ", %[wa] offset:" #S "*3072+824\n\t" \
  "ds_read_b64_tr_b16 " QR(F, 12) ", %[wa] offset:" #S "*3072+112\n\tds_read_b64_tr_b16 " QR(F, 14) ", %[wa] offset:" #S "*3072+880\n\t"
#define Q_T(ACC, F, n) "v_mfma_f32_32x32x16_bf16 " ACC ", v[" #F "+" #n ":" #F "+" #n "+3], v[" #F ":" #F "+3], " ACC "\n\t"
#define Q_N(ACC, F, n) "v_mfma_f32_32x32x16_bf16 " ACC ", v[" #F ":" #F "+3], v[" #F "+" #n ":" #F "+" #n "+3], " ACC "\n\t"
#define Q_MMA(F, M0, M1, M2) M0("%[a0]", F, 4) M1("%[a1]", F, 8) M2("%[a2]", F, 12)
#define Q_ARRIVE "s_mov_b64 exec, 1\n\tds_add_u32 %[ca], %[one]\n\ts_mov_b64 exec, -1\n\t"
#define Q_TR(F, n, S, O) "ds_read_b64_tr_b16 " QR(F, n) ", %[wa] offset:" #S "*3072+" #O "\n\t"
#define Q_GROUP(G, F, XA, S, M0, M1, M2) \
  M0("%[a0]", G, 4) "ds_read_b128 v[" #F ":" #F "+3], " XA "\n\t" Q_TR(F, 4, S, 0) Q_TR(F, 6, S, 768) \
  M1("%[a1]", G, 8) Q_TR(F, 8, S, 56) Q_TR(F, 10, S, 824) \
  M2("%[a2]", G, 12) Q_TR(F, 12, S, 112) Q_TR(F, 14, S, 880)
#define QKV_BODY(M0, M1, M2) \
  "2:\n\t" \
  "v_add_u32 %[cl], 4, %[cl]\n\ts_waitcnt lgkmcnt(7)\n\t" \
  Q_GROUP(100, 132, "%[x2]", 2, M0, M1, M2) "ds_read_b32 %[tv], %[cl]\n\t" \
  "s_waitcnt lgkmcnt(8)\n\t" \
  Q_GROUP(116, 148, "%[x3]", 3, M0, M1, M2) \
  "s_cmp_eq_u32 %[tg], 28\n\ts_cbranch_scc1 5f\n\t" \
  "s_cmp_eq_u32 %[tg], 16\n\ts_cselect_b32 %[ts], %[wrap], %[step]\n\ts_add_u32 %[tg], %[tg], 4\n\t" \
  "v_add_u32 %[x0], %[ts], %[x0]\n\tv_add_u32 %[x1], %[ts], %[x1]\n\tv_add_u32 %[x2], %[ts], %[x2]\n\tv_add_u32 %[x3], %[ts], %[x3]\n\tv_add_u32 %[wa], %[ts], %[wa]\n\t" \
  "s_waitcnt lgkmcnt(7)\n\tv_readfirstlane_b32 %[ts], %[tv]\n\ts_cmp_ge_u32 %[ts], 2\n\ts_cbranch_scc1 0f\n\t" \
  LDS_POLL("%[tv]", "%[cl]", "%[ts]", "%[tn]", "2", "") \
  "0:\n\t" \
  Q_GROUP(132, 100, "%[x0]", 0, M0, M1, M2) \
  "s_cmp_eq_u32 %[tg], 8\n\ts_cbranch_scc1 3f\n\ts_waitcnt vmcnt(0)\n3:\n\t" \
  "s_waitcnt lgkmcnt(7)\n\t" Q_ARRIVE \
  Q_GROUP(148, 116, "%[x1]", 1, M0, M1, M2) \
  "v_add_u32 %[ca], 4, %[ca]\n\ts_branch 2b\n" \
  "5:\n\t" \
  "s_waitcnt lgkmcnt(7)\n\t" Q_MMA(132, M0, M1, M2) "s_waitcnt lgkmcnt(0)\n\t" Q_ARRIVE "s_nop 0\n\t" Q_MMA(148, M0, M1, M2)
#define QKV_LOOP() asm volatile( \
  "s_mov_b32 %[tg], 4\n\t" LDS_POLL("%[tv]", "%[cl]", "%[ts]", "%[tn]", "2", "") \
  Q_READ(100, "%[x0]", 0) Q_READ(116, "%[x1]", 1) \
  "s_cmp_gt_u32 %[g], 10\n\ts_cbranch_scc1 7f\n\ts_cmp_eq_u32 %[g], 10\n\ts_cbranch_scc1 6f\n" \
  QKV_BODY(Q_T, Q_T, Q_T) "s_branch 8f\n6:\n" QKV_BODY(Q_T, Q_T, Q_N) "s_branch 8f\n7:\n" QKV_BODY(Q_N, Q_N, Q_N) \
  "8:\n\ts_nop 7\n\ts_nop 7" \
  : [a0] "+v"(acc[0]), [a1] "+v"(acc[1]), [a2] "+v"(acc[2]), [x0] "+v"(xa0), [x1] "+v"(xa1), [x2] "+v"(xa2), [x3] "+v"(xa3), [wa] "+v"(wa), \
    [tv] "=&v"(tv), [ts] "=&s"(ts), [tn] "=&s"(tn), [tg] "=&s"(tg), [ca] "+v"(car), [cl] "+v"(cla) \
  : [one] "v"(1u), [step] "s"(SLOT_B), [wrap] "s"(0u - 3u * SLOT_B), [g] "s"(g) \
  : "memory", "scc", V100_163)
#define V10(p) "v" #p "0", "v" #p "1", "v" #p "2", "v" #p "3", "v" #p "4", "v" #p "5", "v" #p "6", "v" #p "7", "v" #p "8", "v" #p "9"
#define V100_163 V10(10), V10(11), V10(12), V10(13), V10(14), V10(15), "v160", "v161", "v162", "v163"

NOPK __global__ void __launch_bounds__(512, 2)
fused_mha_s4d(const char* __restrict__ x, const char* __restrict__ wqkv, const char* __restrict__ wo, const char* __restrict__ u,
              const char* __restrict__ logAr, const char* __restrict__ Aim, const char* __restrict__ Cre, const char* __restrict__ Cim,
              const char* __restrict__ logdt, const char* __restrict__ Dsk, char* __restrict__ out, char* __restrict__ ws) {
  extern __shared__ __attribute__((aligned(16))) char smem[];
  const int tid = __builtin_amdgcn_workitem_id_x(), lane = tid & 63;
  const int wid = __builtin_amdgcn_readfirstlane(tid >> 6);
  const int b = __builtin_amdgcn_workgroup_id_x(), xcd = b & 7, idx = b >> 3;
  const int r32 = lane & 31, hh = lane >> 5;
  const unsigned L0 = (unsigned)(uintptr_t)smem;
  (void)L0;
  const __amdgpu_buffer_rsrc_t wsr = __builtin_amdgcn_make_buffer_rsrc(ws, 0, WS_END, 0x00020000);
  asm volatile("" :: "s"(x), "s"(wqkv), "s"(wo), "s"(u), "s"(logAr), "s"(Aim), "s"(Cre), "s"(Cim), "s"(logdt), "s"(Dsk), "s"(out), "s"(ws));
  STAMP(0);
  asm volatile(".Lpf_begin:");
#define MAKE_TAG(e) (((u64)(unsigned)__builtin_amdgcn_readfirstlane((unsigned)((e) >> 32)) << 32 | (unsigned)__builtin_amdgcn_readfirstlane((unsigned)(e))) + 0x9E3779B97F4A7C15ull)
  const int rt = (xcd & 3) * 4 + (idx >> 3), g = (xcd >> 2) * 8 + (idx & 7);
  const int woff = (g == 15) ? 8 : 0;
  const bool has_b = xcd < 7;

  if (__builtin_expect(wid >= 4, 0)) {
    const int w = wid - 4;
    __builtin_amdgcn_s_setprio(APRIO);
    const u64 epoch = __hip_atomic_load((GAS u64*)(ws + WS_EPOCH), __ATOMIC_RELAXED, __HIP_MEMORY_SCOPE_AGENT);
    {
      u64 pc; unsigned below, above;
      asm volatile("s_getpc_b64 %0\n.Lpf_here:\n\ts_mov_b32 %1, .Lpf_here-.Lpf_begin\n\ts_mov_b32 %2, .Lpf_end-.Lpf_here" : "=s"(pc), "=s"(below), "=s"(above));
      const unsigned off = ((w * 64 + lane) * 8 + (idx & 7)) * 128;
      if (off < below + above) asm volatile("s_mov_b32 m0, %2\n\ts_nop 0\n\tglobal_load_lds_dword %0, %1" :: "v"(off), "s"(pc - below), "s"(L_MAX + 512) : "memory");
    }
    if (w == 0) lds_st<u32x4>(L_MAX + lane * 16, u32x4{0u, 0u, 0u, 0u});
    asm volatile("s_waitcnt lgkmcnt(0)\n\ts_barrier" ::: "memory");
    if (has_b) {
      lds_wait_ge(C_U, 2);
      const int oct = (lane >> 1) & 7;
      s16x8 v[2];
#pragma unroll
      for (int i = 0; i < 2; ++i) {
        const int ch = 2 * (w * 8 + i * 4 + (lane >> 4)) + (lane & 1);
        const unsigned a = L_UIMG + oct * 1024 + (((ch >> 3) ^ oct) << 4) + (ch & 7) * 2;
#pragma unroll
        for (int e = 0; e < 8; ++e) v[i][e] = lds_ld<short>(a + e * 128);
      }
#pragma unroll
      for (int i = 0; i < 2; ++i)
        st128(__builtin_bit_cast(u32x4, v[i]), wsr, ((xcd * 32 + w * 8 + i * 4 + (lane >> 4)) * 32 + (31 - idx)) * 256 + (lane & 15) * 16, WS_UT);
    }
    STAMP(9);
    f32x16 acc[3];
#pragma unroll
    for (int j = 0; j < 3; ++j) acc[j] = f32x16{};
    {
      const unsigned xa_l = w * 4096 + r32 * 128, xsw = (r32 >> 1) & 7;
      unsigned xa0 = xa_l + (((0 + hh) ^ xsw) << 4), xa1 = xa_l + (((2 + hh) ^ xsw) << 4), xa2 = xa0 ^ 64, xa3 = xa1 ^ 64;
      unsigned wa = XCH_B + (8 * hh + ((lane & 15) >> 2)) * 192 + woff + ((lane >> 4) & 1) * 32 + (lane & 3) * 8;
      unsigned tv, ts, tn, tg, car = C_R, cla = C_L;
      QKV_LOOP();
    }
    STAMP(2);
    const int rt32 = rt * 4 + w;
    const u64 tag = MAKE_TAG(epoch);
    unsigned l16 = lane * 16; PIN(l16);
#pragma unroll
    for (int j = 0; j < 3; ++j) {
      const int sl = 3 * g + j, ty = sl >> 4;
      const unsigned so = ty == 0 ? WS_QKV + (sl * 64 + rt32) * 2048 : WS_KV + ((sl & 15) * 64 + rt32) * 4096 + (ty - 1) * 2048;
      if (ty == 0) {
#pragma unroll
        for (int i = 0; i < 16; ++i) acc[j][i] *= QSCALE;
      }
      u32x4 f0, f1;
#pragma unroll
      for (int i = 0; i < 4; ++i) { f0[i] = pk_bf16(acc[j][2 * i], acc[j][2 * i + 1]); f1[i] = pk_bf16(acc[j][8 + 2 * i], acc[j][9 + 2 * i]); }
      if (ty == 2) {
        const unsigned cst = r32 == 28 ? 0x3F803F80u : 0u;
#pragma unroll
        for (int i = 0; i < 4; ++i) { f0[i] = r32 >= 28 ? cst : f0[i]; f1[i] = r32 >= 28 ? cst : f1[i]; }
      }
      st128(f0, wsr, l16, so);
      if (ty < 2) {
        const unsigned n2 = f2u(frag_norm2(f0, f1, hh));
        if (ty == 1) asm volatile("ds_max_u32 %0, %1" :: "v"(C_KM + j * 4), "v"(n2) : "memory");
        f1[2] = hh ? (ty ? 0x3F803F80u : n2) : f1[2]; f1[3] = hh ? 0x00003F80u : f1[3];
      }
      st128(f1, wsr, l16 + 1024, so);
    }
    STAMP(14);
    const int h = xcd * 2 + (idx >> 4), qt = idx & 15;
    f32x16 o[4];
#pragma unroll
    for (int i = 0; i < 4; ++i) { o[i] = f32x16{}; PIN(o[i]); }
    if (w == 3) lds_wait_ge(C_BTAG, 4);
    drain_vm();
    STAMP(15);
    if (w == 3) {
      lds_wait_ge(C_APUB, 3);
      const u32x4 km = lds_ld<u32x4>(C_KM);
      const u32x4 rec = {(unsigned)tag, ((km[0] + 0xffffu) >> 16) | ((km[1] + 0xffffu) & 0xffff0000u), (unsigned)(tag >> 32), km[2] + 0xffffu};
      if (lane == 0) st128(rec, wsr, (g * 16 + rt) * 128, WS_QFLAG);
    } else {
      lds_arrive_nw(C_APUB);
      if (w == 0) {
        rec_wait(wsr, WS_QFLAG + ((h / 3) * 16 + qt) * 128, tag);
        lds_wait_ge(C_R + 24, 4);
        const char* qs = ws + WS_QKV + (h * 64 + qt * 4) * 2048;
        glds_dev<0>(l16, qs, L_RING); glds_dev<1024>(l16, qs + 1024, L_RING); glds_dev<2048>(l16, qs + 2048, L_RING); glds_dev<3072>(l16, qs + 3072, L_RING);
        glds_dev<4096>(l16, qs + 4096, L_RING); glds_dev<5120>(l16, qs + 5120, L_RING); glds_dev<6144>(l16, qs + 6144, L_RING); glds_dev<7168>(l16, qs + 7168, L_RING);
        drain_vm();
        lds_arrive_nw(C_Q);
      } else if (w == 1) {
        const u32x4 rec = rec_wait(wsr, WS_QFLAG + ((hh ? (32 + h) / 3 : (16 + h) / 3) * 16 + (lane & 15)) * 128, tag);
        const int j = (16 + h) % 3;
        unsigned v = j == 0 ? rec[1] << 16 : ((j == 1 ? rec[1] : rec[3]) & 0xffff0000u);
        v = dpp_max<0x111>(v); v = dpp_max<0x112>(v); v = dpp_max<0x114>(v); v = dpp_max<0x118>(v);
        lds_st<unsigned>(C_GO, __builtin_amdgcn_readlane(v, 15) + 1u);
      }
    }
    STAMP(3);
    s16x8 qf[4][2], kf[4][2], vf[4][2];
    const unsigned kbase = WS_KV + (h * 64 + w * 16) * 4096;
#define LDR(rs_, soff, voff) __builtin_bit_cast(s16x8, __builtin_amdgcn_raw_buffer_load_b128(rs_, l16 + (voff), (soff), AUX_SC1))
#define LDF(soff, voff) LDR(wsr, soff, voff)
#define LDKV(rs_, B, soff) do { SBAR(); kf[B][0] = LDR(rs_, soff, 0); kf[B][1] = LDR(rs_, soff, 1024); vf[B][0] = LDR(rs_, soff, 2048); vf[B][1] = LDR(rs_, soff, 3072); SBAR(); } while (0)
    lds_wait_ge_slow(C_Q, 1);
    STAMP(1);
#pragma unroll
    for (int i = 0; i < 4; ++i) { const unsigned a = L_RING + ((w + 1 + i) & 3) * 2048 + l16; qf[i][0] = lds_ld<s16x8>(a); qf[i][1] = lds_ld<s16x8>(a + 1024); }
    lds_arrive(C_QR);
    const unsigned km2 = lds_wait_ge_slow(C_GO, 1);
    STAMP(4);
    LDKV(wsr, 0, kbase);
    LDKV(wsr, 1, kbase + 4096); LDKV(wsr, 2, kbase + 8192);
    {
      const float kmax2 = u2f(km2) * 1.02f;
      unsigned pad2[4], pad3[4]; bool unsafe = false;
#pragma unroll
      for (int i = 0; i < 4; ++i) {
        const float m = __builtin_amdgcn_sqrtf(u2f(__builtin_bit_cast(u32x4, qf[i][1])[2]) * kmax2);
        unsafe |= hh && !(m <= SAFE_BOUND);
        pad2[i] = pk_bf16(-m, 0.f); pad3[i] = 0u;
      }
      if (__builtin_expect(__builtin_amdgcn_ballot_w64(unsafe) != 0ull, 0)) {
        float mx[4];
#pragma unroll
        for (int i = 0; i < 4; ++i) { mx[i] = -INFINITY; if (hh) { u32x4 t_ = __builtin_bit_cast(u32x4, qf[i][1]); t_[2] = 0u; t_[3] = 0u; qf[i][1] = __builtin_bit_cast(s16x8, t_); } }
#pragma unroll 1
        for (int t = 0; t < 16; ++t) {
          const s16x8 k0 = LDF(kbase + t * 4096, 0), k1 = LDF(kbase + t * 4096, 1024);
#pragma unroll
          for (int i = 0; i < 4; ++i) {
            f32x16 s = MFMA32(k0, qf[i][0], f32x16{}); s = MFMA32(k1, qf[i][1], s);
#pragma unroll
            for (int e = 0; e < 16; ++e) mx[i] = __builtin_fmaxf(mx[i], s[e]);
          }
        }
#pragma unroll
        for (int i = 0; i < 4; ++i) {
          const float v = half_swap_max(mx[i]);
          const unsigned k_ = f2u(v) ^ ((unsigned)((int)f2u(v) >> 31) | 0x80000000u);
          __hip_atomic_fetch_max((LAS unsigned*)(uintptr_t)(L_MAX + (((w + 1 + i) & 3) * 32 + r32) * 4), k_, __ATOMIC_RELAXED, __HIP_MEMORY_SCOPE_WORKGROUP);
        }
        role_sync(C_FB, 1);
#pragma unroll
        for (int i = 0; i < 4; ++i) {
          const unsigned k_ = lds_ld<unsigned>(L_MAX + (((w + 1 + i) & 3) * 32 + r32) * 4);
          const float m = u2f(k_ ^ ((unsigned)((int)~k_ >> 31) | 0x80000000u));
          const float m0 = bf_round(-m), r1 = -m - m0, m1 = bf_round(r1);
          pad2[i] = pk_bf16(m0, m1); pad3[i] = pk_bf16(r1 - m1, 0.f);
        }
      }
#pragma unroll
      for (int i = 0; i < 4; ++i) {
        u32x4 t_ = __builtin_bit_cast(u32x4, qf[i][1]);
        t_[2] = hh ? pad2[i] : t_[2]; t_[3] = hh ? pad3[i] : t_[3]; qf[i][1] = __builtin_bit_cast(s16x8, t_);
      }
    }
    f32x16 sA, sB; u32x4 p0, p1 = u32x4{0u, 0u, 0u, 0u};
    sA = MFMA32(kf[0][0], qf[0][0], f32x16{}); sA = MFMA32(kf[0][1], qf[0][1], sA);
    __builtin_amdgcn_s_setprio(1);
    STAMP(5);
    unsigned redw = L_B + l16; PIN(redw);
#define EX(S, i) S[i] = __builtin_amdgcn_exp2f(S[i])
#define PK(S, i) pk_bf16(S[i], S[(i) + 1])
#define BF(P) __builtin_bit_cast(s16x8, P)
#define STEP(C, N, KN, QN, VP, OP, VC, OC) do { \
      N = MFMA32(KN[0], QN[0], f32x16{}); SBAR(); EX(C, 0); EX(C, 1); EX(C, 2); EX(C, 3); EX(C, 4); SBAR(); \
      OP = MFMA32(VP[1], BF(p1), OP); SBAR(); EX(C, 5); EX(C, 6); EX(C, 7); p0[0] = PK(C, 0); p0[1] = PK(C, 2); p0[2] = PK(C, 4); p0[3] = PK(C, 6); PIN(p0); SBAR(); \
      N = MFMA32(KN[1], QN[1], N); SBAR(); EX(C, 8); EX(C, 9); EX(C, 10); EX(C, 11); EX(C, 12); SBAR(); \
      OC = MFMA32(VC[0], BF(p0), OC); SBAR(); EX(C, 13); EX(C, 14); EX(C, 15); p1[0] = PK(C, 8); p1[1] = PK(C, 10); p1[2] = PK(C, 12); p1[3] = PK(C, 14); PIN(p1); SBAR(); \
    } while (0)
#define RED_WRITE(i) do { const unsigned a_ = redw + (((w + 1 + (i)) & 3) * 3 + (2 - (i))) * 4096; \
      _Pragma("unroll") for (int q_ = 0; q_ < 4; ++q_) lds_st<f32x4>(a_ + q_ * 1024, f32x4{o[i][4 * q_], o[i][4 * q_ + 1], o[i][4 * q_ + 2], o[i][4 * q_ + 3]}); } while (0)
#define TILE(T, TP, TN, RS, LAST) do { \
      STEP(sA, sB, kf[T], qf[1], vf[TP], o[3], vf[T], o[0]); \
      if (!LAST) LDKV(RS, TP, kcur + (T + 3) * 4096); \
      STEP(sB, sA, kf[T], qf[2], vf[T], o[0], vf[T], o[1]); \
      if (LAST) RED_WRITE(0); \
      STEP(sA, sB, kf[T], qf[3], vf[T], o[1], vf[T], o[2]); \
      if (LAST) RED_WRITE(1); \
      STEP(sB, sA, kf[TN], qf[0], vf[T], o[2], vf[T], o[3]); \
      if (LAST) RED_WRITE(2); \
    } while (0)
    vf[3][1] = s16x8{};
    unsigned kcur = kbase;
#pragma unroll 1
    for (int tb = 0;; tb += 4) {
      const __amdgpu_buffer_rsrc_t wsr_next = __builtin_amdgcn_make_buffer_rsrc(ws, 0, tb < 12 ? WS_END : 0u, 0x00020000);
      TILE(0, 3, 1, wsr, false);
      TILE(1, 0, 2, wsr_next, false);
      TILE(2, 1, 3, wsr_next, false);
      if (tb == 12) break;
      TILE(3, 2, 0, wsr, false);
      kcur += 16384;
      if (STAMPS) { if (tb == 0) STAMP(11); else if (tb == 4) { } else { } }
    }
    lds_wait_ge(C_BFREE, 4);
    TILE(3, 2, 0, wsr, true);
    o[3] = MFMA32(vf[3][1], BF(p1), o[3]);
    STAMP(6);
    role_sync(C_A, 1);
    {
      f32x4 pv[12];
#pragma unroll
      for (int k = 0; k < 12; ++k) pv[k] = lds_ld<f32x4>(redw + w * 12288 + k * 1024);
      SBAR();
      f32x16 a = o[3];
#pragma unroll
      for (int k = 0; k < 12; ++k) { const int q = k & 3; a[4 * q] += pv[k][0]; a[4 * q + 1] += pv[k][1]; a[4 * q + 2] += pv[k][2]; a[4 * q + 3] += pv[k][3]; }
      auto lr = __builtin_amdgcn_permlane32_swap(f2u(a[12]), f2u(a[12]), false, false);
      const float inv = __builtin_amdgcn_rcpf(u2f(lr[1]));
      u32x4 f0, f1;
#pragma unroll
      for (int i = 0; i < 4; ++i) { f0[i] = pk_bf16(a[2 * i] * inv, a[2 * i + 1] * inv); f1[i] = pk_bf16(a[8 + 2 * i] * inv, a[9 + 2 * i] * inv); }
      if (hh) { f1[2] = 0u; f1[3] = 0u; }
      const unsigned so = WS_O + ((qt * 4 + w) * 16 + h) * 2048;
      st128(f0, wsr, l16, so);
      st128(f1, wsr, l16 + 1024, so);
      lds_arrive(C_A);
      drain_vm();
      if (lane == 0) flag_store(ws, WS_OFLAG + ((qt * 4 + w) * 16 + h) * 128, tag);
    }
    STAMP(7);
    const int rs = xcd * 8 + (idx >> 2), cg = idx & 3, n0 = w < 3 ? 32 * w : 80;
    lds_wait_ge(C_WO, 4);
    s16x8 wf[16][2];
    {
      unsigned wa = L_RING + (4 * hh + ((lane & 15) >> 2)) * 224 + n0 * 2 + ((lane >> 4) & 1) * 32 + (lane & 3) * 8;
      unsigned wb2 = wa + (hh ? 16 * 224 : 24 * 224), wa1 = wa + 8 * 28 * 224, wb3 = wb2 + 8 * 28 * 224;
      PIN(wa); PIN(wb2); PIN(wa1); PIN(wb3);
#pragma unroll
      for (int hd = 0; hd < 16; ++hd) {
        const unsigned a_ = (hd < 8 ? wa : wa1) + (hd & 7) * 28 * 224, b_ = (hd < 8 ? wb2 : wb3) + (hd & 7) * 28 * 224;
        wf[hd][0] = cat8(tr_read(a_), tr_read(a_ + 8 * 224));
        wf[hd][1] = cat8(tr_read(a_ + 16 * 224), tr_read(b_));
      }
    }
    lds_wait_ge(C_A, 2 * 4);
    flag_wait(ws, WS_OFLAG + (rs * 16 + w * 4 + (lane & 3)) * 128, tag);
    STAMP(8);
    {
      const char* os = ws + WS_O + (rs * 16 + w * 4) * 2048;
      const unsigned od = L_B + w * 8192;
      glds_dev<0>(l16, os, od); glds_dev<1024>(l16, os + 1024, od); glds_dev<2048>(l16, os + 2048, od); glds_dev<3072>(l16, os + 3072, od);
      glds_dev<4096>(l16, os + 4096, od); glds_dev<5120>(l16, os + 5120, od); glds_dev<6144>(l16, os + 6144, od); glds_dev<7168>(l16, os + 7168, od);
      drain_vm();
    }
    role_sync(C_A, 3);
    STAMP(13);
    f32x16 d = f32x16{};
    {
      s16x8 ob[8];
#pragma unroll
      for (int k = 0; k < 8; ++k) ob[k] = lds_ld<s16x8>(redw + k * 1024);
      SBAR();
#pragma unroll
      for (int k = 0; k < 32; ++k) {
        d = MFMA32(wf[k >> 1][k & 1], ob[k & 7], d);
        if (k + 8 < 32) ob[k & 7] = lds_ld<s16x8>(redw + (k + 8) * 1024);
        SBAR();
      }
    }
    {
      char* op = out + (size_t)(rs * 32 * 1792 + (cg * 112 + n0) * 2) + (unsigned)(r32 * 1792 + hh * 16);
#pragma unroll
      for (int k = 0; k < 2; ++k) {
        const unsigned a0 = pk_bf16(d[8 * k], d[8 * k + 1]), a1 = pk_bf16(d[8 * k + 2], d[8 * k + 3]);
        const unsigned b0 = pk_bf16(d[8 * k + 4], d[8 * k + 5]), b1 = pk_bf16(d[8 * k + 6], d[8 * k + 7]);
        auto s0 = __builtin_amdgcn_permlane32_swap(a0, b0, false, false);
        auto s1 = __builtin_amdgcn_permlane32_swap(a1, b1, false, false);
        *(GAS u32x4*)(op + k * 32) = u32x4{s0[0], s1[0], s0[1], s1[1]};
      }
    }
    STAMP(10);
    STAMP_FLUSH();
    asm volatile(".Lpf_end:");
  } else {
    const int wb = wid ^ 2;
    const int c = wb & 1, half = wb >> 1;
    const int d = (has_b ? xcd * 64 : 0) + idx * 2 + c;
    const unsigned po = d * 512 + lane * 4;
    unsigned pdt, p0[4], p1[4];
    u64 tag;
#define LOAD_P(P, O) do { P[0] = *(const GAS unsigned*)(logAr + (O) + po); P[1] = *(const GAS unsigned*)(Aim + (O) + po); \
      P[2] = *(const GAS unsigned*)(Cre + (O) + po); P[3] = *(const GAS unsigned*)(Cim + (O) + po); } while (0)
    const unsigned T = L_T + c * T_B;
    unsigned ga[2][11], wv[7];
    auto gather_addr = [&]() __attribute__((always_inline)) NOPK {
#pragma unroll
      for (int i = 0; i < 7; ++i) {
        const unsigned p = i * 64 + lane, row = (p * 37450u) >> 19; wv[i] = row * 896 + (p - row * 14) * 16; PIN(wv[i]);
      }
#pragma unroll
      for (int it = 0; it < 2; ++it) {
        const float f0 = (float)(8 * (3 + it * 128 + half * 64 + lane) - 27);
#pragma unroll
        for (int k = 0; k < 11; k += 2) {
          float fa_ = f0 + (float)k, fb_ = f0 + (float)(k + 1);
          if (it == 0 && k < 4) {
            fa_ = __builtin_fmaxf(__builtin_fmaxf(fa_, __builtin_fmaf(fa_, 0.25f, 0.75f)), 0.5f);
            fb_ = __builtin_fmaxf(__builtin_fmaxf(fb_, __builtin_fmaf(fb_, 0.25f, 0.75f)), 0.5f);
          }
          const unsigned pk = pk_bf16(fa_, fb_);
          asm volatile("v_mad_u32_u16 %0, %1, 2, %2" : "=v"(ga[it][k]) : "v"(pk), "s"(T - 2u * (126u << 7)));
          if (k < 10) asm volatile("v_mad_u32_u16 %0, %1, 2, %2 op_sel:[1,0,0,0]" : "=v"(ga[it][k + 1]) : "v"(pk), "s"(T - 2u * (126u << 7)));
        }
      }
    };
    if (half) {
      unsigned xo[4], wo_[6];
#pragma unroll
      for (int q = 0; q < 4; ++q) xo[q] = (q * 8 + (lane >> 3)) * 896 + (((lane & 7) ^ ((q & 1) * 4 + (lane >> 4))) << 4);
      const char* xb = x + (size_t)(rt * 128 + c * 64) * 896;
      const char* wq = wqkv + g * 168 - woff;
      u64 epoch;
      asm volatile("global_load_dwordx2 %0, %1, %2 sc1" : "=v"(epoch) : "v"(0u), "s"(ws + WS_EPOCH) : "memory");
#define X_PIECES(B, D) do { glds<0>(xo[0], B, D); glds<1024>(xo[1], B, D); glds<2048>(xo[2], B, D); glds<3072>(xo[3], B, D); \
        glds<4096>(xo[0], (B) + 32 * 896, D); glds<5120>(xo[1], (B) + 32 * 896, D); glds<6144>(xo[2], (B) + 32 * 896, D); glds<7168>(xo[3], (B) + 32 * 896, D); } while (0)
#define W_PIECES(B, D) do { glds<0>(wo_[0], B, D); glds<1024>(wo_[1], B, D); glds<2048>(wo_[2], B, D); glds<3072>(wo_[3], B, D); glds<4096>(wo_[4], B, D); glds<5120>(wo_[5], B, D); } while (0)
      {
        const char* up = u + (size_t)(idx * 64 + 56 - 32 * c) * 896 + (has_b ? xcd * 128 : 0);
        const unsigned uo = (7 - (lane >> 3)) * 896, k4 = (lane & 7) ^ (4 * c);
        glds<0>(uo + (k4 << 4), up, L_UIMG + c * 4096); glds<1024>(uo + ((k4 ^ 1) << 4), up - 8 * 896, L_UIMG + c * 4096);
        glds<2048>(uo + ((k4 ^ 2) << 4), up - 16 * 896, L_UIMG + c * 4096); glds<3072>(uo + ((k4 ^ 3) << 4), up - 24 * 896, L_UIMG + c * 4096);
      }
      X_PIECES(xb, L_RING + c * 8192);
#pragma unroll
      for (int i = 0; i < 6; ++i) {
        const unsigned p = (6 * c + i) * 64 + lane, row = (p * 43691u) >> 19; unsigned e = p - row * 12; e = e > 10 ? 10 : e;
        wo_[i] = row * 2688 + e * 16;
      }
      W_PIECES(wq, L_RING + XCH_B + c * 6144);
      asm volatile("s_barrier" ::: "memory");
      asm volatile("s_waitcnt vmcnt(14)" : "+v"(epoch) :: "memory");
      lds_arrive_nw(C_U);
      tag = MAKE_TAG(epoch); asm volatile("" :: "s"(tag)); lds_arrive_nw(C_BTAG);
      X_PIECES(xb + 128, L_RING + c * 8192 + SLOT_B);
      W_PIECES(wq + 64 * 2688, L_RING + XCH_B + c * 6144 + SLOT_B);
      asm volatile("s_waitcnt vmcnt(14)" ::: "memory");
      lds_arrive_nw(C_L);
      STAMP(1);
#pragma unroll 1
      for (int n = 2; n < 7; ++n) {
        if (n >= 4) {
          lds_wait_ge(C_R + 4 * (n - 4), 4);
          if (n == 5 && c == 0 && has_b && lane == 0) flag_store(ws, WS_UFLAG + (xcd * 32 + idx) * 128, tag);
        }
        const unsigned slot = (n & 3) * SLOT_B;
        X_PIECES(xb + n * 128, L_RING + c * 8192 + slot);
        if (n > 2) { asm volatile("s_waitcnt vmcnt(22)" ::: "memory"); lds_arrive_nw(C_L + 4 * (n - 2)); }
        W_PIECES(wq + n * 64 * 2688, L_RING + XCH_B + c * 6144 + slot);
      }
      asm volatile("s_waitcnt vmcnt(14)" ::: "memory");
      lds_arrive_nw(C_L + 20);
      STAMP(4);
      pdt = *(const GAS unsigned short*)(logdt + d * 2);
      LOAD_P(p0, 256);
      gather_addr();
      drain_vm();
      lds_arrive_nw(C_L + 24);
      STAMP(12);
#pragma unroll
      for (int i = 0; i < 4; ++i) { p0[i] <<= 16; p1[i] = 0u; }
      lds_wait_ge(C_R + 12, 4);
    } else {
      pdt = *(const GAS unsigned short*)(logdt + d * 2);
      LOAD_P(p0, 0); LOAD_P(p1, 256);
      const u64 epoch = __hip_atomic_load((GAS u64*)(ws + WS_EPOCH), __ATOMIC_RELAXED, __HIP_MEMORY_SCOPE_AGENT);
      asm volatile("s_barrier" ::: "memory");
      gather_addr();
      asm volatile("" : "+v"(pdt), "+v"(p0[0]), "+v"(p1[0]), "+v"(p1[1]), "+v"(p1[2]), "+v"(p1[3]));
      tag = MAKE_TAG(epoch); asm volatile("" :: "s"(tag)); lds_arrive_nw(C_BTAG);
#pragma unroll
      for (int i = 0; i < 4; ++i) p1[i] &= 0xffff0000u;
    }
    __builtin_amdgcn_s_setprio(1);
    STAMP(9);
    if (has_b) {
      const float dt = bf_round(fexp(bf_lo(pdt)));
      const unsigned cm = umax(umax(umax(p0[2] << 16 & 0x7fff0000u, p0[2] & 0x7fff0000u), umax(p0[3] << 16 & 0x7fff0000u, p0[3] & 0x7fff0000u)),
                               umax(p1[2] & 0x7fff0000u, p1[3] & 0x7fff0000u));
      unsigned eb = wave_umax(cm) >> 23; eb = eb < 1u ? 1u : (eb > 253u ? 253u : eb);
      const float csc = u2f((254u - eb) << 23), cinv = u2f(eb << 23);
      const unsigned tb_ = (half ? 3 * SLOT_B : 4 * SLOT_B) + c * TAB_W;
      const unsigned lw = tb_ + (lane >> 3) * 1056 + ((lane >> 2) & 1) * 528 + (lane & 3) * 4;
      const unsigned hw = tb_ + TAB_HI + (lane >> 3) * 672 + ((lane >> 2) & 1) * 336 + (lane & 3) * 4;
      const unsigned lr = tb_ + hh * 528 + r32 * 16, hr = tb_ + TAB_HI + hh * 336 + r32 * 16;
      f32x16 kd = f32x16{};
      u32x4 uv[2] = {};
#pragma unroll 1
      for (int r = half ? 2 : 0; r < 3; ++r) {
        if (r == 2) {
          flag_wait(ws, WS_UFLAG + (xcd * 32 + r32) * 128, tag);
#pragma unroll
          for (int i = 0; i < 2; ++i) uv[i] = __builtin_amdgcn_raw_buffer_load_b128(wsr, wb * 2048 + i * 1024 + lane * 16, WS_UT + (xcd * 32 + idx) * 8192, AUX_SC1);
        }
        const float rho = -bf_round(fexp(bf_hi(p0[0]))) * dt * LOG2E, th = bf_hi(p0[1]) * dt * 0.15915494309189535f;
        f32x2 hA, hB; hA.x = bf_hi(p0[2]) * csc; hA.y = bf_hi(p0[3]) * -csc;
#pragma unroll
        for (int i = 0; i < 4; ++i) p0[i] = r == 0 ? p0[i] << 16 : p1[i];
        const f32x2 z8 = zpow(rho, th, 8.f), z32 = zpow(rho, th, 32.f), z1 = zpow(rho, -th, 1.f), z256 = zpow(rho, -th, 256.f);
        const f32x2 z16 = cmul(z8, z8), z24 = cmul(z8, z16);
        const unsigned q1 = pk_f16(z8.x, z8.y), q2 = pk_f16(z16.x, z16.y), q3 = pk_f16(z24.x, z24.y);
        lds_st<unsigned>(lw, 0x00003C00u); lds_st<unsigned>(lw + 16, q1); lds_st<unsigned>(lw + 32, q2); lds_st<unsigned>(lw + 48, q3);
        f32x2 e = z32;
#pragma unroll
        for (int a = 1; a < 8; ++a) {
          const unsigned p_ = pk_f16(e.x, e.y); unsigned m1, m2, m3;
          if (a < 7) e = cmul(e, z32);
          hmul3(p_, q1, q2, q3, m1, m2, m3);
          lds_st<unsigned>(lw + 64 * a, p_); lds_st<unsigned>(lw + 64 * a + 16, m1); lds_st<unsigned>(lw + 64 * a + 32, m2); lds_st<unsigned>(lw + 64 * a + 48, m3);
        }
#define PUTH(i, v) lds_st<unsigned>(hw + (i) * 16, pk_f16((v).x, (v).y))
        PUTH(0, hA); f32x2 g0 = cmul(hA, z256); PUTH(8, g0);
        hB = cmul(hA, z1); PUTH(1, hB); hA = cmul(hB, z1); PUTH(2, hA); { f32x2 t_ = cmul(hA, z256); PUTH(9, t_); }
        hB = cmul(hA, z1); PUTH(3, hB); hA = cmul(hB, z1); PUTH(4, hA); f32x2 g4 = cmul(hA, z256); PUTH(10, g4);
        hB = cmul(hA, z1); PUTH(5, hB); hA = cmul(hB, z1); PUTH(6, hA); { f32x2 t_ = cmul(hA, z256); PUTH(11, t_); }
        hB = cmul(hA, z1); PUTH(7, hB);
        g0 = cmul(g0, z256); PUTH(12, g0); g4 = cmul(g4, z256); PUTH(13, g4);
        g0 = cmul(g0, z256); PUTH(14, g0); g4 = cmul(g4, z256); PUTH(15, g4);
#pragma unroll
        for (int i = 16; i < 21; ++i) { g0 = cmul(g0, z256); PUTH(i, g0); }
        h16x8 fa[8], fb[8];
#pragma unroll
        for (int s = 0; s < 8; ++s) { fa[s] = lds_ld<h16x8>(hr + s * 672); fb[s] = lds_ld<h16x8>(lr + s * 1056); }
        SBAR();
#pragma unroll
        for (int s = 0; s < 8; ++s) kd = MFMA32H(fa[s], fb[s], kd);
      }
      STAMP(10);
      STAMP(11);
      {
        const float ks = cinv * 2.f * dt;
        if (half) {
#pragma unroll
          for (int q = 0; q < 3; ++q) lds_st<f32x4>(tb_ + q * 1024 + lane * 16, f32x4{kd[4 * q] * ks, kd[4 * q + 1] * ks, kd[4 * q + 2] * ks, kd[4 * q + 3] * ks});
        }
        role_sync(C_B, 1);
        if (!half) {
          const unsigned T128 = T + (1024 - 128) * 2;
          const unsigned i1 = T128 + (hh ? 384 + 2 * r32 : 256 + 4 * r32) * 2;
          const unsigned i2 = T128 + (hh ? (r32 == 0 ? 640 : 643) : 512 + r32) * 2, i3 = T128 + (hh ? 643 : 512 + r32) * 2, st3 = hh ? 0 : 64;
          const f32x4 v0 = lds_ld<f32x4>(tb_ - SLOT_B + lane * 16), v1 = lds_ld<f32x4>(tb_ - SLOT_B + 1024 + lane * 16), v2 = lds_ld<f32x4>(tb_ - SLOT_B + 2048 + lane * 16);
#pragma unroll
          for (int e = 0; e < 4; ++e) {
            const float f = __builtin_fmaxf((float)(8 * r32 + 4 * hh + e), 0.75f);
            lds_st<short>((pk_bf16(f, f) & 0xffffu) * 2u + (T - 2u * (126u << 7)), (short)pk_bf16(__builtin_fmaf(kd[e], ks, v0[e]), 0.f));
          }
          lds_st<unsigned>(i1, pk_bf16(__builtin_fmaf(kd[4], ks, v1[0]), __builtin_fmaf(kd[5], ks, v1[1])));
          lds_st<unsigned>(i1 + (hh ? 128 : 4), pk_bf16(__builtin_fmaf(kd[6], ks, v1[2]), __builtin_fmaf(kd[7], ks, v1[3])));
          const unsigned w0 = pk_bf16(__builtin_fmaf(kd[8], ks, v2[0]), __builtin_fmaf(kd[9], ks, v2[1])), w1 = pk_bf16(__builtin_fmaf(kd[10], ks, v2[2]), __builtin_fmaf(kd[11], ks, v2[3]));
          lds_st<short>(i2, (short)w0); lds_st<short>(i3 + st3, (short)(w0 >> 16)); lds_st<short>(i3 + 2 * st3, (short)w1); lds_st<short>(i3 + 3 * st3, (short)(w1 >> 16));
          if (lane == 0) lds_st<short>(T, 0);
        }
        role_sync(C_B, 2);
      }
      STAMP(2);
      const unsigned cb = L_B + c * CH_B;
      {
        s16x2 P[2][6];
#pragma unroll
        for (int it = 0; it < 2; ++it)
#pragma unroll
          for (int k = 0; k < 11; ++k) P[it][k >> 1][k & 1] = lds_ld<short>(ga[it][k]);
        const unsigned ub = cb + 4 * KC_S + half * UC_S;
        const u32x4 z4 = u32x4{0u, 0u, 0u, 0u};
        if (lane < 28) lds_st<u32x4>(ub + 4096 + lane * 16, z4);
        else if (lane < 34) lds_st<u32x4>(cb + (half * 2 + (lane - 28) / 3) * KC_S + ((lane - 28) % 3) * 16, z4);
        else if (lane == 34 && half) lds_st<u32x2>(ub + 4088, u32x2{0u, 0u});
#pragma unroll
        for (int it = 0; it < 2; ++it) {
          unsigned Q[6], A[5];
#pragma unroll
          for (int k = 0; k < 6; ++k) Q[k] = __builtin_bit_cast(unsigned, P[it][k]);
#pragma unroll
          for (int k = 0; k < 5; ++k) A[k] = __builtin_amdgcn_alignbit(Q[k + 1], Q[k], 16);
          const unsigned a = cb + (3 + it * 128 + half * 64 + lane) * 16;
          lds_st<u32x4>(a, u32x4{Q[0], Q[1], Q[2], Q[3]}); lds_st<u32x4>(a + KC_S, u32x4{A[0], A[1], A[2], A[3]});
          lds_st<u32x4>(a + 2 * KC_S, u32x4{Q[1], Q[2], Q[3], Q[4]}); lds_st<u32x4>(a + 3 * KC_S, u32x4{A[1], A[2], A[3], A[4]});
        }
      }
      STAMP(3);
#pragma unroll
      for (int i = 0; i < 2; ++i) {
        const int p = wb * 128 + i * 64 + lane;
        const unsigned a = L_B + (p & 1) * CH_B + 4 * KC_S + (p >> 1) * 16;
        lds_st<u32x4>(a, uv[i]); lds_st<u32x2>(a + UC_S - 8, u32x2{uv[i][0], uv[i][1]}); lds_st<u32x2>(a + UC_S, u32x2{uv[i][2], uv[i][3]});
      }
      role_sync(C_B, 3);
      __builtin_amdgcn_s_setprio(BCONV);
      STAMP(5);
      f32x4 y[8];
#pragma unroll
      for (int i = 0; i < 8; ++i) y[i] = f32x4{0.f, 0.f, 0.f, 0.f};
      {
        const int r16 = lane & 15, kq = lane >> 4;
        unsigned ka = cb + (r16 & 3) * KC_S + ((r16 >> 2) + kq) * 16 + half * 256;
        unsigned ua = cb + 4 * KC_S + (1 - (r16 & 1)) * UC_S + (2016 - 32 * (r16 >> 1) + 8 * kq) * 2 - 7 * 512 + half * 256;
        PIN(ka); PIN(ua);
        s16x8 af[2][8], bf[2][8];
#pragma unroll
        for (int m = 0; m < 8; ++m) { bf[0][7 - m] = lds_ld<s16x8>(ua + m * 512); af[0][m] = lds_ld<s16x8>(ka + m * 512); }
        auto conv_class = [&](auto sc, auto oc) __attribute__((always_inline)) NOPK {
          constexpr int S = decltype(sc)::value, O = decltype(oc)::value;
          int k = 0;
#pragma unroll
          for (int n = 7; n >= 0; --n)
#pragma unroll
            for (int m = 0; m + n < 8; ++m) {
              y[m + n] = MFMA16(af[S][m], bf[S][n], y[m + n]);
              if (k >= 4 && k < 36 && (k & 1) == 0) {
                const int q = (k - 4) >> 2;
                if (k & 2) af[1 - S][q] = lds_ld<s16x8>(ka + O + q * 512); else bf[1 - S][7 - q] = lds_ld<s16x8>(ua + O + q * 512);
              }
              SBAR();
              ++k;
            }
        };
#pragma unroll 1
        for (int it = 0; it < 2; ++it) {
          conv_class(IC<0>{}, IC<64>{});
          conv_class(IC<1>{}, IC<128>{});
          ka += 128; ua += 128;
        }
      }
      PIN(y[7]); STAMP(13);
      role_sync_slow(C_B, 4);
      __builtin_amdgcn_s_setprio(2);
#pragma unroll
      for (int i = 0; i < 4; ++i) lds_st<f32x4>(L_B + wb * 4096 + i * 1024 + lane * 16, half ? y[i] : y[4 + i]);
      role_sync_slow(C_B, 5);
      STAMP(14);
      {
        const int cc = lane & 15, kq = lane >> 4;
#pragma unroll
        for (int i = 0; i < 4; ++i) {
          const f32x4 v = lds_ld<f32x4>(L_B + (wb ^ 2) * 4096 + i * 1024 + lane * 16) + (half ? y[4 + i] : y[i]);
          const int t64 = 4 * (half * 4 + i) + (cc >> 2);
          __builtin_amdgcn_raw_buffer_store_b64(u32x2{pk_bf16(v[0], v[1]), pk_bf16(v[2], v[3])}, wsr,
                                                ((xcd * 32 + t64) * 64 + idx * 2 + c) * 128 + (32 * ((cc >> 1) & 1) + 8 * kq + 4 * (cc & 1)) * 2, WS_YT, AUX_SC1);
        }
      }
      lds_arrive(C_BFREE);
      __builtin_amdgcn_s_setprio(0);
      STAMP(15);
    } else {
      lds_arrive(C_BFREE);
      __builtin_amdgcn_s_setprio(0);
    }
    lds_wait_ge_slow(C_QR, 4);
#pragma unroll 1
    for (int i = wb; i < 14; i += 4) {
      const char* sb = wo + i * 32 * 896 + (idx & 3) * 224;
      const unsigned dst = L_RING + i * 7168;
#define WO_PIECE(k) glds<(k) * 1024>(wv[k], sb, dst); asm volatile("s_waitcnt vmcnt(2)" ::: "memory")
      WO_PIECE(0); WO_PIECE(1); WO_PIECE(2);
      if (i < 4 && has_b) {
        if (last_of_four(C_BPUB, lane, 0)) { if (lane == 0) flag_store(ws, WS_YFLAG + (xcd * 32 + idx) * 128, tag); }
        STAMP(6);
      }
      WO_PIECE(3); WO_PIECE(4); WO_PIECE(5); WO_PIECE(6);
    }
    drain_vm();
    lds_arrive(C_WO);
    STAMP(7);
    if (has_b) {
      const int tl = wb * 16 + (lane & 15);
      u32x4 uu[2], ds[2];
#pragma unroll
      for (int i = 0; i < 2; ++i) {
        const int oct = (lane >> 4) + 4 * i;
        uu[i] = *(const GAS u32x4*)(u + (size_t)(idx * 64 * 896 + xcd * 128) + (unsigned)(tl * 896 + oct * 16));
        ds[i] = *(const GAS u32x4*)(Dsk + xcd * 128 + (unsigned)(oct * 16));
      }
      if (wb == 0) flag_wait(ws, WS_YFLAG + (xcd * 32 + r32) * 128, tag);
      role_sync_idle(C_B, 6);
      u32x4 v[2];
#pragma unroll
      for (int i = 0; i < 2; ++i) v[i] = __builtin_amdgcn_raw_buffer_load_b128(wsr, wb * 2048 + i * 1024 + lane * 16, WS_YT + (xcd * 32 + idx) * 8192, AUX_SC1);
#pragma unroll
      for (int i = 0; i < 2; ++i) lds_st<u32x4>(L_YIMG + wb * 2048 + i * 1024 + lane * 16, v[i]);
      role_sync_idle(C_B, 7);
#pragma unroll
      for (int i = 0; i < 2; ++i) {
        const int oct = (lane >> 4) + 4 * i;
        const unsigned a = L_YIMG + (8 * oct + ((lane & 15) >> 2)) * 128 + (wb * 16 + (lane & 3) * 4) * 2;
        const u32x4 yy = __builtin_bit_cast(u32x4, cat8(tr_read(a), tr_read(a + 512)));
        u32x4 r;
#pragma unroll
        for (int k = 0; k < 4; ++k) {
          const unsigned sk = pk_bf16(bf_lo(uu[i][k]) * bf_lo(ds[i][k]), bf_hi(uu[i][k]) * bf_hi(ds[i][k]));
          r[k] = pk_bf16(bf_lo(yy[k]) + bf_lo(sk), bf_hi(yy[k]) + bf_hi(sk));
        }
        *(GAS u32x4*)(out + (size_t)(idx * 64 * 1792 + 896 + xcd * 128) + (unsigned)(tl * 1792 + oct * 16)) = r;
      }
      STAMP(8);
    } else if (b == 7 && wb == 0) {
#pragma unroll 1
      for (int i = 0; i < 4; ++i) rec_wait(wsr, WS_QFLAG + (i * 64 + lane) * 128, tag);
      if (lane == 0) flag_store(ws, WS_EPOCH, tag);
    }
    STAMP_FLUSH();
  }
}

extern "C" void kernel_launch(void* const* d_in, const int* in_sizes, int n_in, void* d_out, int out_size, void* d_ws, size_t ws_size,
                              hipStream_t stream) {
  (void)in_sizes; (void)n_in; (void)out_size; (void)ws_size;
  (void)hipFuncSetAttribute((const void*)fused_mha_s4d, hipFuncAttributeMaxDynamicSharedMemorySize, LDS_BYTES);
  fused_mha_s4d<<<256, 512, LDS_BYTES, stream>>>((const char*)d_in[0], (const char*)d_in[1], (const char*)d_in[2], (const char*)d_in[3],
                                                 (const char*)d_in[4], (const char*)d_in[5], (const char*)d_in[6], (const char*)d_in[7],
                                                 (const char*)d_in[8], (const char*)d_in[9], (char*)d_out,
                                                 (char*)d_ws + (WSEND ? ((ws_size - WS_END) & ~(size_t)0xfffff) : 0));
}
```

```cpp
#include <hip/hip_runtime.h>
#include <hip/hip_bf16.h>
#include <stdint.h>

typedef short s16x8 __attribute__((ext_vector_type(8)));
typedef short s16x4 __attribute__((ext_vector_type(4)));
typedef short s16x2 __attribute__((ext_vector_type(2)));
typedef _Float16 h16x8 __attribute__((ext_vector_type(8)));
typedef _Float16 h16x2 __attribute__((ext_vector_type(2)));
typedef __bf16 b16x2 __attribute__((ext_vector_type(2)));
typedef float f32x16 __attribute__((ext_vector_type(16)));
typedef float f32x4 __attribute__((ext_vector_type(4)));
typedef float f32x2 __attribute__((ext_vector_type(2)));
typedef unsigned u32x4 __attribute__((ext_vector_type(4)));
typedef unsigned u32x2 __attribute__((ext_vector_type(2)));
typedef unsigned long long u64;
#define LAS __attribute__((address_space(3)))
#define GAS __attribute__((address_space(1)))
#if defined(__HIP_DEVICE_COMPILE__)
#define NOPK __attribute__((target("no-packed-fp32-ops")))
#else
#define NOPK
#endif
#define DI NOPK __device__ __forceinline__
#define SBAR() __builtin_amdgcn_sched_barrier(0)
#define PIN(x) asm volatile("" : "+v"(x))
#define MFMA32(a, b, c) __builtin_amdgcn_mfma_f32_32x32x16_bf16((a), (b), (c), 0, 0, 0)
#define MFMA32H(a, b, c) __builtin_amdgcn_mfma_f32_32x32x16_f16((a), (b), (c), 0, 0, 0)
#define MFMA16(a, b, c) __builtin_amdgcn_mfma_f32_16x16x32_bf16((a), (b), (c), 0, 0, 0)
#define AUX_SC1 16

#define PREFETCH 1
#define WSEND 0
#define APRIO 3
#define INPF 0
#define BCONV 0
#define STAMPS 0

constexpr unsigned WS_EPOCH = 0;
constexpr unsigned WS_QFLAG = 4096;
constexpr unsigned WS_OFLAG = WS_QFLAG + 256 * 128;
constexpr unsigned WS_UFLAG = WS_OFLAG + 1024 * 128;
constexpr unsigned WS_YFLAG = WS_UFLAG + 256 * 128;
constexpr unsigned WS_QKV = 393216;
constexpr unsigned WS_KV = WS_QKV + 16 * 64 * 2048;
constexpr unsigned WS_O = WS_QKV + 48 * 64 * 2048;
constexpr unsigned WS_UT = WS_O + 64 * 16 * 2048;
constexpr unsigned WS_YT = WS_UT + 256 * 8192;
constexpr unsigned WS_DBG = WS_YT + 224 * 8192;
constexpr unsigned WS_END = WS_DBG + 256 * 8 * 16 * 8;
static_assert(WS_YFLAG + 256 * 128 <= WS_QKV, "ws map");

constexpr unsigned L_RING = 0, SLOT_B = 28672, XCH_B = 16384;
constexpr unsigned L_B = 100352;
constexpr unsigned TAB_W = 14000, TAB_HI = 8448;
constexpr unsigned L_UIMG = 4 * SLOT_B + 2 * TAB_W;
constexpr unsigned L_T = 2 * SLOT_B, T_B = 2816;
constexpr unsigned KC_S = 4160, UC_S = 4640, CH_B = 4 * KC_S + 2 * UC_S;
constexpr unsigned L_YIMG = L_B + 49152;
constexpr unsigned L_CTL = 163584;
constexpr unsigned LDS_BYTES = 163840;
constexpr unsigned C_A = L_CTL, C_B = L_CTL + 4, C_GO = L_CTL + 8, C_APUB = L_CTL + 12, C_BPUB = L_CTL + 16, C_WO = L_CTL + 20, C_BFREE = L_CTL + 24, C_BTAG = L_CTL + 28, C_U = L_CTL + 32, C_Q = L_CTL + 36, C_QR = L_CTL + 40, C_FB = L_CTL + 44, C_KM = L_CTL + 48;
constexpr unsigned C_L = L_CTL + 64, C_R = L_CTL + 96;
constexpr unsigned L_MAX = 162816;

constexpr unsigned L_SINK = L_MAX + 512;
constexpr unsigned SPIN_CAP = 1u << 22;
constexpr float LOG2E = 1.4426950408889634f;
constexpr float QSCALE = 0.18898223650461362f * LOG2E;
constexpr float SAFE_BOUND = 48.0f;

template <int V> struct IC { static constexpr int value = V; };
DI unsigned f2u(float f) { return __builtin_bit_cast(unsigned, f); }
DI float u2f(unsigned u) { return __builtin_bit_cast(float, u); }
DI unsigned umax(unsigned a, unsigned b) { return a > b ? a : b; }
DI unsigned sel(unsigned a, unsigned b, u64 mask) { return __builtin_amdgcn_inverse_ballot_w64(mask) ? b : a; }
DI float fexp(float x) { return __builtin_amdgcn_exp2f(x * LOG2E); }
DI float bf_lo(unsigned v) { return u2f(v << 16); }
DI float bf_hi(unsigned v) { return u2f(v & 0xffff0000u); }
DI unsigned pk_bf16(float a, float b) { f32x2 v = {a, b}; return __builtin_bit_cast(unsigned, __builtin_convertvector(v, b16x2)); }
DI unsigned pk_f16(float a, float b) { f32x2 v = {a, b}; return __builtin_bit_cast(unsigned, __builtin_convertvector(v, h16x2)); }
DI float bf_round(float a) { return bf_lo(pk_bf16(a, a)); }
template <int OFF> DI void glds(unsigned voff, const char* sbase, unsigned lds) {
  asm volatile("s_add_u32 m0, %2, %3\n\ts_nop 0\n\tglobal_load_lds_dwordx4 %0, %1" :: "v"(voff), "s"(sbase), "s"(lds), "i"(OFF) : "memory", "scc");
}
DI float half_swap_add(float v) {
  auto r = __builtin_amdgcn_permlane32_swap(f2u(v), f2u(v), false, false);
  return u2f(r[0]) + u2f(r[1]);
}
template <int OFF> DI void glds_dev(unsigned voff, const char* sbase, unsigned lds) {
  asm volatile("s_add_u32 m0, %2, %3\n\ts_nop 0\n\tglobal_load_lds_dwordx4 %0, %1 sc1" :: "v"(voff), "s"(sbase), "s"(lds), "i"(OFF) : "memory", "scc");
}
DI float dot2(unsigned a, float c) { return __builtin_amdgcn_fdot2_f32_bf16(__builtin_bit_cast(b16x2, a), __builtin_bit_cast(b16x2, a), c, false); }
DI float frag_norm2(u32x4 f0, u32x4 f1, int hh) {
  float a = dot2(f0[0], 0.f), b = dot2(f0[1], 0.f), c = dot2(f1[2], 0.f);
  a = dot2(f0[2], a); b = dot2(f0[3], b); c = dot2(f1[3], c); a = dot2(f1[0], a); b = dot2(f1[1], b);
  return half_swap_add(a + b + (hh ? 0.f : c));
}
DI s16x4 tr_read(unsigned a) { return __builtin_bit_cast(s16x4, __builtin_amdgcn_ds_read_tr16_b64_v4i16((LAS s16x4*)(uintptr_t)a)); }
DI s16x8 cat8(s16x4 lo, s16x4 hi) { return __builtin_shufflevector(lo, hi, 0, 1, 2, 3, 4, 5, 6, 7); }
template <class T> DI T lds_ld(unsigned a) { return *(LAS T*)(uintptr_t)a; }
template <class T> DI void lds_st(unsigned a, T v) { *(LAS T*)(uintptr_t)a = v; }
#define LDS_POLL(V, A, S, N, T, EXTRA) "s_mov_b32 " N ", 0\n1:\n\t" EXTRA "ds_read_b32 " V ", " A "\n\ts_add_u32 " N ", " N ", 1024\n\t" \
  "s_waitcnt lgkmcnt(0)\n\tv_readfirstlane_b32 " S ", " V "\n\ts_cbranch_scc1 4f\n\ts_cmp_lt_u32 " S ", " T "\n\ts_cbranch_scc1 1b\n4:\n\t"
DI unsigned lds_wait_ge(unsigned a, unsigned target) {
  unsigned v, sv, n;
  asm volatile(LDS_POLL("%0", "%3", "%1", "%2", "%4", "") : "=&v"(v), "=&s"(sv), "=&s"(n) : "v"(a), "s"(target) : "memory", "scc");
  return sv;
}
DI unsigned lds_wait_ge_slow(unsigned a, unsigned target) {
  unsigned v, sv, n;
  asm volatile(LDS_POLL("%0", "%3", "%1", "%2", "%4", "s_sleep 1\n\t") : "=&v"(v), "=&s"(sv), "=&s"(n) : "v"(a), "s"(target) : "memory", "scc");
  return sv;
}
DI void lds_arrive(unsigned a) {
  asm volatile("s_waitcnt lgkmcnt(0)\n\ts_mov_b64 exec, 1\n\tds_add_u32 %0, %1\n\ts_mov_b64 exec, -1" :: "v"(a), "v"(1u) : "memory");
}
DI void lds_arrive_nw(unsigned a) {
  asm volatile("s_mov_b64 exec, 1\n\tds_add_u32 %0, %1\n\ts_mov_b64 exec, -1" :: "v"(a), "v"(1u) : "memory");
}
DI void role_sync(unsigned a, unsigned nth) { lds_arrive(a); lds_wait_ge(a, nth * 4u); }
DI void role_sync_slow(unsigned a, unsigned nth) { lds_arrive(a); lds_wait_ge_slow(a, nth * 4u); }
DI void role_sync_idle(unsigned a, unsigned nth) {
  lds_arrive(a);
  unsigned v, sv, n;
  asm volatile(LDS_POLL("%0", "%3", "%1", "%2", "%4", "s_sleep 6\n\t") : "=&v"(v), "=&s"(sv), "=&s"(n) : "v"(a), "s"(nth * 4u) : "memory", "scc");
}
DI bool last_of_four(unsigned a, int lane, unsigned round) {
  unsigned old = 0;
  if (lane == 0) old = __hip_atomic_fetch_add((LAS unsigned*)(uintptr_t)a, 1u, __ATOMIC_RELAXED, __HIP_MEMORY_SCOPE_WORKGROUP);
  return (unsigned)__builtin_amdgcn_readfirstlane(old) == round * 4u + 3u;
}
DI void st128(u32x4 v, __amdgpu_buffer_rsrc_t r, unsigned voff, unsigned soff) {
  __builtin_amdgcn_raw_buffer_store_b128(v, r, voff, soff, AUX_SC1);
  asm volatile("s_nop 1" :: "v"(v) : "memory");
}
DI void drain_vm() { asm volatile("s_waitcnt vmcnt(0)" ::: "memory"); }
DI void flag_store(char* ws, unsigned off, u64 tag) { __hip_atomic_store((GAS u64*)(ws + off), tag, __ATOMIC_RELAXED, __HIP_MEMORY_SCOPE_AGENT); }
DI void flag_wait(char* ws, unsigned off, u64 tag) {
#pragma unroll 1
  for (unsigned n = 0; n < SPIN_CAP; ++n) {
    u64 v = __hip_atomic_load((GAS u64*)(ws + off), __ATOMIC_RELAXED, __HIP_MEMORY_SCOPE_AGENT);
    if (__builtin_amdgcn_ballot_w64(v != tag) == 0ull) break;
    __builtin_amdgcn_s_sleep(2);
  }
  asm volatile("" ::: "memory");
}
DI u32x4 rec_wait(__amdgpu_buffer_rsrc_t r, unsigned off, u64 tag) {
  u32x4 v;
#pragma unroll 1
  for (unsigned n = 0; n < SPIN_CAP; ++n) {
    v = __builtin_amdgcn_raw_buffer_load_b128(r, off, 0, AUX_SC1);
    if (__builtin_amdgcn_ballot_w64(v[0] != (unsigned)tag || v[2] != (unsigned)(tag >> 32)) == 0ull) break;
    asm volatile("s_sleep 1" ::: "memory");
  }
  asm volatile("" ::: "memory");
  return v;
}
template <int CTRL> DI unsigned dpp_max(unsigned v) { unsigned t = __builtin_amdgcn_update_dpp(0u, v, CTRL, 0xf, 0xf, false); return v > t ? v : t; }
DI unsigned wave_umax(unsigned v) {
  v = dpp_max<0x111>(v); v = dpp_max<0x112>(v); v = dpp_max<0x114>(v); v = dpp_max<0x118>(v);
  v = dpp_max<0x142>(v); v = dpp_max<0x143>(v);
  return __builtin_amdgcn_readlane(v, 63);
}
DI float half_swap_max(float v) {
  auto r = __builtin_amdgcn_permlane32_swap(f2u(v), f2u(v), false, false);
  return __builtin_fmaxf(u2f(r[0]), u2f(r[1]));
}
DI f32x2 cmul(f32x2 a, f32x2 b) { f32x2 r; r.x = __builtin_fmaf(-a.y, b.y, a.x * b.x); r.y = __builtin_fmaf(a.y, b.x, a.x * b.y); return r; }
DI void hmul3(unsigned a, unsigned b1, unsigned b2, unsigned b3, unsigned& r1, unsigned& r2, unsigned& r3) {
  asm("v_pk_mul_f16 %0, %3, %4 op_sel:[0,0] op_sel_hi:[0,1]\n\tv_pk_mul_f16 %1, %3, %5 op_sel:[0,0] op_sel_hi:[0,1]\n\tv_pk_mul_f16 %2, %3, %6 op_sel:[0,0] op_sel_hi:[0,1]\n\t"
      "v_pk_fma_f16 %0, %3, %4, %0 op_sel:[1,1,0] op_sel_hi:[1,0,1] neg_lo:[1,0,0]\n\tv_pk_fma_f16 %1, %3, %5, %1 op_sel:[1,1,0] op_sel_hi:[1,0,1] neg_lo:[1,0,0]\n\t"
      "v_pk_fma_f16 %2, %3, %6, %2 op_sel:[1,1,0] op_sel_hi:[1,0,1] neg_lo:[1,0,0]"
      : "=&v"(r1), "=&v"(r2), "=&v"(r3) : "v"(a), "v"(b1), "v"(b2), "v"(b3));
}
DI f32x2 zpow(float rho, float th, float p) {
  const float e = __builtin_amdgcn_exp2f(rho * p), f = __builtin_amdgcn_fractf(th * p);
  f32x2 r; r.x = e * __builtin_amdgcn_cosf(f); r.y = e * __builtin_amdgcn_sinf(f); return r;
}
#define STAMP(k) do { } while (0)
#define STAMP_FLUSH() do { } while (0)

#define QR(a, n) "v[" #a "+" #n ":" #a "+" #n "+1]"
#define Q_READ(F, XA, S) "ds_read_b128 v[" #F ":" #F "+3], " XA "\n\t" \
  "ds_read_b64_tr_b16 " QR(F, 4) ", %[wa] offset:" #S "*3072\n\tds_read_b64_tr_b16 " QR(F, 6) ", %[wa] offset:" #S "*3072+768\n\t" \
  "ds_read_b64_tr_b16 " QR(F, 8) ", %[wa] offset:" #S "*3072+56\n\tds_read_b64_tr_b16 " QR(F, 10) ", %[wa] offset:" #S "*3072+824\n\t" \
  "ds_read_b64_tr_b16 " QR(F, 12) ", %[wa] offset:" #S "*3072+112\n\tds_read_b64_tr_b16 " QR(F, 14) ", %[wa] offset:" #S "*3072+880\n\t"
#define Q_T(ACC, F, n) "v_mfma_f32_32x32x16_bf16 " ACC ", v[" #F "+" #n ":" #F "+" #n "+3], v[" #F ":" #F "+3], " ACC "\n\t"
#define Q_N(ACC, F, n) "v_mfma_f32_32x32x16_bf16 " ACC ", v[" #F ":" #F "+3], v[" #F "+" #n ":" #F "+" #n "+3], " ACC "\n\t"
#define Q_MMA(F, M0, M1, M2) M0("%[a0]", F, 4) M1("%[a1]", F, 8) M2("%[a2]", F, 12)
#define Q_ARRIVE "s_mov_b64 exec, 1\n\tds_add_u32 %[ca], %[one]\n\ts_mov_b64 exec, -1\n\t"
#define Q_TR(F, n, S, O) "ds_read_b64_tr_b16 " QR(F, n) ", %[wa] offset:" #S "*3072+" #O "\n\t"
#define Q_GROUP(G, F, XA, S, M0, M1, M2) \
  M0("%[a0]", G, 4) "ds_read_b128 v[" #F ":" #F "+3], " XA "\n\t" Q_TR(F, 4, S, 0) Q_TR(F, 6, S, 768) \
  M1("%[a1]", G, 8) Q_TR(F, 8, S, 56) Q_TR(F, 10, S, 824) \
  M2("%[a2]", G, 12) Q_TR(F, 12, S, 112) Q_TR(F, 14, S, 880)
#define QKV_BODY(M0, M1, M2) \
  "2:\n\t" \
  "v_add_u32 %[cl], 4, %[cl]\n\ts_waitcnt lgkmcnt(7)\n\t" \
  Q_GROUP(100, 132, "%[x2]", 2, M0, M1, M2) "ds_read_b32 %[tv], %[cl]\n\t" \
  "s_waitcnt lgkmcnt(8)\n\t" \
  Q_GROUP(116, 148, "%[x3]", 3, M0, M1, M2) \
  "s_cmp_eq_u32 %[tg], 28\n\ts_cbranch_scc1 5f\n\t" \
  "s_cmp_eq_u32 %[tg], 16\n\ts_cselect_b32 %[ts], %[wrap], %[step]\n\ts_add_u32 %[tg], %[tg], 4\n\t" \
  "v_add_u32 %[x0], %[ts], %[x0]\n\tv_add_u32 %[x1], %[ts], %[x1]\n\tv_add_u32 %[x2], %[ts], %[x2]\n\tv_add_u32 %[x3], %[ts], %[x3]\n\tv_add_u32 %[wa], %[ts], %[wa]\n\t" \
  "s_waitcnt lgkmcnt(7)\n\tv_readfirstlane_b32 %[ts], %[tv]\n\ts_cmp_ge_u32 %[ts], 2\n\ts_cbranch_scc1 0f\n\t" \
  LDS_POLL("%[tv]", "%[cl]", "%[ts]", "%[tn]", "2", "") \
  "0:\n\t" \
  Q_GROUP(132, 100, "%[x0]", 0, M0, M1, M2) \
  "s_waitcnt lgkmcnt(7)\n\t" Q_ARRIVE \
  Q_GROUP(148, 116, "%[x1]", 1, M0, M1, M2) \
  "v_add_u32 %[ca], 4, %[ca]\n\ts_branch 2b\n" \
  "5:\n\t" \
  "s_waitcnt lgkmcnt(7)\n\t" Q_MMA(132, M0, M1, M2) "s_waitcnt lgkmcnt(0)\n\t" Q_ARRIVE "s_nop 0\n\t" Q_MMA(148, M0, M1, M2)
#define QKV_LOOP() asm volatile( \
  "s_mov_b32 %[tg], 4\n\t" LDS_POLL("%[tv]", "%[cl]", "%[ts]", "%[tn]", "2", "") \
  Q_READ(100, "%[x0]", 0) Q_READ(116, "%[x1]", 1) \
  "s_cmp_gt_u32 %[g], 10\n\ts_cbranch_scc1 7f\n\ts_cmp_eq_u32 %[g], 10\n\ts_cbranch_scc1 6f\n" \
  QKV_BODY(Q_T, Q_T, Q_T) "s_branch 8f\n6:\n" QKV_BODY(Q_T, Q_T, Q_N) "s_branch 8f\n7:\n" QKV_BODY(Q_N, Q_N, Q_N) \
  "8:\n\ts_nop 7\n\ts_nop 7" \
  : [a0] "+v"(acc[0]), [a1] "+v"(acc[1]), [a2] "+v"(acc[2]), [x0] "+v"(xa0), [x1] "+v"(xa1), [x2] "+v"(xa2), [x3] "+v"(xa3), [wa] "+v"(wa), \
    [tv] "=&v"(tv), [ts] "=&s"(ts), [tn] "=&s"(tn), [tg] "=&s"(tg), [ca] "+v"(car), [cl] "+v"(cla) \
  : [one] "v"(1u), [step] "s"(SLOT_B), [wrap] "s"(0u - 3u * SLOT_B), [g] "s"(g) \
  : "memory", "scc", V100_163)
#define V10(p) "v" #p "0", "v" #p "1", "v" #p "2", "v" #p "3", "v" #p "4", "v" #p "5", "v" #p "6", "v" #p "7", "v" #p "8", "v" #p "9"
#define V100_163 V10(10), V10(11), V10(12), V10(13), V10(14), V10(15), "v160", "v161", "v162", "v163"

NOPK __global__ void __launch_bounds__(512, 2)
fused_mha_s4d(const char* __restrict__ x, const char* __restrict__ wqkv, const char* __restrict__ wo, const char* __restrict__ u,
              const char* __restrict__ logAr, const char* __restrict__ Aim, const char* __restrict__ Cre, const char* __restrict__ Cim,
              const char* __restrict__ logdt, const char* __restrict__ Dsk, char* __restrict__ out, char* __restrict__ ws) {
  extern __shared__ __attribute__((aligned(16))) char smem[];
  const int tid = __builtin_amdgcn_workitem_id_x(), lane = tid & 63;
  const int wid = __builtin_amdgcn_readfirstlane(tid >> 6);
  const int b = __builtin_amdgcn_workgroup_id_x(), xcd = b & 7, idx = b >> 3;
  const int r32 = lane & 31, hh = lane >> 5;
  const unsigned L0 = (unsigned)(uintptr_t)smem;
  (void)L0;
  const __amdgpu_buffer_rsrc_t wsr = __builtin_amdgcn_make_buffer_rsrc(ws, 0, WS_END, 0x00020000);
  asm volatile("" :: "s"(x), "s"(wqkv), "s"(wo), "s"(u), "s"(logAr), "s"(Aim), "s"(Cre), "s"(Cim), "s"(logdt), "s"(Dsk), "s"(out), "s"(ws));
  STAMP(0);
  asm volatile(".Lpf_begin:");
#define MAKE_TAG(e) (((u64)(unsigned)__builtin_amdgcn_readfirstlane((unsigned)((e) >> 32)) << 32 | (unsigned)__builtin_amdgcn_readfirstlane((unsigned)(e))) + 0x9E3779B97F4A7C15ull)
  const int rt = (xcd & 3) * 4 + (idx >> 3), g = (xcd >> 2) * 8 + (idx & 7);
  const int woff = (g == 15) ? 8 : 0;
  const bool has_b = xcd < 7;

  if (__builtin_expect(wid >= 4, 0)) {
    const int w = wid - 4;
    __builtin_amdgcn_s_setprio(APRIO);
    const u64 epoch = __hip_atomic_load((GAS u64*)(ws + WS_EPOCH), __ATOMIC_RELAXED, __HIP_MEMORY_SCOPE_AGENT);
    {
      u64 pc; unsigned below, above;
      asm volatile("s_getpc_b64 %0\n.Lpf_here:\n\ts_mov_b32 %1, .Lpf_here-.Lpf_begin\n\ts_mov_b32 %2, .Lpf_end-.Lpf_here" : "=s"(pc), "=s"(below), "=s"(above));
      const unsigned off = ((w * 64 + lane) * 8 + (idx & 7)) * 128;
      if (off < below + above) asm volatile("s_mov_b32 m0, %2\n\ts_nop 0\n\tglobal_load_lds_dword %0, %1" :: "v"(off), "s"(pc - below), "s"(L_SINK) : "memory");
    }
    if (w == 0) lds_st<u32x4>(L_MAX + lane * 16, u32x4{0u, 0u, 0u, 0u});
    asm volatile("s_waitcnt lgkmcnt(0)\n\ts_barrier" ::: "memory");
    STAMP(9);
    const int rt32 = rt * 4 + w;
    unsigned l16 = lane * 16, so[3], kma[3], vcst = r32 == 28 ? 0x3F803F80u : 0u; unsigned scl[3]; u64 vmask[3], pmask[3], qmask[3];
#pragma unroll
    for (int j = 0; j < 3; ++j) {
      const int sl = 3 * g + j, ty = sl >> 4;
      so[j] = ty == 0 ? WS_QKV + (sl * 64 + rt32) * 2048 : WS_KV + ((sl & 15) * 64 + rt32) * 4096 + (ty - 1) * 2048;
      scl[j] = ty == 0 ? __builtin_bit_cast(unsigned, QSCALE) : 0x3F800000u;
      const unsigned mv = __builtin_amdgcn_readfirstlane(ty == 2 ? 0xF0000000u : 0u), mp = __builtin_amdgcn_readfirstlane(ty < 2 ? ~0u : 0u), mq = __builtin_amdgcn_readfirstlane(ty == 0 ? ~0u : 0u);
      vmask[j] = (u64)mv << 32 | mv; pmask[j] = (u64)mp << 32; qmask[j] = (u64)mq << 32;
      kma[j] = C_KM + j * 4;
      asm volatile("" : "+s"(so[j]), "+s"(scl[j]), "+s"(vmask[j]), "+s"(pmask[j]), "+s"(qmask[j]), "+v"(kma[j]));
    }
    PIN(l16); PIN(vcst);
    f32x16 acc[3];
#pragma unroll
    for (int j = 0; j < 3; ++j) acc[j] = f32x16{};
    {
      const unsigned xa_l = w * 4096 + r32 * 128, xsw = (r32 >> 1) & 7;
      unsigned xa0 = xa_l + (((0 + hh) ^ xsw) << 4), xa1 = xa_l + (((2 + hh) ^ xsw) << 4), xa2 = xa0 ^ 64, xa3 = xa1 ^ 64;
      unsigned wa = XCH_B + (8 * hh + ((lane & 15) >> 2)) * 192 + woff + ((lane >> 4) & 1) * 32 + (lane & 3) * 8;
      unsigned tv, ts, tn, tg, car = C_R, cla = C_L;
      QKV_LOOP();
    }
    STAMP(2);
#pragma unroll
    for (int j = 0; j < 3; ++j) {
      u32x4 f0, f1;
#pragma unroll
      for (int i = 0; i < 4; ++i) f0[i] = sel(pk_bf16(acc[j][2 * i] * u2f(scl[j]), acc[j][2 * i + 1] * u2f(scl[j])), vcst, vmask[j]);
      st128(f0, wsr, l16, so[j]);
#pragma unroll
      for (int i = 0; i < 4; ++i) f1[i] = sel(pk_bf16(acc[j][8 + 2 * i] * u2f(scl[j]), acc[j][9 + 2 * i] * u2f(scl[j])), vcst, vmask[j]);
      const unsigned n2 = f2u(frag_norm2(f0, f1, hh));
      asm volatile("ds_max_u32 %0, %1" :: "v"(kma[j]), "v"(n2) : "memory");
      f1[2] = sel(f1[2], sel(0x3F803F80u, n2, qmask[j]), pmask[j]); f1[3] = sel(f1[3], 0x00003F80u, pmask[j]);
      st128(f1, wsr, l16 + 1024, so[j]);
    }
    const u64 tag = MAKE_TAG(epoch);
    STAMP(14);
    const int h = xcd * 2 + (idx >> 4), qt = idx & 15;
    f32x16 o[4];
#pragma unroll
    for (int i = 0; i < 4; ++i) { o[i] = f32x16{}; PIN(o[i]); }
    if (w == 3) lds_wait_ge(C_BTAG, 4);
    drain_vm();
    STAMP(15);
    if (w == 3) {
      lds_wait_ge(C_APUB, 3);
      const u32x4 km = lds_ld<u32x4>(C_KM);
      const u32x4 rec = {(unsigned)tag, ((km[0] + 0xffffu) >> 16) | ((km[1] + 0xffffu) & 0xffff0000u), (unsigned)(tag >> 32), km[2] + 0xffffu};
      if (lane == 0) st128(rec, wsr, (g * 16 + rt) * 128, WS_QFLAG);
    } else {
      lds_arrive_nw(C_APUB);
      if (w == 0) {
        rec_wait(wsr, WS_QFLAG + ((h / 3) * 16 + qt) * 128, tag);
        lds_wait_ge(C_R + 24, 4);
        const char* qs = ws + WS_QKV + (h * 64 + qt * 4) * 2048;
        glds_dev<0>(l16, qs, L_RING); glds_dev<1024>(l16, qs + 1024, L_RING); glds_dev<2048>(l16, qs + 2048, L_RING); glds_dev<3072>(l16, qs + 3072, L_RING);
        glds_dev<4096>(l16, qs + 4096, L_RING); glds_dev<5120>(l16, qs + 5120, L_RING); glds_dev<6144>(l16, qs + 6144, L_RING); glds_dev<7168>(l16, qs + 7168, L_RING);
        drain_vm();
        lds_arrive_nw(C_Q);
      } else if (w == 1) {
        const u32x4 rec = rec_wait(wsr, WS_QFLAG + ((hh ? (32 + h) / 3 : (16 + h) / 3) * 16 + (lane & 15)) * 128, tag);
        const int j = (16 + h) % 3;
        unsigned v = j == 0 ? rec[1] << 16 : ((j == 1 ? rec[1] : rec[3]) & 0xffff0000u);
        v = dpp_max<0x111>(v); v = dpp_max<0x112>(v); v = dpp_max<0x114>(v); v = dpp_max<0x118>(v);
        lds_st<unsigned>(C_GO, __builtin_amdgcn_readlane(v, 15) + 1u);
      }
    }
    STAMP(3);
    s16x8 qf[4][2], kf[4][2], vf[4][2];
    const unsigned kbase = WS_KV + (h * 64 + w * 16) * 4096;
#define LDR(rs_, soff, voff) __builtin_bit_cast(s16x8, __builtin_amdgcn_raw_buffer_load_b128(rs_, l16 + (voff), (soff), AUX_SC1))
#define LDF(soff, voff) LDR(wsr, soff, voff)
#define LDKV(rs_, B, soff) do { SBAR(); kf[B][0] = LDR(rs_, soff, 0); kf[B][1] = LDR(rs_, soff, 1024); vf[B][0] = LDR(rs_, soff, 2048); vf[B][1] = LDR(rs_, soff, 3072); SBAR(); } while (0)
    lds_wait_ge_slow(C_Q, 1);
    STAMP(1);
#pragma unroll
    for (int i = 0; i < 4; ++i) { const unsigned a = L_RING + ((w + 1 + i) & 3) * 2048 + l16; qf[i][0] = lds_ld<s16x8>(a); qf[i][1] = lds_ld<s16x8>(a + 1024); }
    lds_arrive(C_QR);
    const unsigned km2 = lds_wait_ge_slow(C_GO, 1);
    STAMP(4);
    LDKV(wsr, 0, kbase);
    LDKV(wsr, 1, kbase + 4096); LDKV(wsr, 2, kbase + 8192);
    {
      const float kmax2 = u2f(km2) * 1.02f;
      unsigned pad2[4], pad3[4]; bool unsafe = false;
#pragma unroll
      for (int i = 0; i < 4; ++i) {
        const float m = __builtin_amdgcn_sqrtf(u2f(__builtin_bit_cast(u32x4, qf[i][1])[2]) * kmax2);
        unsafe |= hh && !(m <= SAFE_BOUND);
        pad2[i] = pk_bf16(-m, 0.f); pad3[i] = 0u;
      }
      if (__builtin_expect(__builtin_amdgcn_ballot_w64(unsafe) != 0ull, 0)) {
        float mx[4];
#pragma unroll
        for (int i = 0; i < 4; ++i) { mx[i] = -INFINITY; if (hh) { u32x4 t_ = __builtin_bit_cast(u32x4, qf[i][1]); t_[2] = 0u; t_[3] = 0u; qf[i][1] = __builtin_bit_cast(s16x8, t_); } }
#pragma unroll 1
        for (int t = 0; t < 16; ++t) {
          const s16x8 k0 = LDF(kbase + t * 4096, 0), k1 = LDF(kbase + t * 4096, 1024);
#pragma unroll
          for (int i = 0; i < 4; ++i) {
            f32x16 s = MFMA32(k0, qf[i][0], f32x16{}); s = MFMA32(k1, qf[i][1], s);
#pragma unroll
            for (int e = 0; e < 16; ++e) mx[i] = __builtin_fmaxf(mx[i], s[e]);
          }
        }
#pragma unroll
        for (int i = 0; i < 4; ++i) {
          const float v = half_swap_max(mx[i]);
          const unsigned k_ = f2u(v) ^ ((unsigned)((int)f2u(v) >> 31) | 0x80000000u);
          __hip_atomic_fetch_max((LAS unsigned*)(uintptr_t)(L_MAX + (((w + 1 + i) & 3) * 32 + r32) * 4), k_, __ATOMIC_RELAXED, __HIP_MEMORY_SCOPE_WORKGROUP);
        }
        role_sync(C_FB, 1);
#pragma unroll
        for (int i = 0; i < 4; ++i) {
          const unsigned k_ = lds_ld<unsigned>(L_MAX + (((w + 1 + i) & 3) * 32 + r32) * 4);
          const float m = u2f(k_ ^ ((unsigned)((int)~k_ >> 31) | 0x80000000u));
          const float m0 = bf_round(-m), r1 = -m - m0, m1 = bf_round(r1);
          pad2[i] = pk_bf16(m0, m1); pad3[i] = pk_bf16(r1 - m1, 0.f);
        }
      }
#pragma unroll
      for (int i = 0; i < 4; ++i) {
        u32x4 t_ = __builtin_bit_cast(u32x4, qf[i][1]);
        t_[2] = hh ? pad2[i] : t_[2]; t_[3] = hh ? pad3[i] : t_[3]; qf[i][1] = __builtin_bit_cast(s16x8, t_);
      }
    }
    f32x16 sA, sB; u32x4 p0, p1 = u32x4{0u, 0u, 0u, 0u};
    sA = MFMA32(kf[0][0], qf[0][0], f32x16{}); sA = MFMA32(kf[0][1], qf[0][1], sA);
    __builtin_amdgcn_s_setprio(1);
    STAMP(5);
    unsigned redw = L_B + l16; PIN(redw);
#define EX(S, i) S[i] = __builtin_amdgcn_exp2f(S[i])
#define PK(S, i) pk_bf16(S[i], S[(i) + 1])
#define BF(P) __builtin_bit_cast(s16x8, P)
#define STEP(C, N, KN, QN, VP, OP, VC, OC) do { \
      N = MFMA32(KN[0], QN[0], f32x16{}); SBAR(); EX(C, 0); EX(C, 1); EX(C, 2); EX(C, 3); EX(C, 4); SBAR(); \
      OP = MFMA32(VP[1], BF(p1), OP); SBAR(); EX(C, 5); EX(C, 6); EX(C, 7); p0[0] = PK(C, 0); p0[1] = PK(C, 2); p0[2] = PK(C, 4); p0[3] = PK(C, 6); PIN(p0); SBAR(); \
      N = MFMA32(KN[1], QN[1], N); SBAR(); EX(C, 8); EX(C, 9); EX(C, 10); EX(C, 11); EX(C, 12); SBAR(); \
      OC = MFMA32(VC[0], BF(p0), OC); SBAR(); EX(C, 13); EX(C, 14); EX(C, 15); p1[0] = PK(C, 8); p1[1] = PK(C, 10); p1[2] = PK(C, 12); p1[3] = PK(C, 14); PIN(p1); SBAR(); \
    } while (0)
#define RED_WRITE(i) do { const unsigned a_ = redw + (((w + 1 + (i)) & 3) * 3 + (2 - (i))) * 4096; \
      _Pragma("unroll") for (int q_ = 0; q_ < 4; ++q_) lds_st<f32x4>(a_ + q_ * 1024, f32x4{o[i][4 * q_], o[i][4 * q_ + 1], o[i][4 * q_ + 2], o[i][4 * q_ + 3]}); } while (0)
#define TILE(T, TP, TN, RS, LAST) do { \
      STEP(sA, sB, kf[T], qf[1], vf[TP], o[3], vf[T], o[0]); \
      if (!LAST) LDKV(RS, TP, kcur + (T + 3) * 4096); \
      STEP(sB, sA, kf[T], qf[2], vf[T], o[0], vf[T], o[1]); \
      if (LAST) RED_WRITE(0); \
      STEP(sA, sB, kf[T], qf[3], vf[T], o[1], vf[T], o[2]); \
      if (LAST) RED_WRITE(1); \
      STEP(sB, sA, kf[TN], qf[0], vf[T], o[2], vf[T], o[3]); \
      if (LAST) RED_WRITE(2); \
    } while (0)
    vf[3][1] = s16x8{};
    unsigned kcur = kbase;
#pragma unroll 1
    for (int tb = 0;; tb += 4) {
      const __amdgpu_buffer_rsrc_t wsr_next = __builtin_amdgcn_make_buffer_rsrc(ws, 0, tb < 12 ? WS_END : 0u, 0x00020000);
      TILE(0, 3, 1, wsr, false);
      TILE(1, 0, 2, wsr_next, false);
      TILE(2, 1, 3, wsr_next, false);
      if (tb == 12) break;
      TILE(3, 2, 0, wsr, false);
      kcur += 16384;
      if (STAMPS) { if (tb == 0) STAMP(11); else if (tb == 4) { } else { } }
    }
    lds_wait_ge(C_BFREE, 4);
    TILE(3, 2, 0, wsr, true);
    o[3] = MFMA32(vf[3][1], BF(p1), o[3]);
    STAMP(6);
    role_sync(C_A, 1);
    {
      f32x4 pv[12];
#pragma unroll
      for (int k = 0; k < 12; ++k) pv[k] = lds_ld<f32x4>(redw + w * 12288 + k * 1024);
      SBAR();
      f32x16 a = o[3];
#pragma unroll
      for (int k = 0; k < 12; ++k) { const int q = k & 3; a[4 * q] += pv[k][0]; a[4 * q + 1] += pv[k][1]; a[4 * q + 2] += pv[k][2]; a[4 * q + 3] += pv[k][3]; }
      auto lr = __builtin_amdgcn_permlane32_swap(f2u(a[12]), f2u(a[12]), false, false);
      const float inv = __builtin_amdgcn_rcpf(u2f(lr[1]));
      u32x4 f0, f1;
#pragma unroll
      for (int i = 0; i < 4; ++i) { f0[i] = pk_bf16(a[2 * i] * inv, a[2 * i + 1] * inv); f1[i] = pk_bf16(a[8 + 2 * i] * inv, a[9 + 2 * i] * inv); }
      if (hh) { f1[2] = 0u; f1[3] = 0u; }
      const unsigned so = WS_O + ((qt * 4 + w) * 16 + h) * 2048;
      st128(f0, wsr, l16, so);
      st128(f1, wsr, l16 + 1024, so);
      lds_arrive(C_A);
      drain_vm();
      if (lane == 0) flag_store(ws, WS_OFLAG + ((qt * 4 + w) * 16 + h) * 128, tag);
    }
    STAMP(7);
    const int rs = xcd * 8 + (idx >> 2), cg = idx & 3, n0 = w < 3 ? 32 * w : 80;
    lds_wait_ge(C_A, 2 * 4);
    lds_wait_ge(C_WO, 4);
    flag_wait(ws, WS_OFLAG + (rs * 16 + w * 4 + (lane & 3)) * 128, tag);
    STAMP(8);
    {
      const char* os = ws + WS_O + (rs * 16 + w * 4) * 2048;
      const unsigned od = L_B + w * 8192;
      glds_dev<0>(l16, os, od); glds_dev<1024>(l16, os + 1024, od); glds_dev<2048>(l16, os + 2048, od); glds_dev<3072>(l16, os + 3072, od);
      glds_dev<4096>(l16, os + 4096, od); glds_dev<5120>(l16, os + 5120, od); glds_dev<6144>(l16, os + 6144, od); glds_dev<7168>(l16, os + 7168, od);
    }
    s16x8 wf[32];
    unsigned oa[4];
#pragma unroll
    for (int q = 0; q < 4; ++q) {
      const int gq = (w + q) & 3;
      unsigned wa = L_RING + (gq * 112 + 4 * hh + ((lane & 15) >> 2)) * 224 + n0 * 2 + ((lane >> 4) & 1) * 32 + (lane & 3) * 8;
      unsigned wb2 = wa + (hh ? 16 * 224 : 24 * 224);
      oa[q] = redw + gq * 8192;
      PIN(wa); PIN(wb2); PIN(oa[q]);
#pragma unroll
      for (int hd = 0; hd < 4; ++hd) {
        wf[q * 8 + hd * 2] = cat8(tr_read(wa + hd * 28 * 224), tr_read(wa + hd * 28 * 224 + 8 * 224));
        wf[q * 8 + hd * 2 + 1] = cat8(tr_read(wa + hd * 28 * 224 + 16 * 224), tr_read(wb2 + hd * 28 * 224));
      }
    }
    drain_vm();
    lds_arrive_nw(C_A);
    STAMP(13);
    f32x16 d = f32x16{};
    {
      s16x8 ob[8];
#pragma unroll
      for (int k = 0; k < 8; ++k) ob[k] = lds_ld<s16x8>(oa[0] + k * 1024);
      SBAR();
#pragma unroll
      for (int k = 0; k < 4; ++k) { d = MFMA32(wf[k], ob[k], d); SBAR(); }
      lds_wait_ge(C_A, 3 * 4);
#pragma unroll
      for (int k = 0; k < 4; ++k) ob[k] = lds_ld<s16x8>(oa[1] + k * 1024);
      SBAR();
#pragma unroll
      for (int k = 4; k < 32; ++k) {
        d = MFMA32(wf[k], ob[k & 7], d);
        if (k + 8 < 32) ob[k & 7] = lds_ld<s16x8>(oa[(k + 8) >> 3] + (k & 7) * 1024);
        SBAR();
      }
    }
    {
      char* op = out + (size_t)(rs * 32 * 1792 + (cg * 112 + n0) * 2) + (unsigned)(r32 * 1792 + hh * 16);
#pragma unroll
      for (int k = 0; k < 2; ++k) {
        const unsigned a0 = pk_bf16(d[8 * k], d[8 * k + 1]), a1 = pk_bf16(d[8 * k + 2], d[8 * k + 3]);
        const unsigned b0 = pk_bf16(d[8 * k + 4], d[8 * k + 5]), b1 = pk_bf16(d[8 * k + 6], d[8 * k + 7]);
        auto s0 = __builtin_amdgcn_permlane32_swap(a0, b0, false, false);
        auto s1 = __builtin_amdgcn_permlane32_swap(a1, b1, false, false);
        if (k || w < 3) *(GAS u32x4*)(op + k * 32) = u32x4{s0[0], s1[0], s0[1], s1[1]};
      }
    }
    STAMP(10);
    STAMP_FLUSH();
    asm volatile(".Lpf_end:");
  } else {
    const int wb = wid ^ 2;
    const int c = wb & 1, half = wb >> 1;
    const int d = (has_b ? xcd * 64 : 0) + idx * 2 + c;
    const unsigned po = d * 512 + lane * 4;
    unsigned pdt, p0[4], p1[4];
    u64 tag;
#define LOAD_P(P, O) do { P[0] = *(const GAS unsigned*)(logAr + (O) + po); P[1] = *(const GAS unsigned*)(Aim + (O) + po); \
      P[2] = *(const GAS unsigned*)(Cre + (O) + po); P[3] = *(const GAS unsigned*)(Cim + (O) + po); } while (0)
    const unsigned T = L_T + c * 2 * T_B;
    unsigned ga[2][11], wv[7];
    auto gather_addr = [&]() __attribute__((always_inline)) NOPK {
#pragma unroll
      for (int i = 0; i < 7; ++i) {
        const unsigned p = i * 64 + lane, row = (p * 37450u) >> 19; wv[i] = row * 896 + (p - row * 14) * 16; PIN(wv[i]);
      }
#pragma unroll
      for (int it = 0; it < 2; ++it) {
        const int l0 = 8 * (3 + it * 128 + half * 64 + lane) - 27;
        const float f0 = (float)l0;
#pragma unroll
        for (int k = 0; k < 11; k += 2) {
          const unsigned pk = pk_bf16(f0 + (float)k, f0 + (float)(k + 1));
          asm volatile("v_mad_u32_u16 %0, %1, 4, %2" : "=v"(ga[it][k]) : "v"(pk), "s"(T - 4u * 17024u));
          if (k < 10) asm volatile("v_mad_u32_u16 %0, %1, 4, %2 op_sel:[1,0,0,0]" : "=v"(ga[it][k + 1]) : "v"(pk), "s"(T - 4u * 17024u));
        }
        if (it == 0 && !half) {
#pragma unroll
          for (int k = 0; k < 11; ++k) { const int l = l0 + k; ga[0][k] = l < 128 ? T + 4 * ((k < 3 && l < 0) ? 641 : l) : ga[0][k]; PIN(ga[0][k]); }
        }
      }
    };
    if (half) {
      unsigned xo[4], wo_[6];
#pragma unroll
      for (int q = 0; q < 4; ++q) xo[q] = (q * 8 + (lane >> 3)) * 896 + (((lane & 7) ^ ((q & 1) * 4 + (lane >> 4))) << 4);
      const char* xb = x + (size_t)(rt * 128 + c * 64) * 896;
      const char* wq = wqkv + g * 168 - woff;
      u64 epoch;
      asm volatile("global_load_dwordx2 %0, %1, %2 sc1" : "=v"(epoch) : "v"(0u), "s"(ws + WS_EPOCH) : "memory");
#define X_PIECES(B, D) do { glds<0>(xo[0], B, D); glds<1024>(xo[1], B, D); glds<2048>(xo[2], B, D); glds<3072>(xo[3], B, D); \
        glds<4096>(xo[0], (B) + 32 * 896, D); glds<5120>(xo[1], (B) + 32 * 896, D); glds<6144>(xo[2], (B) + 32 * 896, D); glds<7168>(xo[3], (B) + 32 * 896, D); } while (0)
#define W_PIECES(B, D) do { glds<0>(wo_[0], B, D); glds<1024>(wo_[1], B, D); glds<2048>(wo_[2], B, D); glds<3072>(wo_[3], B, D); glds<4096>(wo_[4], B, D); glds<5120>(wo_[5], B, D); } while (0)
      {
        const char* up = u + (size_t)(idx * 64 + 56 - 32 * c) * 896 + (has_b ? xcd * 128 : 0);
        const unsigned uo = (7 - (lane >> 3)) * 896, k4 = (lane & 7) ^ (4 * c);
        glds<0>(uo + (k4 << 4), up, L_UIMG + c * 4096); glds<1024>(uo + ((k4 ^ 1) << 4), up - 8 * 896, L_UIMG + c * 4096);
        glds<2048>(uo + ((k4 ^ 2) << 4), up - 16 * 896, L_UIMG + c * 4096); glds<3072>(uo + ((k4 ^ 3) << 4), up - 24 * 896, L_UIMG + c * 4096);
      }
      X_PIECES(xb, L_RING + c * 8192);
#pragma unroll
      for (int i = 0; i < 6; ++i) {
        const unsigned p = (6 * c + i) * 64 + lane, row = (p * 43691u) >> 19; unsigned e = p - row * 12; e = e > 10 ? 10 : e;
        wo_[i] = row * 2688 + e * 16;
      }
      W_PIECES(wq, L_RING + XCH_B + c * 6144);
      asm volatile("s_barrier" ::: "memory");
#define CHUNK(n, YOUNGER) do { if ((n) >= 4) lds_wait_ge(C_R + 4 * ((n) - 4), 4); \
        X_PIECES(xb + (n) * 128, L_RING + c * 8192 + ((n) & 3) * SLOT_B); W_PIECES(wq + (n) * 64 * 2688, L_RING + XCH_B + c * 6144 + ((n) & 3) * SLOT_B); \
        asm volatile("s_waitcnt vmcnt(" #YOUNGER ")" ::: "memory"); lds_arrive_nw(C_L + 4 * ((n) - 1)); } while (0)
      const int oct = 4 * c + ((lane >> 1) & 3);
      s16x8 uq[4];
#define U_READ(i0) do { _Pragma("unroll") for (int i = 0; i < 2; ++i) { const int ch = 2 * (((i0) + i) * 8 + (lane >> 3)) + (lane & 1); \
          const unsigned a = L_UIMG + oct * 1024 + (((ch >> 3) ^ oct) << 4) + (ch & 7) * 2; \
          _Pragma("unroll") for (int e = 0; e < 8; ++e) uq[(i0) + i][e] = lds_ld<short>(a + e * 128); } } while (0)
#define U_STORE(i0) do { _Pragma("unroll") for (int i = 0; i < 2; ++i) \
          st128(__builtin_bit_cast(u32x4, uq[(i0) + i]), wsr, ((xcd * 32 + ((i0) + i) * 8 + (lane >> 3)) * 32 + (31 - idx)) * 256 + c * 128 + (lane & 7) * 16, WS_UT); } while (0)
      CHUNK(1, 14);
      asm volatile("" : "+v"(epoch));
      tag = MAKE_TAG(epoch); asm volatile("" :: "s"(tag)); lds_arrive_nw(C_BTAG);
      STAMP(1);
      U_READ(0); U_READ(2);
      CHUNK(2, 14);
      U_STORE(0); U_STORE(2);
      CHUNK(3, 18);
      CHUNK(4, 14);
      {
        unsigned old = 0;
        if (lane == 0) old = __hip_atomic_fetch_add((LAS unsigned*)(uintptr_t)C_U, 1u, __ATOMIC_RELAXED, __HIP_MEMORY_SCOPE_WORKGROUP);
        if (__builtin_amdgcn_readfirstlane(old) == 1 && lane == 0) flag_store(ws, WS_UFLAG + (xcd * 32 + idx) * 128, tag);
      }
      CHUNK(5, 14);
      CHUNK(6, 14);
      pdt = *(const GAS unsigned short*)(logdt + d * 2);
      LOAD_P(p0, 256);
      asm volatile("s_waitcnt vmcnt(5)" ::: "memory");
      lds_arrive_nw(C_L + 24);
      STAMP(12);
      gather_addr();
#pragma unroll
      for (int i = 0; i < 4; ++i) { p0[i] <<= 16; p1[i] = 0u; }
      lds_wait_ge(C_R + 12, 4);
    } else {
      pdt = *(const GAS unsigned short*)(logdt + d * 2);
      LOAD_P(p0, 0); LOAD_P(p1, 256);
      const u64 epoch = __hip_atomic_load((GAS u64*)(ws + WS_EPOCH), __ATOMIC_RELAXED, __HIP_MEMORY_SCOPE_AGENT);
      asm volatile("s_barrier" ::: "memory");
      gather_addr();
      asm volatile("" : "+v"(pdt), "+v"(p0[0]), "+v"(p1[0]), "+v"(p1[1]), "+v"(p1[2]), "+v"(p1[3]));
      tag = MAKE_TAG(epoch); asm volatile("" :: "s"(tag)); lds_arrive_nw(C_BTAG);
#pragma unroll
      for (int i = 0; i < 4; ++i) p1[i] &= 0xffff0000u;
    }
    __builtin_amdgcn_s_setprio(1);
    STAMP(9);
    if (has_b) {
      const float dt = bf_round(fexp(bf_lo(pdt)));
      const unsigned cm = umax(umax(umax(p0[2] << 16 & 0x7fff0000u, p0[2] & 0x7fff0000u), umax(p0[3] << 16 & 0x7fff0000u, p0[3] & 0x7fff0000u)),
                               umax(p1[2] & 0x7fff0000u, p1[3] & 0x7fff0000u));
      unsigned eb = wave_umax(cm) >> 23; eb = eb < 1u ? 1u : (eb > 253u ? 253u : eb);
      const float csc = u2f((254u - eb) << 23), cinv = u2f(eb << 23);
      const unsigned tb_ = (half ? 3 * SLOT_B : 4 * SLOT_B) + c * TAB_W;
      const unsigned lw = tb_ + (lane >> 3) * 1056 + ((lane >> 2) & 1) * 528 + (lane & 3) * 4;
      const unsigned hw = tb_ + TAB_HI + (lane >> 3) * 672 + ((lane >> 2) & 1) * 336 + (lane & 3) * 4;
      const unsigned lr = tb_ + hh * 528 + r32 * 16, hr = tb_ + TAB_HI + hh * 336 + r32 * 16;
      f32x16 kd = f32x16{};
      u64 uflag = 0;
#pragma unroll 1
      for (int r = half ? 2 : 0; r < 3; ++r) {
        const float rho = -bf_round(fexp(bf_hi(p0[0]))) * dt * LOG2E, th = bf_hi(p0[1]) * dt * 0.15915494309189535f;
        f32x2 hA, hB; hA.x = bf_hi(p0[2]) * csc; hA.y = bf_hi(p0[3]) * -csc;
#pragma unroll
        for (int i = 0; i < 4; ++i) p0[i] = r == 0 ? p0[i] << 16 : p1[i];
        const f32x2 z8 = zpow(rho, th, 8.f), z32 = zpow(rho, th, 32.f), z1 = zpow(rho, -th, 1.f), z256 = zpow(rho, -th, 256.f);
        const f32x2 z16 = cmul(z8, z8), z24 = cmul(z8, z16);
        const unsigned q1 = pk_f16(z8.x, z8.y), q2 = pk_f16(z16.x, z16.y), q3 = pk_f16(z24.x, z24.y);
        lds_st<unsigned>(lw, 0x00003C00u); lds_st<unsigned>(lw + 16, q1); lds_st<unsigned>(lw + 32, q2); lds_st<unsigned>(lw + 48, q3);
        f32x2 e = z32;
#pragma unroll
        for (int a = 1; a < 8; ++a) {
          const unsigned p_ = pk_f16(e.x, e.y); unsigned m1, m2, m3;
          if (a < 7) e = cmul(e, z32);
          hmul3(p_, q1, q2, q3, m1, m2, m3);
          lds_st<unsigned>(lw + 64 * a, p_); lds_st<unsigned>(lw + 64 * a + 16, m1); lds_st<unsigned>(lw + 64 * a + 32, m2); lds_st<unsigned>(lw + 64 * a + 48, m3);
        }
        uflag = __hip_atomic_load((GAS u64*)(ws + WS_UFLAG + (xcd * 32 + r32) * 128), __ATOMIC_RELAXED, __HIP_MEMORY_SCOPE_AGENT);
#define PUTH(i, v) lds_st<unsigned>(hw + (i) * 16, pk_f16((v).x, (v).y))
        PUTH(0, hA); f32x2 g0 = cmul(hA, z256); PUTH(8, g0);
        hB = cmul(hA, z1); PUTH(1, hB); hA = cmul(hB, z1); PUTH(2, hA); { f32x2 t_ = cmul(hA, z256); PUTH(9, t_); }
        hB = cmul(hA, z1); PUTH(3, hB); hA = cmul(hB, z1); PUTH(4, hA); f32x2 g4 = cmul(hA, z256); PUTH(10, g4);
        hB = cmul(hA, z1); PUTH(5, hB); hA = cmul(hB, z1); PUTH(6, hA); { f32x2 t_ = cmul(hA, z256); PUTH(11, t_); }
        hB = cmul(hA, z1); PUTH(7, hB);
        g0 = cmul(g0, z256); PUTH(12, g0); g4 = cmul(g4, z256); PUTH(13, g4);
        g0 = cmul(g0, z256); PUTH(14, g0); g4 = cmul(g4, z256); PUTH(15, g4);
#pragma unroll
        for (int i = 16; i < 21; ++i) { g0 = cmul(g0, z256); PUTH(i, g0); }
        h16x8 fa[8], fb[8];
#pragma unroll
        for (int s = 0; s < 8; ++s) { fa[s] = lds_ld<h16x8>(hr + s * 672); fb[s] = lds_ld<h16x8>(lr + s * 1056); }
        SBAR();
#pragma unroll
        for (int s = 0; s < 8; ++s) kd = MFMA32H(fa[s], fb[s], kd);
      }
      STAMP(10);
      if (__builtin_amdgcn_ballot_w64(uflag != tag) != 0ull) flag_wait(ws, WS_UFLAG + (xcd * 32 + r32) * 128, tag);
      asm volatile("" ::: "memory");
      u32x4 uv[2];
#pragma unroll
      for (int i = 0; i < 2; ++i) uv[i] = __builtin_amdgcn_raw_buffer_load_b128(wsr, wb * 2048 + i * 1024 + lane * 16, WS_UT + (xcd * 32 + idx) * 8192, AUX_SC1);
      STAMP(11);
      {
        const float ks = cinv * 2.f * dt;
        const unsigned Tw = T + half * T_B;
        const unsigned i1 = Tw + (hh ? 384 + 2 * r32 : 256 + 4 * r32) * 4;
        const unsigned i2 = Tw + (hh ? (r32 == 0 ? 640 : 643) : 512 + r32) * 4, i3 = Tw + (hh ? 641 : 512 + r32) * 4, st3 = hh ? 0 : 128;
        lds_wait_ge(C_R + 24, 4);
        lds_st<f32x4>(Tw + (8 * r32 + 4 * hh) * 4, f32x4{kd[0] * ks, kd[1] * ks, kd[2] * ks, kd[3] * ks});
        lds_st<f32x2>(i1, f32x2{kd[4] * ks, kd[5] * ks}); lds_st<f32x2>(i1 + (hh ? 256 : 8), f32x2{kd[6] * ks, kd[7] * ks});
        lds_st<float>(i2, kd[8] * ks);
        lds_st<float>(i3 + st3, hh ? 0.f : kd[9] * ks); lds_st<float>(i3 + 2 * st3, hh ? 0.f : kd[10] * ks); lds_st<float>(i3 + 3 * st3, hh ? 0.f : kd[11] * ks);
        role_sync(C_B, 1);
      }
      STAMP(2);
      const unsigned cb = L_B + c * CH_B;
      {
        float P[2][12];
#pragma unroll
        for (int it = 0; it < 2; ++it)
#pragma unroll
          for (int k = 0; k < 11; ++k) P[it][k] = lds_ld<float>(ga[it][k]) + lds_ld<float>(ga[it][k] + T_B);
        const unsigned ub = cb + 4 * KC_S + half * UC_S;
        const u32x4 z4 = u32x4{0u, 0u, 0u, 0u};
        if (lane < 28) lds_st<u32x4>(ub + 4096 + lane * 16, z4);
        else if (lane < 34) lds_st<u32x4>(cb + (half * 2 + (lane - 28) / 3) * KC_S + ((lane - 28) % 3) * 16, z4);
        else if (lane == 34 && half) lds_st<u32x2>(ub + 4088, u32x2{0u, 0u});
#pragma unroll
        for (int it = 0; it < 2; ++it) {
          unsigned Q[5], A[5];
#pragma unroll
          for (int k = 0; k < 5; ++k) Q[k] = pk_bf16(P[it][2 * k], P[it][2 * k + 1]);
#pragma unroll
          for (int k = 0; k < 5; ++k) A[k] = pk_bf16(P[it][2 * k + 1], P[it][2 * k + 2]);
          const unsigned a = cb + (3 + it * 128 + half * 64 + lane) * 16;
          lds_st<u32x4>(a, u32x4{Q[0], Q[1], Q[2], Q[3]}); lds_st<u32x4>(a + KC_S, u32x4{A[0], A[1], A[2], A[3]});
          lds_st<u32x4>(a + 2 * KC_S, u32x4{Q[1], Q[2], Q[3], Q[4]}); lds_st<u32x4>(a + 3 * KC_S, u32x4{A[1], A[2], A[3], A[4]});
        }
      }
      STAMP(3);
#pragma unroll
      for (int i = 0; i < 2; ++i) {
        const int p = wb * 128 + i * 64 + lane;
        const unsigned a = L_B + (p & 1) * CH_B + 4 * KC_S + (p >> 1) * 16;
        lds_st<u32x4>(a, uv[i]); lds_st<u32x2>(a + UC_S - 8, u32x2{uv[i][0], uv[i][1]}); lds_st<u32x2>(a + UC_S, u32x2{uv[i][2], uv[i][3]});
      }
      role_sync(C_B, 2);
      __builtin_amdgcn_s_setprio(BCONV);
      STAMP(5);
      f32x4 y[8];
#pragma unroll
      for (int i = 0; i < 8; ++i) y[i] = f32x4{0.f, 0.f, 0.f, 0.f};
      {
        const int r16 = lane & 15, kq = lane >> 4;
        unsigned ka = cb + (r16 & 3) * KC_S + ((r16 >> 2) + kq) * 16 + half * 256;
        unsigned ua = cb + 4 * KC_S + (1 - (r16 & 1)) * UC_S + (2016 - 32 * (r16 >> 1) + 8 * kq) * 2 - 7 * 512 + half * 256;
        PIN(ka); PIN(ua);
        s16x8 af[2][8], bf[2][8];
#pragma unroll
        for (int m = 0; m < 8; ++m) { bf[0][7 - m] = lds_ld<s16x8>(ua + m * 512); af[0][m] = lds_ld<s16x8>(ka + m * 512); }
        auto conv_class = [&](auto sc, auto oc) __attribute__((always_inline)) NOPK {
          constexpr int S = decltype(sc)::value, O = decltype(oc)::value;
          int k = 0;
#pragma unroll
          for (int n = 7; n >= 0; --n)
#pragma unroll
            for (int m = 0; m + n < 8; ++m) {
              y[m + n] = MFMA16(af[S][m], bf[S][n], y[m + n]);
              if (k >= 4 && k < 36 && (k & 1) == 0) {
                const int q = (k - 4) >> 2;
                if (k & 2) af[1 - S][q] = lds_ld<s16x8>(ka + O + q * 512); else bf[1 - S][7 - q] = lds_ld<s16x8>(ua + O + q * 512);
              }
              SBAR();
              ++k;
            }
        };
#pragma unroll 1
        for (int it = 0; it < 2; ++it) {
          conv_class(IC<0>{}, IC<64>{});
          conv_class(IC<1>{}, IC<128>{});
          ka += 128; ua += 128;
        }
      }
      PIN(y[7]); STAMP(13);
      role_sync_slow(C_B, 3);
      __builtin_amdgcn_s_setprio(2);
#pragma unroll
      for (int i = 0; i < 4; ++i) lds_st<f32x4>(L_B + wb * 4096 + i * 1024 + lane * 16, half ? y[i] : y[4 + i]);
      role_sync_slow(C_B, 4);
      STAMP(14);
      {
        const int cc = lane & 15, kq = lane >> 4;
#pragma unroll
        for (int i = 0; i < 4; ++i) {
          const f32x4 v = lds_ld<f32x4>(L_B + (wb ^ 2) * 4096 + i * 1024 + lane * 16) + (half ? y[4 + i] : y[i]);
          const int t64 = 4 * (half * 4 + i) + (cc >> 2);
          __builtin_amdgcn_raw_buffer_store_b64(u32x2{pk_bf16(v[0], v[1]), pk_bf16(v[2], v[3])}, wsr,
                                                ((xcd * 32 + t64) * 64 + idx * 2 + c) * 128 + (32 * ((cc >> 1) & 1) + 8 * kq + 4 * (cc & 1)) * 2, WS_YT, AUX_SC1);
        }
      }
      lds_arrive(C_BFREE);
      __builtin_amdgcn_s_setprio(0);
      STAMP(15);
    } else {
      lds_arrive(C_BFREE);
      __builtin_amdgcn_s_setprio(0);
    }
    lds_wait_ge_slow(C_QR, 4);
#pragma unroll 1
    for (int i = wb; i < 14; i += 4) {
      const char* sb = wo + i * 32 * 896 + (idx & 3) * 224;
      const unsigned dst = L_RING + i * 7168;
#define WO_PIECE(k) glds<(k) * 1024>(wv[k], sb, dst); asm volatile("s_waitcnt vmcnt(2)" ::: "memory")
      WO_PIECE(0); WO_PIECE(1); WO_PIECE(2);
      if (i < 4 && has_b) {
        if (last_of_four(C_BPUB, lane, 0)) { if (lane == 0) flag_store(ws, WS_YFLAG + (xcd * 32 + idx) * 128, tag); }
        STAMP(6);
      }
      WO_PIECE(3); WO_PIECE(4); WO_PIECE(5); WO_PIECE(6);
    }
    drain_vm();
    lds_arrive(C_WO);
    STAMP(7);
    if (has_b) {
      const int tl = wb * 16 + (lane & 15);
      u32x4 uu[2], ds[2];
#pragma unroll
      for (int i = 0; i < 2; ++i) {
        const int oct = (lane >> 4) + 4 * i;
        uu[i] = *(const GAS u32x4*)(u + (size_t)(idx * 64 * 896 + xcd * 128) + (unsigned)(tl * 896 + oct * 16));
        ds[i] = *(const GAS u32x4*)(Dsk + xcd * 128 + (unsigned)(oct * 16));
      }
      if (wb == 0) flag_wait(ws, WS_YFLAG + (xcd * 32 + r32) * 128, tag);
      role_sync_idle(C_B, 5);
      u32x4 v[2];
#pragma unroll
      for (int i = 0; i < 2; ++i) v[i] = __builtin_amdgcn_raw_buffer_load_b128(wsr, wb * 2048 + i * 1024 + lane * 16, WS_YT + (xcd * 32 + idx) * 8192, AUX_SC1);
#pragma unroll
      for (int i = 0; i < 2; ++i) lds_st<u32x4>(L_YIMG + wb * 2048 + i * 1024 + lane * 16, v[i]);
      role_sync_idle(C_B, 6);
#pragma unroll
      for (int i = 0; i < 2; ++i) {
        const int oct = (lane >> 4) + 4 * i;
        const unsigned a = L_YIMG + (8 * oct + ((lane & 15) >> 2)) * 128 + (wb * 16 + (lane & 3) * 4) * 2;
        const u32x4 yy = __builtin_bit_cast(u32x4, cat8(tr_read(a), tr_read(a + 512)));
        u32x4 r;
#pragma unroll
        for (int k = 0; k < 4; ++k) {
          const unsigned sk = pk_bf16(bf_lo(uu[i][k]) * bf_lo(ds[i][k]), bf_hi(uu[i][k]) * bf_hi(ds[i][k]));
          r[k] = pk_bf16(bf_lo(yy[k]) + bf_lo(sk), bf_hi(yy[k]) + bf_hi(sk));
        }
        *(GAS u32x4*)(out + (size_t)(idx * 64 * 1792 + 896 + xcd * 128) + (unsigned)(tl * 1792 + oct * 16)) = r;
      }
      STAMP(8);
    } else if (b == 7 && wb == 0) {
#pragma unroll 1
      for (int i = 0; i < 4; ++i) rec_wait(wsr, WS_QFLAG + (i * 64 + lane) * 128, tag);
      if (lane == 0) flag_store(ws, WS_EPOCH, tag);
    }
    STAMP_FLUSH();
  }
}

extern "C" void kernel_launch(void* const* d_in, const int* in_sizes, int n_in, void* d_out, int out_size, void* d_ws, size_t ws_size,
                              hipStream_t stream) {
  (void)in_sizes; (void)n_in; (void)out_size; (void)ws_size;
  (void)hipFuncSetAttribute((const void*)fused_mha_s4d, hipFuncAttributeMaxDynamicSharedMemorySize, LDS_BYTES);
  fused_mha_s4d<<<256, 512, LDS_BYTES, stream>>>((const char*)d_in[0], (const char*)d_in[1], (const char*)d_in[2], (const char*)d_in[3],
                                                 (const char*)d_in[4], (const char*)d_in[5], (const char*)d_in[6], (const char*)d_in[7],
                                                 (const char*)d_in[8], (const char*)d_in[9], (char*)d_out,
                                                 (char*)d_ws + (WSEND ? ((ws_size - WS_END) & ~(size_t)0xfffff) : 0));
}
```

```cpp
#include <hip/hip_runtime.h>
#include <hip/hip_bf16.h>
#include <stdint.h>

typedef short s16x8 __attribute__((ext_vector_type(8)));
typedef short s16x4 __attribute__((ext_vector_type(4)));
typedef short s16x2 __attribute__((ext_vector_type(2)));
typedef _Float16 h16x8 __attribute__((ext_vector_type(8)));
typedef _Float16 h16x2 __attribute__((ext_vector_type(2)));
typedef __bf16 b16x2 __attribute__((ext_vector_type(2)));
typedef float f32x16 __attribute__((ext_vector_type(16)));
typedef float f32x4 __attribute__((ext_vector_type(4)));
typedef float f32x2 __attribute__((ext_vector_type(2)));
typedef unsigned u32x4 __attribute__((ext_vector_type(4)));
typedef unsigned u32x2 __attribute__((ext_vector_type(2)));
typedef unsigned long long u64;
#define LAS __attribute__((address_space(3)))
#define GAS __attribute__((address_space(1)))
#if defined(__HIP_DEVICE_COMPILE__)
#define NOPK __attribute__((target("no-packed-fp32-ops")))
#else
#define NOPK
#endif
#define DI NOPK __device__ __forceinline__
#define SBAR() __builtin_amdgcn_sched_barrier(0)
#define PIN(x) asm volatile("" : "+v"(x))
#define MFMA32(a, b, c) __builtin_amdgcn_mfma_f32_32x32x16_bf16((a), (b), (c), 0, 0, 0)
#define MFMA32H(a, b, c) __builtin_amdgcn_mfma_f32_32x32x16_f16((a), (b), (c), 0, 0, 0)
#define MFMA16(a, b, c) __builtin_amdgcn_mfma_f32_16x16x32_bf16((a), (b), (c), 0, 0, 0)
#define AUX_SC1 16

#define PREFETCH 1
#define WSEND 0
#define APRIO 3
#define INPF 0
#define BCONV 0
#define ROT 0
#define STAMPS 0

constexpr unsigned WS_EPOCH = 0;
constexpr unsigned WS_QFLAG = 4096;
constexpr unsigned WS_OFLAG = WS_QFLAG + 256 * 128;
constexpr unsigned WS_UFLAG = WS_OFLAG + 1024 * 128;
constexpr unsigned WS_YFLAG = WS_UFLAG + 256 * 128;
constexpr unsigned WS_QKV = 393216;
constexpr unsigned WS_KV = WS_QKV + 16 * 64 * 2048;
constexpr unsigned WS_O = WS_QKV + 48 * 64 * 2048;
constexpr unsigned WS_UT = WS_O + 64 * 16 * 2048;
constexpr unsigned WS_YT = WS_UT + 256 * 8192;
constexpr unsigned WS_DBG = WS_YT + 224 * 8192;
constexpr unsigned WS_END = WS_DBG + 256 * 8 * 16 * 8;
static_assert(WS_YFLAG + 256 * 128 <= WS_QKV, "ws map");

constexpr unsigned L_RING = 0, SLOT_B = 28672, XCH_B = 16384;
constexpr unsigned L_B = 100352;
constexpr unsigned TAB_W = 14000, TAB_HI = 8448;
constexpr unsigned L_UIMG = 4 * SLOT_B + 2 * TAB_W;
constexpr unsigned L_T = 2 * SLOT_B, T_B = 2816;
constexpr unsigned KC_S = 4160, UC_S = 4640, CH_B = 4 * KC_S + 2 * UC_S;
constexpr unsigned L_YIMG = L_B + 49152;
constexpr unsigned L_CTL = 163584;
constexpr unsigned LDS_BYTES = 163840;
constexpr unsigned C_A = L_CTL, C_B = L_CTL + 4, C_GO = L_CTL + 8, C_APUB = L_CTL + 12, C_BPUB = L_CTL + 16, C_WO = L_CTL + 20, C_BFREE = L_CTL + 24, C_BTAG = L_CTL + 28, C_U = L_CTL + 32, C_Q = L_CTL + 36, C_QR = L_CTL + 40, C_FB = L_CTL + 44, C_KM = L_CTL + 48, C_UI = L_CTL + 60;
constexpr unsigned C_L = L_CTL + 64, C_R = L_CTL + 96;
constexpr unsigned L_MAX = 162816;

constexpr unsigned L_SINK = L_MAX + 512;
constexpr unsigned SPIN_CAP = 1u << 22;
constexpr float LOG2E = 1.4426950408889634f;
constexpr float QSCALE = 0.18898223650461362f * LOG2E;
constexpr float SAFE_BOUND = 48.0f;

template <int V> struct IC { static constexpr int value = V; };
DI unsigned f2u(float f) { return __builtin_bit_cast(unsigned, f); }
DI float u2f(unsigned u) { return __builtin_bit_cast(float, u); }
DI unsigned umax(unsigned a, unsigned b) { return a > b ? a : b; }
DI unsigned sel(unsigned a, unsigned b, u64 mask) { return __builtin_amdgcn_inverse_ballot_w64(mask) ? b : a; }
DI float fexp(float x) { return __builtin_amdgcn_exp2f(x * LOG2E); }
DI float bf_lo(unsigned v) { return u2f(v << 16); }
DI float bf_hi(unsigned v) { return u2f(v & 0xffff0000u); }
DI unsigned pk_bf16(float a, float b) { f32x2 v = {a, b}; return __builtin_bit_cast(unsigned, __builtin_convertvector(v, b16x2)); }
DI unsigned pk_f16(float a, float b) { f32x2 v = {a, b}; return __builtin_bit_cast(unsigned, __builtin_convertvector(v, h16x2)); }
DI float bf_round(float a) { return bf_lo(pk_bf16(a, a)); }
template <int OFF> DI void glds(unsigned voff, const char* sbase, unsigned lds) {
  asm volatile("s_add_u32 m0, %2, %3\n\ts_nop 0\n\tglobal_load_lds_dwordx4 %0, %1" :: "v"(voff), "s"(sbase), "s"(lds), "i"(OFF) : "memory", "scc");
}
DI float half_swap_add(float v) {
  auto r = __builtin_amdgcn_permlane32_swap(f2u(v), f2u(v), false, false);
  return u2f(r[0]) + u2f(r[1]);
}
template <int OFF> DI void glds_dev(unsigned voff, const char* sbase, unsigned lds) {
  asm volatile("s_add_u32 m0, %2, %3\n\ts_nop 0\n\tglobal_load_lds_dwordx4 %0, %1 sc1" :: "v"(voff), "s"(sbase), "s"(lds), "i"(OFF) : "memory", "scc");
}
DI float dot2(unsigned a, float c) { return __builtin_amdgcn_fdot2_f32_bf16(__builtin_bit_cast(b16x2, a), __builtin_bit_cast(b16x2, a), c, false); }
DI float frag_norm2(u32x4 f0, u32x4 f1, int hh) {
  float a = dot2(f0[0], 0.f), b = dot2(f0[1], 0.f), c = dot2(f1[2], 0.f);
  a = dot2(f0[2], a); b = dot2(f0[3], b); c = dot2(f1[3], c); a = dot2(f1[0], a); b = dot2(f1[1], b);
  return half_swap_add(a + b + (hh ? 0.f : c));
}
DI s16x4 tr_read(unsigned a) { return __builtin_bit_cast(s16x4, __builtin_amdgcn_ds_read_tr16_b64_v4i16((LAS s16x4*)(uintptr_t)a)); }
DI s16x8 cat8(s16x4 lo, s16x4 hi) { return __builtin_shufflevector(lo, hi, 0, 1, 2, 3, 4, 5, 6, 7); }
template <class T> DI T lds_ld(unsigned a) { return *(LAS T*)(uintptr_t)a; }
template <class T> DI void lds_st(unsigned a, T v) { *(LAS T*)(uintptr_t)a = v; }
#define LDS_POLL(V, A, S, N, T, EXTRA) "s_mov_b32 " N ", 0\n1:\n\t" EXTRA "ds_read_b32 " V ", " A "\n\ts_add_u32 " N ", " N ", 1024\n\t" \
  "s_waitcnt lgkmcnt(0)\n\tv_readfirstlane_b32 " S ", " V "\n\ts_cbranch_scc1 4f\n\ts_cmp_lt_u32 " S ", " T "\n\ts_cbranch_scc1 1b\n4:\n\t"
DI unsigned lds_wait_ge(unsigned a, unsigned target) {
  unsigned v, sv, n;
  asm volatile(LDS_POLL("%0", "%3", "%1", "%2", "%4", "") : "=&v"(v), "=&s"(sv), "=&s"(n) : "v"(a), "s"(target) : "memory", "scc");
  return sv;
}
DI unsigned lds_wait_ge_slow(unsigned a, unsigned target) {
  unsigned v, sv, n;
  asm volatile(LDS_POLL("%0", "%3", "%1", "%2", "%4", "s_sleep 1\n\t") : "=&v"(v), "=&s"(sv), "=&s"(n) : "v"(a), "s"(target) : "memory", "scc");
  return sv;
}
DI void lds_arrive(unsigned a) {
  asm volatile("s_waitcnt lgkmcnt(0)\n\ts_mov_b64 exec, 1\n\tds_add_u32 %0, %1\n\ts_mov_b64 exec, -1" :: "v"(a), "v"(1u) : "memory");
}
DI void lds_arrive_nw(unsigned a) {
  asm volatile("s_mov_b64 exec, 1\n\tds_add_u32 %0, %1\n\ts_mov_b64 exec, -1" :: "v"(a), "v"(1u) : "memory");
}
DI void role_sync(unsigned a, unsigned nth) { lds_arrive(a); lds_wait_ge(a, nth * 4u); }
DI void role_sync_slow(unsigned a, unsigned nth) { lds_arrive(a); lds_wait_ge_slow(a, nth * 4u); }
DI void role_sync_idle(unsigned a, unsigned nth) {
  lds_arrive(a);
  unsigned v, sv, n;
  asm volatile(LDS_POLL("%0", "%3", "%1", "%2", "%4", "s_sleep 6\n\t") : "=&v"(v), "=&s"(sv), "=&s"(n) : "v"(a), "s"(nth * 4u) : "memory", "scc");
}
DI bool last_of_four(unsigned a, int lane, unsigned round) {
  unsigned old = 0;
  if (lane == 0) old = __hip_atomic_fetch_add((LAS unsigned*)(uintptr_t)a, 1u, __ATOMIC_RELAXED, __HIP_MEMORY_SCOPE_WORKGROUP);
  return (unsigned)__builtin_amdgcn_readfirstlane(old) == round * 4u + 3u;
}
DI void st128(u32x4 v, __amdgpu_buffer_rsrc_t r, unsigned voff, unsigned soff) {
  __builtin_amdgcn_raw_buffer_store_b128(v, r, voff, soff, AUX_SC1);
  asm volatile("s_nop 1" :: "v"(v) : "memory");
}
DI void drain_vm() { asm volatile("s_waitcnt vmcnt(0)" ::: "memory"); }
DI void flag_store(char* ws, unsigned off, u64 tag) { __hip_atomic_store((GAS u64*)(ws + off), tag, __ATOMIC_RELAXED, __HIP_MEMORY_SCOPE_AGENT); }
DI void flag_wait(char* ws, unsigned off, u64 tag) {
#pragma unroll 1
  for (unsigned n = 0; n < SPIN_CAP; ++n) {
    u64 v = __hip_atomic_load((GAS u64*)(ws + off), __ATOMIC_RELAXED, __HIP_MEMORY_SCOPE_AGENT);
    if (__builtin_amdgcn_ballot_w64(v != tag) == 0ull) break;
    __builtin_amdgcn_s_sleep(2);
  }
  asm volatile("" ::: "memory");
}
DI u32x4 rec_wait(__amdgpu_buffer_rsrc_t r, unsigned off, u64 tag) {
  u32x4 v;
#pragma unroll 1
  for (unsigned n = 0; n < SPIN_CAP; ++n) {
    v = __builtin_amdgcn_raw_buffer_load_b128(r, off, 0, AUX_SC1);
    if (__builtin_amdgcn_ballot_w64(v[0] != (unsigned)tag || v[2] != (unsigned)(tag >> 32)) == 0ull) break;
    asm volatile("s_sleep 1" ::: "memory");
  }
  asm volatile("" ::: "memory");
  return v;
}
template <int CTRL> DI unsigned dpp_max(unsigned v) { unsigned t = __builtin_amdgcn_update_dpp(0u, v, CTRL, 0xf, 0xf, false); return v > t ? v : t; }
DI unsigned wave_umax(unsigned v) {
  v = dpp_max<0x111>(v); v = dpp_max<0x112>(v); v = dpp_max<0x114>(v); v = dpp_max<0x118>(v);
  v = dpp_max<0x142>(v); v = dpp_max<0x143>(v);
  return __builtin_amdgcn_readlane(v, 63);
}
DI float half_swap_max(float v) {
  auto r = __builtin_amdgcn_permlane32_swap(f2u(v), f2u(v), false, false);
  return __builtin_fmaxf(u2f(r[0]), u2f(r[1]));
}
DI f32x2 cmul(f32x2 a, f32x2 b) { f32x2 r; r.x = __builtin_fmaf(-a.y, b.y, a.x * b.x); r.y = __builtin_fmaf(a.y, b.x, a.x * b.y); return r; }
DI void hmul3(unsigned a, unsigned b1, unsigned b2, unsigned b3, unsigned& r1, unsigned& r2, unsigned& r3) {
  asm("v_pk_mul_f16 %0, %3, %4 op_sel:[0,0] op_sel_hi:[0,1]\n\tv_pk_mul_f16 %1, %3, %5 op_sel:[0,0] op_sel_hi:[0,1]\n\tv_pk_mul_f16 %2, %3, %6 op_sel:[0,0] op_sel_hi:[0,1]\n\t"
      "v_pk_fma_f16 %0, %3, %4, %0 op_sel:[1,1,0] op_sel_hi:[1,0,1] neg_lo:[1,0,0]\n\tv_pk_fma_f16 %1, %3, %5, %1 op_sel:[1,1,0] op_sel_hi:[1,0,1] neg_lo:[1,0,0]\n\t"
      "v_pk_fma_f16 %2, %3, %6, %2 op_sel:[1,1,0] op_sel_hi:[1,0,1] neg_lo:[1,0,0]"
      : "=&v"(r1), "=&v"(r2), "=&v"(r3) : "v"(a), "v"(b1), "v"(b2), "v"(b3));
}
DI f32x2 zpow(float rho, float th, float p) {
  const float e = __builtin_amdgcn_exp2f(rho * p), f = __builtin_amdgcn_fractf(th * p);
  f32x2 r; r.x = e * __builtin_amdgcn_cosf(f); r.y = e * __builtin_amdgcn_sinf(f); return r;
}
#define STAMP(k) do { } while (0)
#define STAMP_FLUSH() do { } while (0)

#define QR(a, n) "v[" #a "+" #n ":" #a "+" #n "+1]"
#define Q_READ(F, XA, S) "ds_read_b128 v[" #F ":" #F "+3], " XA "\n\t" \
  "ds_read_b64_tr_b16 " QR(F, 4) ", %[wa] offset:" #S "*3072\n\tds_read_b64_tr_b16 " QR(F, 6) ", %[wa] offset:" #S "*3072+768\n\t" \
  "ds_read_b64_tr_b16 " QR(F, 8) ", %[wa] offset:" #S "*3072+56\n\tds_read_b64_tr_b16 " QR(F, 10) ", %[wa] offset:" #S "*3072+824\n\t" \
  "ds_read_b64_tr_b16 " QR(F, 12) ", %[wa] offset:" #S "*3072+112\n\tds_read_b64_tr_b16 " QR(F, 14) ", %[wa] offset:" #S "*3072+880\n\t"
#define Q_T(ACC, F, n) "v_mfma_f32_32x32x16_bf16 " ACC ", v[" #F "+" #n ":" #F "+" #n "+3], v[" #F ":" #F "+3], " ACC "\n\t"
#define Q_N(ACC, F, n) "v_mfma_f32_32x32x16_bf16 " ACC ", v[" #F ":" #F "+3], v[" #F "+" #n ":" #F "+" #n "+3], " ACC "\n\t"
#define Q_MMA(F, M0, M1, M2) M0("%[a0]", F, 4) M1("%[a1]", F, 8) M2("%[a2]", F, 12)
#define Q_ARRIVE "s_mov_b64 exec, 1\n\tds_add_u32 %[ca], %[one]\n\ts_mov_b64 exec, -1\n\t"
#define Q_TR(F, n, S, O) "ds_read_b64_tr_b16 " QR(F, n) ", %[wa] offset:" #S "*3072+" #O "\n\t"
#define Q_GROUP(G, F, XA, S, M0, M1, M2) \
  M0("%[a0]", G, 4) "ds_read_b128 v[" #F ":" #F "+3], " XA "\n\t" Q_TR(F, 4, S, 0) Q_TR(F, 6, S, 768) \
  M1("%[a1]", G, 8) Q_TR(F, 8, S, 56) Q_TR(F, 10, S, 824) \
  M2("%[a2]", G, 12) Q_TR(F, 12, S, 112) Q_TR(F, 14, S, 880)
#define QKV_BODY(M0, M1, M2) \
  "2:\n\t" \
  "v_add_u32 %[cl], 4, %[cl]\n\ts_waitcnt lgkmcnt(7)\n\t" \
  Q_GROUP(100, 132, "%[x2]", 2, M0, M1, M2) "ds_read_b32 %[tv], %[cl]\n\t" \
  "s_waitcnt lgkmcnt(8)\n\t" \
  Q_GROUP(116, 148, "%[x3]", 3, M0, M1, M2) \
  "s_cmp_eq_u32 %[tg], 28\n\ts_cbranch_scc1 5f\n\t" \
  "s_cmp_eq_u32 %[tg], 16\n\ts_cselect_b32 %[ts], %[wrap], %[step]\n\ts_add_u32 %[tg], %[tg], 4\n\t" \
  "v_add_u32 %[x0], %[ts], %[x0]\n\tv_add_u32 %[x1], %[ts], %[x1]\n\tv_add_u32 %[x2], %[ts], %[x2]\n\tv_add_u32 %[x3], %[ts], %[x3]\n\tv_add_u32 %[wa], %[ts], %[wa]\n\t" \
  "s_waitcnt lgkmcnt(7)\n\tv_readfirstlane_b32 %[ts], %[tv]\n\ts_cmp_ge_u32 %[ts], 2\n\ts_cbranch_scc1 0f\n\t" \
  LDS_POLL("%[tv]", "%[cl]", "%[ts]", "%[tn]", "2", "") \
  "0:\n\t" \
  Q_GROUP(132, 100, "%[x0]", 0, M0, M1, M2) \
  "s_cmp_lt_u32 %[tg], 16\n\ts_cbranch_scc1 3f\n\ts_waitcnt vmcnt(0)\n3:\n\t" \
  "s_waitcnt lgkmcnt(7)\n\t" Q_ARRIVE \
  Q_GROUP(148, 116, "%[x1]", 1, M0, M1, M2) \
  "v_add_u32 %[ca], 4, %[ca]\n\ts_branch 2b\n" \
  "5:\n\t" \
  "s_waitcnt lgkmcnt(7)\n\t" Q_MMA(132, M0, M1, M2) "s_waitcnt lgkmcnt(0)\n\t" Q_ARRIVE "s_nop 0\n\t" Q_MMA(148, M0, M1, M2)
#define QKV_LOOP() asm volatile( \
  "s_mov_b32 %[tg], 4\n\t" LDS_POLL("%[tv]", "%[cl]", "%[ts]", "%[tn]", "2", "") \
  Q_READ(100, "%[x0]", 0) Q_READ(116, "%[x1]", 1) \
  "s_cmp_gt_u32 %[g], 10\n\ts_cbranch_scc1 7f\n\ts_cmp_eq_u32 %[g], 10\n\ts_cbranch_scc1 6f\n" \
  QKV_BODY(Q_T, Q_T, Q_T) "s_branch 8f\n6:\n" QKV_BODY(Q_T, Q_T, Q_N) "s_branch 8f\n7:\n" QKV_BODY(Q_N, Q_N, Q_N) \
  "8:\n\ts_nop 7\n\ts_nop 7" \
  : [a0] "+v"(acc[0]), [a1] "+v"(acc[1]), [a2] "+v"(acc[2]), [x0] "+v"(xa0), [x1] "+v"(xa1), [x2] "+v"(xa2), [x3] "+v"(xa3), [wa] "+v"(wa), \
    [tv] "=&v"(tv), [ts] "=&s"(ts), [tn] "=&s"(tn), [tg] "=&s"(tg), [ca] "+v"(car), [cl] "+v"(cla) \
  : [one] "v"(1u), [step] "s"(SLOT_B), [wrap] "s"(0u - 3u * SLOT_B), [g] "s"(g) \
  : "memory", "scc", V100_163)
#define V10(p) "v" #p "0", "v" #p "1", "v" #p "2", "v" #p "3", "v" #p "4", "v" #p "5", "v" #p "6", "v" #p "7", "v" #p "8", "v" #p "9"
#define V100_163 V10(10), V10(11), V10(12), V10(13), V10(14), V10(15), "v160", "v161", "v162", "v163"

NOPK __global__ void __launch_bounds__(512, 2)
fused_mha_s4d(const char* __restrict__ x, const char* __restrict__ wqkv, const char* __restrict__ wo, const char* __restrict__ u,
              const char* __restrict__ logAr, const char* __restrict__ Aim, const char* __restrict__ Cre, const char* __restrict__ Cim,
              const char* __restrict__ logdt, const char* __restrict__ Dsk, char* __restrict__ out, char* __restrict__ ws) {
  extern __shared__ __attribute__((aligned(16))) char smem[];
  const int tid = __builtin_amdgcn_workitem_id_x(), lane = tid & 63;
  const int wid = __builtin_amdgcn_readfirstlane(tid >> 6);
  const int b = __builtin_amdgcn_workgroup_id_x(), xcd = b & 7, idx = b >> 3;
  const int r32 = lane & 31, hh = lane >> 5;
  const unsigned L0 = (unsigned)(uintptr_t)smem;
  (void)L0;
  const __amdgpu_buffer_rsrc_t wsr = __builtin_amdgcn_make_buffer_rsrc(ws, 0, WS_END, 0x00020000);
  asm volatile("" :: "s"(x), "s"(wqkv), "s"(wo), "s"(u), "s"(logAr), "s"(Aim), "s"(Cre), "s"(Cim), "s"(logdt), "s"(Dsk), "s"(out), "s"(ws));
  STAMP(0);
  asm volatile(".Lpf_begin:");
#define MAKE_TAG(e) (((u64)(unsigned)__builtin_amdgcn_readfirstlane((unsigned)((e) >> 32)) << 32 | (unsigned)__builtin_amdgcn_readfirstlane((unsigned)(e))) + 0x9E3779B97F4A7C15ull)
  const int rt = (xcd & 3) * 4 + (idx >> 3), g = (xcd >> 2) * 8 + (idx & 7);
  const int woff = (g == 15) ? 8 : 0;
  const bool has_b = xcd < 7;

  if (__builtin_expect(wid >= 4, 0)) {
    const int w = wid - 4;
    __builtin_amdgcn_s_setprio(APRIO);
    const u64 epoch = __hip_atomic_load((GAS u64*)(ws + WS_EPOCH), __ATOMIC_RELAXED, __HIP_MEMORY_SCOPE_AGENT);
    {
      u64 pc; unsigned below, above;
      asm volatile("s_getpc_b64 %0\n.Lpf_here:\n\ts_mov_b32 %1, .Lpf_here-.Lpf_begin\n\ts_mov_b32 %2, .Lpf_end-.Lpf_here" : "=s"(pc), "=s"(below), "=s"(above));
      const unsigned off = ((w * 64 + lane) * 8 + (idx & 7)) * 128;
      if (off < below + above) asm volatile("s_mov_b32 m0, %2\n\ts_nop 0\n\tglobal_load_lds_dword %0, %1" :: "v"(off), "s"(pc - below), "s"(L_SINK) : "memory");
    }
    if (w == 0) lds_st<u32x4>(L_MAX + lane * 16, u32x4{0u, 0u, 0u, 0u});
    asm volatile("s_waitcnt lgkmcnt(0)\n\ts_barrier" ::: "memory");
    if (has_b) {
      lds_wait_ge(C_UI, 2);
      const int oct = (lane >> 1) & 7;
      s16x8 v[2];
#pragma unroll
      for (int i = 0; i < 2; ++i) {
        const int ch = 2 * (w * 8 + i * 4 + (lane >> 4)) + (lane & 1);
        const unsigned a = L_UIMG + oct * 1024 + (((ch >> 3) ^ oct) << 4) + (ch & 7) * 2;
#pragma unroll
        for (int e = 0; e < 8; ++e) v[i][e] = lds_ld<short>(a + e * 128);
      }
#pragma unroll
      for (int i = 0; i < 2; ++i)
        st128(__builtin_bit_cast(u32x4, v[i]), wsr, ((xcd * 32 + w * 8 + i * 4 + (lane >> 4)) * 32 + (31 - idx)) * 256 + (lane & 15) * 16, WS_UT);
    }
    STAMP(9);
    const int rt32 = rt * 4 + w;
    unsigned l16 = lane * 16, so[3], kma[3], vcst = r32 == 28 ? 0x3F803F80u : 0u; unsigned scl[3]; u64 vmask[3], pmask[3], qmask[3];
#pragma unroll
    for (int j = 0; j < 3; ++j) {
      const int sl = 3 * g + j, ty = sl >> 4;
      so[j] = ty == 0 ? WS_QKV + (sl * 64 + rt32) * 2048 : WS_KV + ((sl & 15) * 64 + rt32) * 4096 + (ty - 1) * 2048;
      scl[j] = ty == 0 ? __builtin_bit_cast(unsigned, QSCALE) : 0x3F800000u;
      const unsigned mv = __builtin_amdgcn_readfirstlane(ty == 2 ? 0xF0000000u : 0u), mp = __builtin_amdgcn_readfirstlane(ty < 2 ? ~0u : 0u), mq = __builtin_amdgcn_readfirstlane(ty == 0 ? ~0u : 0u);
      vmask[j] = (u64)mv << 32 | mv; pmask[j] = (u64)mp << 32; qmask[j] = (u64)mq << 32;
      kma[j] = C_KM + j * 4;
      asm volatile("" : "+s"(so[j]), "+s"(scl[j]), "+s"(vmask[j]), "+s"(pmask[j]), "+s"(qmask[j]), "+v"(kma[j]));
    }
    PIN(l16); PIN(vcst);
    f32x16 acc[3];
#pragma unroll
    for (int j = 0; j < 3; ++j) acc[j] = f32x16{};
    {
      const unsigned xa_l = w * 4096 + r32 * 128, xsw = (r32 >> 1) & 7;
      unsigned xa0 = xa_l + (((0 + hh) ^ xsw) << 4), xa1 = xa_l + (((2 + hh) ^ xsw) << 4), xa2 = xa0 ^ 64, xa3 = xa1 ^ 64;
      unsigned wa = XCH_B + (8 * hh + ((lane & 15) >> 2)) * 192 + woff + ((lane >> 4) & 1) * 32 + (lane & 3) * 8;
      unsigned tv, ts, tn, tg, car = C_R, cla = C_L;
      QKV_LOOP();
    }
    STAMP(2);
#pragma unroll
    for (int j = 0; j < 3; ++j) {
      u32x4 f0, f1;
#pragma unroll
      for (int i = 0; i < 4; ++i) f0[i] = sel(pk_bf16(acc[j][2 * i] * u2f(scl[j]), acc[j][2 * i + 1] * u2f(scl[j])), vcst, vmask[j]);
      st128(f0, wsr, l16, so[j]);
#pragma unroll
      for (int i = 0; i < 4; ++i) f1[i] = sel(pk_bf16(acc[j][8 + 2 * i] * u2f(scl[j]), acc[j][9 + 2 * i] * u2f(scl[j])), vcst, vmask[j]);
      const unsigned n2 = f2u(frag_norm2(f0, f1, hh));
      asm volatile("ds_max_u32 %0, %1" :: "v"(kma[j]), "v"(n2) : "memory");
      f1[2] = sel(f1[2], sel(0x3F803F80u, n2, qmask[j]), pmask[j]); f1[3] = sel(f1[3], 0x00003F80u, pmask[j]);
      st128(f1, wsr, l16 + 1024, so[j]);
    }
    const u64 tag = MAKE_TAG(epoch);
    STAMP(14);
    const int h = xcd * 2 + (idx >> 4), qt = idx & 15;
    f32x16 o[4];
#pragma unroll
    for (int i = 0; i < 4; ++i) { o[i] = f32x16{}; PIN(o[i]); }
    if (w == 3) lds_wait_ge(C_BTAG, 4);
    drain_vm();
    STAMP(15);
    if (w == 3) {
      lds_wait_ge(C_APUB, 3);
      const u32x4 km = lds_ld<u32x4>(C_KM);
      const u32x4 rec = {(unsigned)tag, ((km[0] + 0xffffu) >> 16) | ((km[1] + 0xffffu) & 0xffff0000u), (unsigned)(tag >> 32), km[2] + 0xffffu};
      if (lane == 0) st128(rec, wsr, (g * 16 + rt) * 128, WS_QFLAG);
    } else {
      lds_arrive_nw(C_APUB);
      if (w == 0) {
        rec_wait(wsr, WS_QFLAG + ((h / 3) * 16 + qt) * 128, tag);
        lds_wait_ge(C_R + 24, 4);
        const char* qs = ws + WS_QKV + (h * 64 + qt * 4) * 2048;
        glds_dev<0>(l16, qs, L_RING); glds_dev<1024>(l16, qs + 1024, L_RING); glds_dev<2048>(l16, qs + 2048, L_RING); glds_dev<3072>(l16, qs + 3072, L_RING);
        glds_dev<4096>(l16, qs + 4096, L_RING); glds_dev<5120>(l16, qs + 5120, L_RING); glds_dev<6144>(l16, qs + 6144, L_RING); glds_dev<7168>(l16, qs + 7168, L_RING);
        drain_vm();
        lds_arrive_nw(C_Q);
      } else if (w == 1) {
        const u32x4 rec = rec_wait(wsr, WS_QFLAG + ((hh ? (32 + h) / 3 : (16 + h) / 3) * 16 + (lane & 15)) * 128, tag);
        const int j = (16 + h) % 3;
        unsigned v = j == 0 ? rec[1] << 16 : ((j == 1 ? rec[1] : rec[3]) & 0xffff0000u);
        v = dpp_max<0x111>(v); v = dpp_max<0x112>(v); v = dpp_max<0x114>(v); v = dpp_max<0x118>(v);
        lds_st<unsigned>(C_GO, __builtin_amdgcn_readlane(v, 15) + 1u);
      }
    }
    STAMP(3);
    s16x8 qf[4][2], kf[4][2], vf[4][2];
    const unsigned kbase = WS_KV + (h * 64 + w * 16) * 4096;
#define LDR(rs_, soff, voff) __builtin_bit_cast(s16x8, __builtin_amdgcn_raw_buffer_load_b128(rs_, l16 + (voff), (soff), AUX_SC1))
#define LDF(soff, voff) LDR(wsr, soff, voff)
#define LDKV(rs_, B, soff) do { SBAR(); kf[B][0] = LDR(rs_, soff, 0); kf[B][1] = LDR(rs_, soff, 1024); vf[B][0] = LDR(rs_, soff, 2048); vf[B][1] = LDR(rs_, soff, 3072); SBAR(); } while (0)
    lds_wait_ge_slow(C_Q, 1);
    STAMP(1);
#pragma unroll
    for (int i = 0; i < 4; ++i) { const unsigned a = L_RING + ((w + 1 + i) & 3) * 2048 + l16; qf[i][0] = lds_ld<s16x8>(a); qf[i][1] = lds_ld<s16x8>(a + 1024); }
    lds_arrive(C_QR);
    const unsigned km2 = lds_wait_ge_slow(C_GO, 1);
    STAMP(4);
    LDKV(wsr, 0, kbase);
    LDKV(wsr, 1, kbase + 4096); LDKV(wsr, 2, kbase + 8192);
    {
      const float kmax2 = u2f(km2) * 1.02f;
      unsigned pad2[4], pad3[4]; bool unsafe = false;
#pragma unroll
      for (int i = 0; i < 4; ++i) {
        const float m = __builtin_amdgcn_sqrtf(u2f(__builtin_bit_cast(u32x4, qf[i][1])[2]) * kmax2);
        unsafe |= hh && !(m <= SAFE_BOUND);
        pad2[i] = pk_bf16(-m, 0.f); pad3[i] = 0u;
      }
      if (__builtin_expect(__builtin_amdgcn_ballot_w64(unsafe) != 0ull, 0)) {
        float mx[4];
#pragma unroll
        for (int i = 0; i < 4; ++i) { mx[i] = -INFINITY; if (hh) { u32x4 t_ = __builtin_bit_cast(u32x4, qf[i][1]); t_[2] = 0u; t_[3] = 0u; qf[i][1] = __builtin_bit_cast(s16x8, t_); } }
#pragma unroll 1
        for (int t = 0; t < 16; ++t) {
          const s16x8 k0 = LDF(kbase + t * 4096, 0), k1 = LDF(kbase + t * 4096, 1024);
#pragma unroll
          for (int i = 0; i < 4; ++i) {
            f32x16 s = MFMA32(k0, qf[i][0], f32x16{}); s = MFMA32(k1, qf[i][1], s);
#pragma unroll
            for (int e = 0; e < 16; ++e) mx[i] = __builtin_fmaxf(mx[i], s[e]);
          }
        }
#pragma unroll
        for (int i = 0; i < 4; ++i) {
          const float v = half_swap_max(mx[i]);
          const unsigned k_ = f2u(v) ^ ((unsigned)((int)f2u(v) >> 31) | 0x80000000u);
          __hip_atomic_fetch_max((LAS unsigned*)(uintptr_t)(L_MAX + (((w + 1 + i) & 3) * 32 + r32) * 4), k_, __ATOMIC_RELAXED, __HIP_MEMORY_SCOPE_WORKGROUP);
        }
        role_sync(C_FB, 1);
#pragma unroll
        for (int i = 0; i < 4; ++i) {
          const unsigned k_ = lds_ld<unsigned>(L_MAX + (((w + 1 + i) & 3) * 32 + r32) * 4);
          const float m = u2f(k_ ^ ((unsigned)((int)~k_ >> 31) | 0x80000000u));
          const float m0 = bf_round(-m), r1 = -m - m0, m1 = bf_round(r1);
          pad2[i] = pk_bf16(m0, m1); pad3[i] = pk_bf16(r1 - m1, 0.f);
        }
      }
#pragma unroll
      for (int i = 0; i < 4; ++i) {
        u32x4 t_ = __builtin_bit_cast(u32x4, qf[i][1]);
        t_[2] = hh ? pad2[i] : t_[2]; t_[3] = hh ? pad3[i] : t_[3]; qf[i][1] = __builtin_bit_cast(s16x8, t_);
      }
    }
    f32x16 sA, sB; u32x4 p0, p1 = u32x4{0u, 0u, 0u, 0u};
    sA = MFMA32(kf[0][0], qf[0][0], f32x16{}); sA = MFMA32(kf[0][1], qf[0][1], sA);
    __builtin_amdgcn_s_setprio(1);
    STAMP(5);
    unsigned redw = L_B + l16; PIN(redw);
#define EX(S, i) S[i] = __builtin_amdgcn_exp2f(S[i])
#define PK(S, i) pk_bf16(S[i], S[(i) + 1])
#define BF(P) __builtin_bit_cast(s16x8, P)
#define STEP(C, N, KN, QN, VP, OP, VC, OC) do { \
      N = MFMA32(KN[0], QN[0], f32x16{}); SBAR(); EX(C, 0); EX(C, 1); EX(C, 2); EX(C, 3); EX(C, 4); SBAR(); \
      OP = MFMA32(VP[1], BF(p1), OP); SBAR(); EX(C, 5); EX(C, 6); EX(C, 7); p0[0] = PK(C, 0); p0[1] = PK(C, 2); p0[2] = PK(C, 4); p0[3] = PK(C, 6); PIN(p0); SBAR(); \
      N = MFMA32(KN[1], QN[1], N); SBAR(); EX(C, 8); EX(C, 9); EX(C, 10); EX(C, 11); EX(C, 12); SBAR(); \
      OC = MFMA32(VC[0], BF(p0), OC); SBAR(); EX(C, 13); EX(C, 14); EX(C, 15); p1[0] = PK(C, 8); p1[1] = PK(C, 10); p1[2] = PK(C, 12); p1[3] = PK(C, 14); PIN(p1); SBAR(); \
    } while (0)
#define RED_WRITE(i) do { const unsigned a_ = redw + (((w + 1 + (i)) & 3) * 3 + (2 - (i))) * 4096; \
      _Pragma("unroll") for (int q_ = 0; q_ < 4; ++q_) lds_st<f32x4>(a_ + q_ * 1024, f32x4{o[i][4 * q_], o[i][4 * q_ + 1], o[i][4 * q_ + 2], o[i][4 * q_ + 3]}); } while (0)
#define TILE(T, TP, TN, RS, LAST) do { \
      STEP(sA, sB, kf[T], qf[1], vf[TP], o[3], vf[T], o[0]); \
      if (!LAST) LDKV(RS, TP, kcur + (T + 3) * 4096); \
      STEP(sB, sA, kf[T], qf[2], vf[T], o[0], vf[T], o[1]); \
      if (LAST) RED_WRITE(0); \
      STEP(sA, sB, kf[T], qf[3], vf[T], o[1], vf[T], o[2]); \
      if (LAST) RED_WRITE(1); \
      STEP(sB, sA, kf[TN], qf[0], vf[T], o[2], vf[T], o[3]); \
      if (LAST) RED_WRITE(2); \
    } while (0)
    vf[3][1] = s16x8{};
    unsigned kcur = kbase;
#pragma unroll 1
    for (int tb = 0;; tb += 4) {
      const __amdgpu_buffer_rsrc_t wsr_next = __builtin_amdgcn_make_buffer_rsrc(ws, 0, tb < 12 ? WS_END : 0u, 0x00020000);
      TILE(0, 3, 1, wsr, false);
      TILE(1, 0, 2, wsr_next, false);
      TILE(2, 1, 3, wsr_next, false);
      if (tb == 12) break;
      TILE(3, 2, 0, wsr, false);
      kcur += 16384;
      if (STAMPS) { if (tb == 0) STAMP(11); else if (tb == 4) { } else { } }
    }
    lds_wait_ge(C_BFREE, 4);
    TILE(3, 2, 0, wsr, true);
    o[3] = MFMA32(vf[3][1], BF(p1), o[3]);
    STAMP(6);
    role_sync(C_A, 1);
    {
      f32x4 pv[12];
#pragma unroll
      for (int k = 0; k < 12; ++k) pv[k] = lds_ld<f32x4>(redw + w * 12288 + k * 1024);
      SBAR();
      f32x16 a = o[3];
#pragma unroll
      for (int k = 0; k < 12; ++k) { const int q = k & 3; a[4 * q] += pv[k][0]; a[4 * q + 1] += pv[k][1]; a[4 * q + 2] += pv[k][2]; a[4 * q + 3] += pv[k][3]; }
      auto lr = __builtin_amdgcn_permlane32_swap(f2u(a[12]), f2u(a[12]), false, false);
      const float inv = __builtin_amdgcn_rcpf(u2f(lr[1]));
      u32x4 f0, f1;
#pragma unroll
      for (int i = 0; i < 4; ++i) { f0[i] = pk_bf16(a[2 * i] * inv, a[2 * i + 1] * inv); f1[i] = pk_bf16(a[8 + 2 * i] * inv, a[9 + 2 * i] * inv); }
      if (hh) { f1[2] = 0u; f1[3] = 0u; }
      const unsigned so = WS_O + ((qt * 4 + w) * 16 + h) * 2048;
      st128(f0, wsr, l16, so);
      st128(f1, wsr, l16 + 1024, so);
      lds_arrive(C_A);
      drain_vm();
      if (lane == 0) flag_store(ws, WS_OFLAG + ((qt * 4 + w) * 16 + h) * 128, tag);
    }
    STAMP(7);
    const int rs = xcd * 8 + (idx >> 2), cg = idx & 3, n0 = w < 3 ? 32 * w : 80;
    lds_wait_ge(C_A, 2 * 4);
    lds_wait_ge(C_WO, 4);
    flag_wait(ws, WS_OFLAG + (rs * 16 + w * 4 + (lane & 3)) * 128, tag);
    STAMP(8);
    {
      const char* os = ws + WS_O + (rs * 16 + w * 4) * 2048;
      const unsigned od = L_B + w * 8192;
      glds_dev<0>(l16, os, od); glds_dev<1024>(l16, os + 1024, od); glds_dev<2048>(l16, os + 2048, od); glds_dev<3072>(l16, os + 3072, od);
      glds_dev<4096>(l16, os + 4096, od); glds_dev<5120>(l16, os + 5120, od); glds_dev<6144>(l16, os + 6144, od); glds_dev<7168>(l16, os + 7168, od);
    }
    s16x8 wf[32];
    unsigned oa[4];
#pragma unroll
    for (int q = 0; q < 4; ++q) {
      const int gq = (w + q) & 3;
      unsigned wa = L_RING + (gq * 112 + 4 * hh + ((lane & 15) >> 2)) * 224 + n0 * 2 + ((lane >> 4) & 1) * 32 + (lane & 3) * 8;
      unsigned wb2 = wa + (hh ? 16 * 224 : 24 * 224);
      oa[q] = redw + gq * 8192;
      PIN(wa); PIN(wb2); PIN(oa[q]);
#pragma unroll
      for (int hd = 0; hd < 4; ++hd) {
        wf[q * 8 + hd * 2] = cat8(tr_read(wa + hd * 28 * 224), tr_read(wa + hd * 28 * 224 + 8 * 224));
        wf[q * 8 + hd * 2 + 1] = cat8(tr_read(wa + hd * 28 * 224 + 16 * 224), tr_read(wb2 + hd * 28 * 224));
      }
    }
    drain_vm();
    lds_arrive_nw(C_A);
    STAMP(13);
    f32x16 d = f32x16{};
    {
      s16x8 ob[8];
#pragma unroll
      for (int k = 0; k < 8; ++k) ob[k] = lds_ld<s16x8>(oa[0] + k * 1024);
      SBAR();
#pragma unroll
      for (int k = 0; k < 4; ++k) { d = MFMA32(wf[k], ob[k], d); SBAR(); }
      lds_wait_ge(C_A, 3 * 4);
#pragma unroll
      for (int k = 0; k < 4; ++k) ob[k] = lds_ld<s16x8>(oa[1] + k * 1024);
      SBAR();
#pragma unroll
      for (int k = 4; k < 32; ++k) {
        d = MFMA32(wf[k], ob[k & 7], d);
        if (k + 8 < 32) ob[k & 7] = lds_ld<s16x8>(oa[(k + 8) >> 3] + (k & 7) * 1024);
        SBAR();
      }
    }
    {
      char* op = out + (size_t)(rs * 32 * 1792 + (cg * 112 + n0) * 2) + (unsigned)(r32 * 1792 + hh * 16);
#pragma unroll
      for (int k = 0; k < 2; ++k) {
        const unsigned a0 = pk_bf16(d[8 * k], d[8 * k + 1]), a1 = pk_bf16(d[8 * k + 2], d[8 * k + 3]);
        const unsigned b0 = pk_bf16(d[8 * k + 4], d[8 * k + 5]), b1 = pk_bf16(d[8 * k + 6], d[8 * k + 7]);
        auto s0 = __builtin_amdgcn_permlane32_swap(a0, b0, false, false);
        auto s1 = __builtin_amdgcn_permlane32_swap(a1, b1, false, false);
        if (k || w < 3) *(GAS u32x4*)(op + k * 32) = u32x4{s0[0], s1[0], s0[1], s1[1]};
      }
    }
    STAMP(10);
    STAMP_FLUSH();
    asm volatile(".Lpf_end:");
  } else {
    const int wb = wid ^ 2;
    const int c = wb & 1, half = wb >> 1;
    const int d = (has_b ? xcd * 64 : 0) + idx * 2 + c;
    const unsigned po = d * 512 + lane * 4;
    unsigned pdt, p0[4], p1[4];
    u64 tag;
#define LOAD_P(P, O) do { P[0] = *(const GAS unsigned*)(logAr + (O) + po); P[1] = *(const GAS unsigned*)(Aim + (O) + po); \
      P[2] = *(const GAS unsigned*)(Cre + (O) + po); P[3] = *(const GAS unsigned*)(Cim + (O) + po); } while (0)
    const unsigned T = L_T + c * 2 * T_B;
    unsigned ga[2][11], wv[7];
    auto gather_addr = [&]() __attribute__((always_inline)) NOPK {
#pragma unroll
      for (int i = 0; i < 7; ++i) {
        const unsigned p = i * 64 + lane, row = (p * 37450u) >> 19; wv[i] = row * 896 + (p - row * 14) * 16; PIN(wv[i]);
      }
#pragma unroll
      for (int it = 0; it < 2; ++it) {
        const int l0 = 8 * (3 + it * 128 + half * 64 + lane) - 27;
        const float f0 = (float)l0;
#pragma unroll
        for (int k = 0; k < 11; k += 2) {
          const unsigned pk = pk_bf16(f0 + (float)k, f0 + (float)(k + 1));
          asm volatile("v_mad_u32_u16 %0, %1, 4, %2" : "=v"(ga[it][k]) : "v"(pk), "s"(T - 4u * 17024u));
          if (k < 10) asm volatile("v_mad_u32_u16 %0, %1, 4, %2 op_sel:[1,0,0,0]" : "=v"(ga[it][k + 1]) : "v"(pk), "s"(T - 4u * 17024u));
        }
        if (it == 0 && !half) {
#pragma unroll
          for (int k = 0; k < 11; ++k) { const int l = l0 + k; ga[0][k] = l < 128 ? T + 4 * ((k < 3 && l < 0) ? 641 : l) : ga[0][k]; PIN(ga[0][k]); }
        }
      }
    };
    if (half) {
      unsigned xo[4], wo_[6];
#pragma unroll
      for (int q = 0; q < 4; ++q) xo[q] = (q * 8 + (lane >> 3)) * 896 + (((lane & 7) ^ ((q & 1) * 4 + (lane >> 4))) << 4);
      const char* xb = x + (size_t)(rt * 128 + c * 64) * 896;
      const char* wq = wqkv + g * 168 - woff;
      int kc = ROT ? ((idx & 7) + 2 * (idx >> 3)) % 7 : 0;
      u64 epoch;
      asm volatile("global_load_dwordx2 %0, %1, %2 sc1" : "=v"(epoch) : "v"(0u), "s"(ws + WS_EPOCH) : "memory");
#define X_PIECES(B, D) do { glds<0>(xo[0], B, D); glds<1024>(xo[1], B, D); glds<2048>(xo[2], B, D); glds<3072>(xo[3], B, D); \
        glds<4096>(xo[0], (B) + 32 * 896, D); glds<5120>(xo[1], (B) + 32 * 896, D); glds<6144>(xo[2], (B) + 32 * 896, D); glds<7168>(xo[3], (B) + 32 * 896, D); } while (0)
#define W_PIECES(B, D) do { glds<0>(wo_[0], B, D); glds<1024>(wo_[1], B, D); glds<2048>(wo_[2], B, D); glds<3072>(wo_[3], B, D); glds<4096>(wo_[4], B, D); glds<5120>(wo_[5], B, D); } while (0)
      {
        const char* up = u + (size_t)(idx * 64 + 56 - 32 * c) * 896 + (has_b ? xcd * 128 : 0);
        const unsigned uo = (7 - (lane >> 3)) * 896, k4 = (lane & 7) ^ (4 * c);
        glds<0>(uo + (k4 << 4), up, L_UIMG + c * 4096); glds<1024>(uo + ((k4 ^ 1) << 4), up - 8 * 896, L_UIMG + c * 4096);
        glds<2048>(uo + ((k4 ^ 2) << 4), up - 16 * 896, L_UIMG + c * 4096); glds<3072>(uo + ((k4 ^ 3) << 4), up - 24 * 896, L_UIMG + c * 4096);
      }
      X_PIECES(xb + kc * 128, L_RING + c * 8192);
#pragma unroll
      for (int i = 0; i < 6; ++i) {
        const unsigned p = (6 * c + i) * 64 + lane, row = (p * 43691u) >> 19; unsigned e = p - row * 12; e = e > 10 ? 10 : e;
        wo_[i] = row * 2688 + e * 16;
      }
      W_PIECES(wq + kc * 64 * 2688, L_RING + XCH_B + c * 6144);
      asm volatile("s_barrier" ::: "memory");
      asm volatile("s_waitcnt vmcnt(14)" : "+v"(epoch) :: "memory");
      lds_arrive_nw(C_UI);
      tag = MAKE_TAG(epoch); asm volatile("" :: "s"(tag)); lds_arrive_nw(C_BTAG);
#pragma unroll 1
      for (int n = 1; n < 7; ++n) {
        if (n >= 4) lds_wait_ge(C_R + 4 * (n - 4), 4);
        const unsigned slot = (n & 3) * SLOT_B;
        kc = kc == 6 ? 0 : kc + 1;
        X_PIECES(xb + kc * 128, L_RING + c * 8192 + slot);
        W_PIECES(wq + kc * 64 * 2688, L_RING + XCH_B + c * 6144 + slot);
        asm volatile("s_waitcnt vmcnt(14)" ::: "memory"); lds_arrive_nw(C_L + 4 * (n - 1));
        if (n == 1) STAMP(1);
      }
      pdt = *(const GAS unsigned short*)(logdt + d * 2);
      LOAD_P(p0, 256);
      asm volatile("s_waitcnt vmcnt(5)" ::: "memory");
      lds_arrive_nw(C_L + 24);
      if (c == 0 && has_b && lane == 0) flag_store(ws, WS_UFLAG + (xcd * 32 + idx) * 128, tag);
      STAMP(12);
      gather_addr();
#pragma unroll
      for (int i = 0; i < 4; ++i) { p0[i] <<= 16; p1[i] = 0u; }
      lds_wait_ge(C_R + 12, 4);
    } else {
      pdt = *(const GAS unsigned short*)(logdt + d * 2);
      LOAD_P(p0, 0); LOAD_P(p1, 256);
      const u64 epoch = __hip_atomic_load((GAS u64*)(ws + WS_EPOCH), __ATOMIC_RELAXED, __HIP_MEMORY_SCOPE_AGENT);
      asm volatile("s_barrier" ::: "memory");
      gather_addr();
      asm volatile("" : "+v"(pdt), "+v"(p0[0]), "+v"(p1[0]), "+v"(p1[1]), "+v"(p1[2]), "+v"(p1[3]));
      tag = MAKE_TAG(epoch); asm volatile("" :: "s"(tag)); lds_arrive_nw(C_BTAG);
#pragma unroll
      for (int i = 0; i < 4; ++i) p1[i] &= 0xffff0000u;
    }
    __builtin_amdgcn_s_setprio(1);
    STAMP(9);
    if (has_b) {
      const float dt = bf_round(fexp(bf_lo(pdt)));
      const unsigned cm = umax(umax(umax(p0[2] << 16 & 0x7fff0000u, p0[2] & 0x7fff0000u), umax(p0[3] << 16 & 0x7fff0000u, p0[3] & 0x7fff0000u)),
                               umax(p1[2] & 0x7fff0000u, p1[3] & 0x7fff0000u));
      unsigned eb = wave_umax(cm) >> 23; eb = eb < 1u ? 1u : (eb > 253u ? 253u : eb);
      const float csc = u2f((254u - eb) << 23), cinv = u2f(eb << 23);
      const unsigned tb_ = (half ? 3 * SLOT_B : 4 * SLOT_B) + c * TAB_W;
      const unsigned lw = tb_ + (lane >> 3) * 1056 + ((lane >> 2) & 1) * 528 + (lane & 3) * 4;
      const unsigned hw = tb_ + TAB_HI + (lane >> 3) * 672 + ((lane >> 2) & 1) * 336 + (lane & 3) * 4;
      const unsigned lr = tb_ + hh * 528 + r32 * 16, hr = tb_ + TAB_HI + hh * 336 + r32 * 16;
      f32x16 kd = f32x16{};
      u64 uflag = 0;
#pragma unroll 1
      for (int r = half ? 2 : 0; r < 3; ++r) {
        const float rho = -bf_round(fexp(bf_hi(p0[0]))) * dt * LOG2E, th = bf_hi(p0[1]) * dt * 0.15915494309189535f;
        f32x2 hA, hB; hA.x = bf_hi(p0[2]) * csc; hA.y = bf_hi(p0[3]) * -csc;
#pragma unroll
        for (int i = 0; i < 4; ++i) p0[i] = r == 0 ? p0[i] << 16 : p1[i];
        const f32x2 z8 = zpow(rho, th, 8.f), z32 = zpow(rho, th, 32.f), z1 = zpow(rho, -th, 1.f), z256 = zpow(rho, -th, 256.f);
        const f32x2 z16 = cmul(z8, z8), z24 = cmul(z8, z16);
        const unsigned q1 = pk_f16(z8.x, z8.y), q2 = pk_f16(z16.x, z16.y), q3 = pk_f16(z24.x, z24.y);
        lds_st<unsigned>(lw, 0x00003C00u); lds_st<unsigned>(lw + 16, q1); lds_st<unsigned>(lw + 32, q2); lds_st<unsigned>(lw + 48, q3);
        f32x2 e = z32;
#pragma unroll
        for (int a = 1; a < 8; ++a) {
          const unsigned p_ = pk_f16(e.x, e.y); unsigned m1, m2, m3;
          if (a < 7) e = cmul(e, z32);
          hmul3(p_, q1, q2, q3, m1, m2, m3);
          lds_st<unsigned>(lw + 64 * a, p_); lds_st<unsigned>(lw + 64 * a + 16, m1); lds_st<unsigned>(lw + 64 * a + 32, m2); lds_st<unsigned>(lw + 64 * a + 48, m3);
        }
        uflag = __hip_atomic_load((GAS u64*)(ws + WS_UFLAG + (xcd * 32 + r32) * 128), __ATOMIC_RELAXED, __HIP_MEMORY_SCOPE_AGENT);
#define PUTH(i, v) lds_st<unsigned>(hw + (i) * 16, pk_f16((v).x, (v).y))
        PUTH(0, hA); f32x2 g0 = cmul(hA, z256); PUTH(8, g0);
        hB = cmul(hA, z1); PUTH(1, hB); hA = cmul(hB, z1); PUTH(2, hA); { f32x2 t_ = cmul(hA, z256); PUTH(9, t_); }
        hB = cmul(hA, z1); PUTH(3, hB); hA = cmul(hB, z1); PUTH(4, hA); f32x2 g4 = cmul(hA, z256); PUTH(10, g4);
        hB = cmul(hA, z1); PUTH(5, hB); hA = cmul(hB, z1); PUTH(6, hA); { f32x2 t_ = cmul(hA, z256); PUTH(11, t_); }
        hB = cmul(hA, z1); PUTH(7, hB);
        g0 = cmul(g0, z256); PUTH(12, g0); g4 = cmul(g4, z256); PUTH(13, g4);
        g0 = cmul(g0, z256); PUTH(14, g0); g4 = cmul(g4, z256); PUTH(15, g4);
#pragma unroll
        for (int i = 16; i < 21; ++i) { g0 = cmul(g0, z256); PUTH(i, g0); }
        h16x8 fa[8], fb[8];
#pragma unroll
        for (int s = 0; s < 8; ++s) { fa[s] = lds_ld<h16x8>(hr + s * 672); fb[s] = lds_ld<h16x8>(lr + s * 1056); }
        SBAR();
#pragma unroll
        for (int s = 0; s < 8; ++s) kd = MFMA32H(fa[s], fb[s], kd);
      }
      STAMP(10);
      if (__builtin_amdgcn_ballot_w64(uflag != tag) != 0ull) flag_wait(ws, WS_UFLAG + (xcd * 32 + r32) * 128, tag);
      asm volatile("" ::: "memory");
      u32x4 uv[2];
#pragma unroll
      for (int i = 0; i < 2; ++i) uv[i] = __builtin_amdgcn_raw_buffer_load_b128(wsr, wb * 2048 + i * 1024 + lane * 16, WS_UT + (xcd * 32 + idx) * 8192, AUX_SC1);
      STAMP(11);
      {
        const float ks = cinv * 2.f * dt;
        const unsigned Tw = T + half * T_B;
        const unsigned i1 = Tw + (hh ? 384 + 2 * r32 : 256 + 4 * r32) * 4;
        const unsigned i2 = Tw + (hh ? (r32 == 0 ? 640 : 643) : 512 + r32) * 4, i3 = Tw + (hh ? 641 : 512 + r32) * 4, st3 = hh ? 0 : 128;
        lds_wait_ge(C_R + 24, 4);
        lds_st<f32x4>(Tw + (8 * r32 + 4 * hh) * 4, f32x4{kd[0] * ks, kd[1] * ks, kd[2] * ks, kd[3] * ks});
        lds_st<f32x2>(i1, f32x2{kd[4] * ks, kd[5] * ks}); lds_st<f32x2>(i1 + (hh ? 256 : 8), f32x2{kd[6] * ks, kd[7] * ks});
        lds_st<float>(i2, kd[8] * ks);
        lds_st<float>(i3 + st3, hh ? 0.f : kd[9] * ks); lds_st<float>(i3 + 2 * st3, hh ? 0.f : kd[10] * ks); lds_st<float>(i3 + 3 * st3, hh ? 0.f : kd[11] * ks);
        role_sync(C_B, 1);
      }
      STAMP(2);
      const unsigned cb = L_B + c * CH_B;
      {
        float P[2][12];
#pragma unroll
        for (int it = 0; it < 2; ++it)
#pragma unroll
          for (int k = 0; k < 11; ++k) P[it][k] = lds_ld<float>(ga[it][k]) + lds_ld<float>(ga[it][k] + T_B);
        const unsigned ub = cb + 4 * KC_S + half * UC_S;
        const u32x4 z4 = u32x4{0u, 0u, 0u, 0u};
        if (lane < 28) lds_st<u32x4>(ub + 4096 + lane * 16, z4);
        else if (lane < 34) lds_st<u32x4>(cb + (half * 2 + (lane - 28) / 3) * KC_S + ((lane - 28) % 3) * 16, z4);
        else if (lane == 34 && half) lds_st<u32x2>(ub + 4088, u32x2{0u, 0u});
#pragma unroll
        for (int it = 0; it < 2; ++it) {
          unsigned Q[5], A[5];
#pragma unroll
          for (int k = 0; k < 5; ++k) Q[k] = pk_bf16(P[it][2 * k], P[it][2 * k + 1]);
#pragma unroll
          for (int k = 0; k < 5; ++k) A[k] = pk_bf16(P[it][2 * k + 1], P[it][2 * k + 2]);
          const unsigned a = cb + (3 + it * 128 + half * 64 + lane) * 16;
          lds_st<u32x4>(a, u32x4{Q[0], Q[1], Q[2], Q[3]}); lds_st<u32x4>(a + KC_S, u32x4{A[0], A[1], A[2], A[3]});
          lds_st<u32x4>(a + 2 * KC_S, u32x4{Q[1], Q[2], Q[3], Q[4]}); lds_st<u32x4>(a + 3 * KC_S, u32x4{A[1], A[2], A[3], A[4]});
        }
      }
      STAMP(3);
#pragma unroll
      for (int i = 0; i < 2; ++i) {
        const int p = wb * 128 + i * 64 + lane;
        const unsigned a = L_B + (p & 1) * CH_B + 4 * KC_S + (p >> 1) * 16;
        lds_st<u32x4>(a, uv[i]); lds_st<u32x2>(a + UC_S - 8, u32x2{uv[i][0], uv[i][1]}); lds_st<u32x2>(a + UC_S, u32x2{uv[i][2], uv[i][3]});
      }
      role_sync(C_B, 2);
      __builtin_amdgcn_s_setprio(BCONV);
      STAMP(5);
      f32x4 y[8];
#pragma unroll
      for (int i = 0; i < 8; ++i) y[i] = f32x4{0.f, 0.f, 0.f, 0.f};
      {
        const int r16 = lane & 15, kq = lane >> 4;
        unsigned ka = cb + (r16 & 3) * KC_S + ((r16 >> 2) + kq) * 16 + half * 256;
        unsigned ua = cb + 4 * KC_S + (1 - (r16 & 1)) * UC_S + (2016 - 32 * (r16 >> 1) + 8 * kq) * 2 - 7 * 512 + half * 256;
        PIN(ka); PIN(ua);
        s16x8 af[2][8], bf[2][8];
#pragma unroll
        for (int m = 0; m < 8; ++m) { bf[0][7 - m] = lds_ld<s16x8>(ua + m * 512); af[0][m] = lds_ld<s16x8>(ka + m * 512); }
        auto conv_class = [&](auto sc, auto oc) __attribute__((always_inline)) NOPK {
          constexpr int S = decltype(sc)::value, O = decltype(oc)::value;
          int k = 0;
#pragma unroll
          for (int n = 7; n >= 0; --n)
#pragma unroll
            for (int m = 0; m + n < 8; ++m) {
              y[m + n] = MFMA16(af[S][m], bf[S][n], y[m + n]);
              if (k >= 4 && k < 36 && (k & 1) == 0) {
                const int q = (k - 4) >> 2;
                if (k & 2) af[1 - S][q] = lds_ld<s16x8>(ka + O + q * 512); else bf[1 - S][7 - q] = lds_ld<s16x8>(ua + O + q * 512);
              }
              SBAR();
              ++k;
            }
        };
#pragma unroll 1
        for (int it = 0; it < 2; ++it) {
          conv_class(IC<0>{}, IC<64>{});
          conv_class(IC<1>{}, IC<128>{});
          ka += 128; ua += 128;
        }
      }
      PIN(y[7]); STAMP(13);
      role_sync_slow(C_B, 3);
      __builtin_amdgcn_s_setprio(2);
#pragma unroll
      for (int i = 0; i < 4; ++i) lds_st<f32x4>(L_B + wb * 4096 + i * 1024 + lane * 16, half ? y[i] : y[4 + i]);
      role_sync_slow(C_B, 4);
      STAMP(14);
      {
        const int cc = lane & 15, kq = lane >> 4;
#pragma unroll
        for (int i = 0; i < 4; ++i) {
          const f32x4 v = lds_ld<f32x4>(L_B + (wb ^ 2) * 4096 + i * 1024 + lane * 16) + (half ? y[4 + i] : y[i]);
          const int t64 = 4 * (half * 4 + i) + (cc >> 2);
          __builtin_amdgcn_raw_buffer_store_b64(u32x2{pk_bf16(v[0], v[1]), pk_bf16(v[2], v[3])}, wsr,
                                                ((xcd * 32 + t64) * 64 + idx * 2 + c) * 128 + (32 * ((cc >> 1) & 1) + 8 * kq + 4 * (cc & 1)) * 2, WS_YT, AUX_SC1);
        }
      }
      lds_arrive(C_BFREE);
      __builtin_amdgcn_s_setprio(0);
      STAMP(15);
    } else {
      lds_arrive(C_BFREE);
      __builtin_amdgcn_s_setprio(0);
    }
    lds_wait_ge_slow(C_QR, 4);
#pragma unroll 1
    for (int i = wb; i < 14; i += 4) {
      const char* sb = wo + i * 32 * 896 + (idx & 3) * 224;
      const unsigned dst = L_RING + i * 7168;
#define WO_PIECE(k) glds<(k) * 1024>(wv[k], sb, dst); asm volatile("s_waitcnt vmcnt(2)" ::: "memory")
      WO_PIECE(0); WO_PIECE(1); WO_PIECE(2);
      if (i < 4 && has_b) {
        if (last_of_four(C_BPUB, lane, 0)) { if (lane == 0) flag_store(ws, WS_YFLAG + (xcd * 32 + idx) * 128, tag); }
        STAMP(6);
      }
      WO_PIECE(3); WO_PIECE(4); WO_PIECE(5); WO_PIECE(6);
    }
    drain_vm();
    lds_arrive(C_WO);
    STAMP(7);
    if (has_b) {
      const int tl = wb * 16 + (lane & 15);
      u32x4 uu[2], ds[2];
#pragma unroll
      for (int i = 0; i < 2; ++i) {
        const int oct = (lane >> 4) + 4 * i;
        uu[i] = *(const GAS u32x4*)(u + (size_t)(idx * 64 * 896 + xcd * 128) + (unsigned)(tl * 896 + oct * 16));
        ds[i] = *(const GAS u32x4*)(Dsk + xcd * 128 + (unsigned)(oct * 16));
      }
      if (wb == 0) flag_wait(ws, WS_YFLAG + (xcd * 32 + r32) * 128, tag);
      role_sync_idle(C_B, 5);
      u32x4 v[2];
#pragma unroll
      for (int i = 0; i < 2; ++i) v[i] = __builtin_amdgcn_raw_buffer_load_b128(wsr, wb * 2048 + i * 1024 + lane * 16, WS_YT + (xcd * 32 + idx) * 8192, AUX_SC1);
#pragma unroll
      for (int i = 0; i < 2; ++i) lds_st<u32x4>(L_YIMG + wb * 2048 + i * 1024 + lane * 16, v[i]);
      role_sync_idle(C_B, 6);
#pragma unroll
      for (int i = 0; i < 2; ++i) {
        const int oct = (lane >> 4) + 4 * i;
        const unsigned a = L_YIMG + (8 * oct + ((lane & 15) >> 2)) * 128 + (wb * 16 + (lane & 3) * 4) * 2;
        const u32x4 yy = __builtin_bit_cast(u32x4, cat8(tr_read(a), tr_read(a + 512)));
        u32x4 r;
#pragma unroll
        for (int k = 0; k < 4; ++k) {
          const unsigned sk = pk_bf16(bf_lo(uu[i][k]) * bf_lo(ds[i][k]), bf_hi(uu[i][k]) * bf_hi(ds[i][k]));
          r[k] = pk_bf16(bf_lo(yy[k]) + bf_lo(sk), bf_hi(yy[k]) + bf_hi(sk));
        }
        *(GAS u32x4*)(out + (size_t)(idx * 64 * 1792 + 896 + xcd * 128) + (unsigned)(tl * 1792 + oct * 16)) = r;
      }
      STAMP(8);
    } else if (b == 7 && wb == 0) {
#pragma unroll 1
      for (int i = 0; i < 4; ++i) rec_wait(wsr, WS_QFLAG + (i * 64 + lane) * 128, tag);
      if (lane == 0) flag_store(ws, WS_EPOCH, tag);
    }
    STAMP_FLUSH();
  }
}

extern "C" void kernel_launch(void* const* d_in, const int* in_sizes, int n_in, void* d_out, int out_size, void* d_ws, size_t ws_size,
                              hipStream_t stream) {
  (void)in_sizes; (void)n_in; (void)out_size; (void)ws_size;
  (void)hipFuncSetAttribute((const void*)fused_mha_s4d, hipFuncAttributeMaxDynamicSharedMemorySize, LDS_BYTES);
  fused_mha_s4d<<<256, 512, LDS_BYTES, stream>>>((const char*)d_in[0], (const char*)d_in[1], (const char*)d_in[2], (const char*)d_in[3],
                                                 (const char*)d_in[4], (const char*)d_in[5], (const char*)d_in[6], (const char*)d_in[7],
                                                 (const char*)d_in[8], (const char*)d_in[9], (char*)d_out,
                                                 (char*)d_ws + (WSEND ? ((ws_size - WS_END) & ~(size_t)0xfffff) : 0));
}
```

```cpp
#include <hip/hip_runtime.h>
#include <hip/hip_bf16.h>
#include <stdint.h>

typedef short s16x8 __attribute__((ext_vector_type(8)));
typedef short s16x4 __attribute__((ext_vector_type(4)));
typedef short s16x2 __attribute__((ext_vector_type(2)));
typedef _Float16 h16x8 __attribute__((ext_vector_type(8)));
typedef _Float16 h16x2 __attribute__((ext_vector_type(2)));
typedef __bf16 b16x2 __attribute__((ext_vector_type(2)));
typedef float f32x16 __attribute__((ext_vector_type(16)));
typedef float f32x4 __attribute__((ext_vector_type(4)));
typedef float f32x2 __attribute__((ext_vector_type(2)));
typedef unsigned u32x4 __attribute__((ext_vector_type(4)));
typedef unsigned u32x2 __attribute__((ext_vector_type(2)));
typedef unsigned long long u64;
#define LAS __attribute__((address_space(3)))
#define GAS __attribute__((address_space(1)))
#if defined(__HIP_DEVICE_COMPILE__)
#define NOPK __attribute__((target("no-packed-fp32-ops")))
#else
#define NOPK
#endif
#define DI NOPK __device__ __forceinline__
#define SBAR() __builtin_amdgcn_sched_barrier(0)
#define PIN(x) asm volatile("" : "+v"(x))
#define MFMA32(a, b, c) __builtin_amdgcn_mfma_f32_32x32x16_bf16((a), (b), (c), 0, 0, 0)
#define MFMA32H(a, b, c) __builtin_amdgcn_mfma_f32_32x32x16_f16((a), (b), (c), 0, 0, 0)
#define MFMA16(a, b, c) __builtin_amdgcn_mfma_f32_16x16x32_bf16((a), (b), (c), 0, 0, 0)
#define AUX_SC1 16

#define PREFETCH 1
#define WSEND 0
#define APRIO 3
#define BCONV 0
#define ROT 0
#define STAMPS 0

constexpr unsigned WS_EPOCH = 0;
constexpr unsigned WS_QFLAG = 4096;
constexpr unsigned WS_OFLAG = WS_QFLAG + 256 * 128;
constexpr unsigned WS_UFLAG = WS_OFLAG + 1024 * 128;
constexpr unsigned WS_YFLAG = WS_UFLAG + 256 * 128;
constexpr unsigned WS_QKV = 393216;
constexpr unsigned WS_KV = WS_QKV + 16 * 64 * 2048;
constexpr unsigned WS_O = WS_QKV + 48 * 64 * 2048;
constexpr unsigned WS_UT = WS_O + 64 * 16 * 2048;
constexpr unsigned WS_YT = WS_UT + 256 * 8192;
constexpr unsigned WS_DBG = WS_YT + 224 * 8192;
constexpr unsigned WS_END = WS_DBG + 256 * 8 * 16 * 8;
static_assert(WS_YFLAG + 256 * 128 <= WS_QKV, "ws map");

constexpr unsigned L_RING = 0, SLOT_B = 28672, XCH_B = 16384;
constexpr unsigned L_B = 100352;
constexpr unsigned TAB_W = 14000, TAB_HI = 8448;
constexpr unsigned L_UIMG = 4 * SLOT_B + 2 * TAB_W;
constexpr unsigned L_T = 156352, T_B = 1296, L_KX = L_T + 2 * T_B;
constexpr unsigned KC_S = 4160, UC_S = 4640, CH_B = 4 * KC_S + 2 * UC_S;
constexpr unsigned L_YIMG = L_B + 49152;
constexpr unsigned L_CTL = 163584;
constexpr unsigned LDS_BYTES = 163840;
constexpr unsigned C_A = L_CTL, C_B = L_CTL + 4, C_GO = L_CTL + 8, C_APUB = L_CTL + 12, C_BPUB = L_CTL + 16, C_WO = L_CTL + 20, C_BFREE = L_CTL + 24, C_BTAG = L_CTL + 28, C_U = L_CTL + 32, C_Q = L_CTL + 36, C_QR = L_CTL + 40, C_FB = L_CTL + 44, C_KM = L_CTL + 48, C_UI = L_CTL + 60, C_KX = L_CTL + 128;
constexpr unsigned C_L = L_CTL + 64, C_R = L_CTL + 96;
constexpr unsigned L_MAX = 162816;

constexpr unsigned L_SINK = L_MAX + 512;
constexpr unsigned SPIN_CAP = 1u << 22;
constexpr float LOG2E = 1.4426950408889634f;
constexpr float QSCALE = 0.18898223650461362f * LOG2E;
constexpr float SAFE_BOUND = 48.0f;

template <int V> struct IC { static constexpr int value = V; };
DI unsigned f2u(float f) { return __builtin_bit_cast(unsigned, f); }
DI float u2f(unsigned u) { return __builtin_bit_cast(float, u); }
DI unsigned umax(unsigned a, unsigned b) { return a > b ? a : b; }
DI unsigned sel(unsigned a, unsigned b, u64 mask) { return __builtin_amdgcn_inverse_ballot_w64(mask) ? b : a; }
DI float fexp(float x) { return __builtin_amdgcn_exp2f(x * LOG2E); }
DI float bf_lo(unsigned v) { return u2f(v << 16); }
DI float bf_hi(unsigned v) { return u2f(v & 0xffff0000u); }
DI unsigned pk_bf16(float a, float b) { f32x2 v = {a, b}; return __builtin_bit_cast(unsigned, __builtin_convertvector(v, b16x2)); }
DI unsigned pk_f16(float a, float b) { f32x2 v = {a, b}; return __builtin_bit_cast(unsigned, __builtin_convertvector(v, h16x2)); }
DI float bf_round(float a) { return bf_lo(pk_bf16(a, a)); }
template <int OFF> DI void glds(unsigned voff, const char* sbase, unsigned lds) {
  asm volatile("s_add_u32 m0, %2, %3\n\ts_nop 0\n\tglobal_load_lds_dwordx4 %0, %1" :: "v"(voff), "s"(sbase), "s"(lds), "i"(OFF) : "memory", "scc");
}
DI float half_swap_add(float v) {
  auto r = __builtin_amdgcn_permlane32_swap(f2u(v), f2u(v), false, false);
  return u2f(r[0]) + u2f(r[1]);
}
template <int OFF> DI void glds_dev(unsigned voff, const char* sbase, unsigned lds) {
  asm volatile("s_add_u32 m0, %2, %3\n\ts_nop 0\n\tglobal_load_lds_dwordx4 %0, %1 sc1" :: "v"(voff), "s"(sbase), "s"(lds), "i"(OFF) : "memory", "scc");
}
DI float dot2(unsigned a, float c) { return __builtin_amdgcn_fdot2_f32_bf16(__builtin_bit_cast(b16x2, a), __builtin_bit_cast(b16x2, a), c, false); }
DI float frag_norm2(u32x4 f0, u32x4 f1, int hh) {
  float a = dot2(f0[0], 0.f), b = dot2(f0[1], 0.f), c = dot2(f1[2], 0.f);
  a = dot2(f0[2], a); b = dot2(f0[3], b); c = dot2(f1[3], c); a = dot2(f1[0], a); b = dot2(f1[1], b);
  return half_swap_add(a + b + (hh ? 0.f : c));
}
DI s16x4 tr_read(unsigned a) { return __builtin_bit_cast(s16x4, __builtin_amdgcn_ds_read_tr16_b64_v4i16((LAS s16x4*)(uintptr_t)a)); }
DI s16x8 cat8(s16x4 lo, s16x4 hi) { return __builtin_shufflevector(lo, hi, 0, 1, 2, 3, 4, 5, 6, 7); }
template <class T> DI T lds_ld(unsigned a) { return *(LAS T*)(uintptr_t)a; }
template <class T> DI void lds_st(unsigned a, T v) { *(LAS T*)(uintptr_t)a = v; }
#define LDS_POLL(V, A, S, N, T, EXTRA) "s_mov_b32 " N ", 0\n1:\n\t" EXTRA "ds_read_b32 " V ", " A "\n\ts_add_u32 " N ", " N ", 1024\n\t" \
  "s_waitcnt lgkmcnt(0)\n\tv_readfirstlane_b32 " S ", " V "\n\ts_cbranch_scc1 4f\n\ts_cmp_lt_u32 " S ", " T "\n\ts_cbranch_scc1 1b\n4:\n\t"
DI unsigned lds_wait_ge(unsigned a, unsigned target) {
  unsigned v, sv, n;
  asm volatile(LDS_POLL("%0", "%3", "%1", "%2", "%4", "") : "=&v"(v), "=&s"(sv), "=&s"(n) : "v"(a), "s"(target) : "memory", "scc");
  return sv;
}
DI unsigned lds_wait_ge_slow(unsigned a, unsigned target) {
  unsigned v, sv, n;
  asm volatile(LDS_POLL("%0", "%3", "%1", "%2", "%4", "s_sleep 1\n\t") : "=&v"(v), "=&s"(sv), "=&s"(n) : "v"(a), "s"(target) : "memory", "scc");
  return sv;
}
DI void lds_arrive(unsigned a) {
  asm volatile("s_waitcnt lgkmcnt(0)\n\ts_mov_b64 exec, 1\n\tds_add_u32 %0, %1\n\ts_mov_b64 exec, -1" :: "v"(a), "v"(1u) : "memory");
}
DI void lds_arrive_nw(unsigned a) {
  asm volatile("s_mov_b64 exec, 1\n\tds_add_u32 %0, %1\n\ts_mov_b64 exec, -1" :: "v"(a), "v"(1u) : "memory");
}
DI void role_sync(unsigned a, unsigned nth) { lds_arrive(a); lds_wait_ge(a, nth * 4u); }
DI void role_sync_slow(unsigned a, unsigned nth) { lds_arrive(a); lds_wait_ge_slow(a, nth * 4u); }
DI void role_sync_idle(unsigned a, unsigned nth) {
  lds_arrive(a);
  unsigned v, sv, n;
  asm volatile(LDS_POLL("%0", "%3", "%1", "%2", "%4", "s_sleep 6\n\t") : "=&v"(v), "=&s"(sv), "=&s"(n) : "v"(a), "s"(nth * 4u) : "memory", "scc");
}
DI bool last_of_four(unsigned a, int lane, unsigned round) {
  unsigned old = 0;
  if (lane == 0) old = __hip_atomic_fetch_add((LAS unsigned*)(uintptr_t)a, 1u, __ATOMIC_RELAXED, __HIP_MEMORY_SCOPE_WORKGROUP);
  return (unsigned)__builtin_amdgcn_readfirstlane(old) == round * 4u + 3u;
}
DI void st128(u32x4 v, __amdgpu_buffer_rsrc_t r, unsigned voff, unsigned soff) {
  __builtin_amdgcn_raw_buffer_store_b128(v, r, voff, soff, AUX_SC1);
  asm volatile("s_nop 1" :: "v"(v) : "memory");
}
DI void drain_vm() { asm volatile("s_waitcnt vmcnt(0)" ::: "memory"); }
DI void flag_store(char* ws, unsigned off, u64 tag) { __hip_atomic_store((GAS u64*)(ws + off), tag, __ATOMIC_RELAXED, __HIP_MEMORY_SCOPE_AGENT); }
DI void flag_wait(char* ws, unsigned off, u64 tag) {
#pragma unroll 1
  for (unsigned n = 0; n < SPIN_CAP; ++n) {
    u64 v = __hip_atomic_load((GAS u64*)(ws + off), __ATOMIC_RELAXED, __HIP_MEMORY_SCOPE_AGENT);
    if (__builtin_amdgcn_ballot_w64(v != tag) == 0ull) break;
    __builtin_amdgcn_s_sleep(2);
  }
  asm volatile("" ::: "memory");
}
DI u32x4 rec_wait(__amdgpu_buffer_rsrc_t r, unsigned off, u64 tag) {
  u32x4 v;
#pragma unroll 1
  for (unsigned n = 0; n < SPIN_CAP; ++n) {
    v = __builtin_amdgcn_raw_buffer_load_b128(r, off, 0, AUX_SC1);
    if (__builtin_amdgcn_ballot_w64(v[0] != (unsigned)tag || v[2] != (unsigned)(tag >> 32)) == 0ull) break;
    asm volatile("s_sleep 1" ::: "memory");
  }
  asm volatile("" ::: "memory");
  return v;
}
template <int CTRL> DI unsigned dpp_max(unsigned v) { unsigned t = __builtin_amdgcn_update_dpp(0u, v, CTRL, 0xf, 0xf, false); return v > t ? v : t; }
DI unsigned wave_umax(unsigned v) {
  v = dpp_max<0x111>(v); v = dpp_max<0x112>(v); v = dpp_max<0x114>(v); v = dpp_max<0x118>(v);
  v = dpp_max<0x142>(v); v = dpp_max<0x143>(v);
  return __builtin_amdgcn_readlane(v, 63);
}
DI float half_swap_max(float v) {
  auto r = __builtin_amdgcn_permlane32_swap(f2u(v), f2u(v), false, false);
  return __builtin_fmaxf(u2f(r[0]), u2f(r[1]));
}
DI f32x2 cmul(f32x2 a, f32x2 b) { f32x2 r; r.x = __builtin_fmaf(-a.y, b.y, a.x * b.x); r.y = __builtin_fmaf(a.y, b.x, a.x * b.y); return r; }
DI void hmul3(unsigned a, unsigned b1, unsigned b2, unsigned b3, unsigned& r1, unsigned& r2, unsigned& r3) {
  asm("v_pk_mul_f16 %0, %3, %4 op_sel:[0,0] op_sel_hi:[0,1]\n\tv_pk_mul_f16 %1, %3, %5 op_sel:[0,0] op_sel_hi:[0,1]\n\tv_pk_mul_f16 %2, %3, %6 op_sel:[0,0] op_sel_hi:[0,1]\n\t"
      "v_pk_fma_f16 %0, %3, %4, %0 op_sel:[1,1,0] op_sel_hi:[1,0,1] neg_lo:[1,0,0]\n\tv_pk_fma_f16 %1, %3, %5, %1 op_sel:[1,1,0] op_sel_hi:[1,0,1] neg_lo:[1,0,0]\n\t"
      "v_pk_fma_f16 %2, %3, %6, %2 op_sel:[1,1,0] op_sel_hi:[1,0,1] neg_lo:[1,0,0]"
      : "=&v"(r1), "=&v"(r2), "=&v"(r3) : "v"(a), "v"(b1), "v"(b2), "v"(b3));
}
DI f32x2 zpow(float rho, float th, float p) {
  const float e = __builtin_amdgcn_exp2f(rho * p), f = __builtin_amdgcn_fractf(th * p);
  f32x2 r; r.x = e * __builtin_amdgcn_cosf(f); r.y = e * __builtin_amdgcn_sinf(f); return r;
}
#define STAMP(k) do { } while (0)
#define STAMP_FLUSH() do { } while (0)

#define QR(a, n) "v[" #a "+" #n ":" #a "+" #n "+1]"
#define Q_READ(F, XA, S) "ds_read_b128 v[" #F ":" #F "+3], " XA "\n\t" \
  "ds_read_b64_tr_b16 " QR(F, 4) ", %[wa] offset:" #S "*3072\n\tds_read_b64_tr_b16 " QR(F, 6) ", %[wa] offset:" #S "*3072+768\n\t" \
  "ds_read_b64_tr_b16 " QR(F, 8) ", %[wa] offset:" #S "*3072+56\n\tds_read_b64_tr_b16 " QR(F, 10) ", %[wa] offset:" #S "*3072+824\n\t" \
  "ds_read_b64_tr_b16 " QR(F, 12) ", %[wa] offset:" #S "*3072+112\n\tds_read_b64_tr_b16 " QR(F, 14) ", %[wa] offset:" #S "*3072+880\n\t"
#define Q_T(ACC, F, n) "v_mfma_f32_32x32x16_bf16 " ACC ", v[" #F "+" #n ":" #F "+" #n "+3], v[" #F ":" #F "+3], " ACC "\n\t"
#define Q_N(ACC, F, n) "v_mfma_f32_32x32x16_bf16 " ACC ", v[" #F ":" #F "+3], v[" #F "+" #n ":" #F "+" #n "+3], " ACC "\n\t"
#define Q_MMA(F, M0, M1, M2) M0("%[a0]", F, 4) M1("%[a1]", F, 8) M2("%[a2]", F, 12)
#define Q_ARRIVE "s_mov_b64 exec, 1\n\tds_add_u32 %[ca], %[one]\n\ts_mov_b64 exec, -1\n\t"
#define Q_TR(F, n, S, O) "ds_read_b64_tr_b16 " QR(F, n) ", %[wa] offset:" #S "*3072+" #O "\n\t"
#define Q_GROUP(G, F, XA, S, M0, M1, M2) \
  M0("%[a0]", G, 4) "ds_read_b128 v[" #F ":" #F "+3], " XA "\n\t" Q_TR(F, 4, S, 0) Q_TR(F, 6, S, 768) \
  M1("%[a1]", G, 8) Q_TR(F, 8, S, 56) Q_TR(F, 10, S, 824) \
  M2("%[a2]", G, 12) Q_TR(F, 12, S, 112) Q_TR(F, 14, S, 880)
#define QKV_BODY(M0, M1, M2) \
  "2:\n\t" \
  "v_add_u32 %[cl], 4, %[cl]\n\ts_waitcnt lgkmcnt(7)\n\t" \
  Q_GROUP(100, 132, "%[x2]", 2, M0, M1, M2) "ds_read_b32 %[tv], %[cl]\n\t" \
  "s_waitcnt lgkmcnt(8)\n\t" \
  Q_GROUP(116, 148, "%[x3]", 3, M0, M1, M2) \
  "s_cmp_eq_u32 %[tg], 28\n\ts_cbranch_scc1 5f\n\t" \
  "s_cmp_eq_u32 %[tg], 16\n\ts_cselect_b32 %[ts], %[wrap], %[step]\n\ts_add_u32 %[tg], %[tg], 4\n\t" \
  "v_add_u32 %[x0], %[ts], %[x0]\n\tv_add_u32 %[x1], %[ts], %[x1]\n\tv_add_u32 %[x2], %[ts], %[x2]\n\tv_add_u32 %[x3], %[ts], %[x3]\n\tv_add_u32 %[wa], %[ts], %[wa]\n\t" \
  "s_waitcnt lgkmcnt(7)\n\tv_readfirstlane_b32 %[ts], %[tv]\n\ts_cmp_ge_u32 %[ts], 2\n\ts_cbranch_scc1 0f\n\t" \
  Q_MMA(132, M0, M1, M2) \
  "s_cmp_lt_u32 %[tg], 16\n\ts_cbranch_scc1 3f\n\ts_waitcnt vmcnt(0)\n3:\n\t" \
  "s_waitcnt lgkmcnt(0)\n\t" Q_ARRIVE "s_nop 0\n\t" Q_MMA(148, M0, M1, M2) "v_add_u32 %[ca], 4, %[ca]\n\t" \
  LDS_POLL("%[tv]", "%[cl]", "%[ts]", "%[tn]", "2", "") \
  Q_READ(100, "%[x0]", 0) Q_READ(116, "%[x1]", 1) "s_branch 2b\n" \
  "0:\n\t" \
  Q_GROUP(132, 100, "%[x0]", 0, M0, M1, M2) \
  "s_cmp_lt_u32 %[tg], 16\n\ts_cbranch_scc1 3f\n\ts_waitcnt vmcnt(0)\n3:\n\t" \
  "s_waitcnt lgkmcnt(7)\n\t" Q_ARRIVE \
  Q_GROUP(148, 116, "%[x1]", 1, M0, M1, M2) \
  "v_add_u32 %[ca], 4, %[ca]\n\ts_branch 2b\n" \
  "5:\n\t" \
  "s_waitcnt lgkmcnt(7)\n\t" Q_MMA(132, M0, M1, M2) "s_waitcnt lgkmcnt(0)\n\t" Q_ARRIVE "s_nop 0\n\t" Q_MMA(148, M0, M1, M2)
#define QKV_LOOP() asm volatile( \
  "s_mov_b32 %[tg], 4\n\t" LDS_POLL("%[tv]", "%[cl]", "%[ts]", "%[tn]", "2", "") \
  Q_READ(100, "%[x0]", 0) Q_READ(116, "%[x1]", 1) \
  "s_cmp_gt_u32 %[g], 10\n\ts_cbranch_scc1 7f\n\ts_cmp_eq_u32 %[g], 10\n\ts_cbranch_scc1 6f\n" \
  QKV_BODY(Q_T, Q_T, Q_T) "s_branch 8f\n6:\n" QKV_BODY(Q_T, Q_T, Q_N) "s_branch 8f\n7:\n" QKV_BODY(Q_N, Q_N, Q_N) \
  "8:\n\ts_nop 7\n\ts_nop 7" \
  : [a0] "+v"(acc[0]), [a1] "+v"(acc[1]), [a2] "+v"(acc[2]), [x0] "+v"(xa0), [x1] "+v"(xa1), [x2] "+v"(xa2), [x3] "+v"(xa3), [wa] "+v"(wa), \
    [tv] "=&v"(tv), [ts] "=&s"(ts), [tn] "=&s"(tn), [tg] "=&s"(tg), [ca] "+v"(car), [cl] "+v"(cla) \
  : [one] "v"(1u), [step] "s"(SLOT_B), [wrap] "s"(0u - 3u * SLOT_B), [g] "s"(g) \
  : "memory", "scc", V100_163)
#define V10(p) "v" #p "0", "v" #p "1", "v" #p "2", "v" #p "3", "v" #p "4", "v" #p "5", "v" #p "6", "v" #p "7", "v" #p "8", "v" #p "9"
#define V100_163 V10(10), V10(11), V10(12), V10(13), V10(14), V10(15), "v160", "v161", "v162", "v163"

NOPK __global__ void __launch_bounds__(512, 2)
fused_mha_s4d(const char* __restrict__ x, const char* __restrict__ wqkv, const char* __restrict__ wo, const char* __restrict__ u,
              const char* __restrict__ logAr, const char* __restrict__ Aim, const char* __restrict__ Cre, const char* __restrict__ Cim,
              const char* __restrict__ logdt, const char* __restrict__ Dsk, char* __restrict__ out, char* __restrict__ ws) {
  extern __shared__ __attribute__((aligned(16))) char smem[];
  const int tid = __builtin_amdgcn_workitem_id_x(), lane = tid & 63;
  const int wid = __builtin_amdgcn_readfirstlane(tid >> 6);
  const int b = __builtin_amdgcn_workgroup_id_x(), xcd = b & 7, idx = b >> 3;
  const int r32 = lane & 31, hh = lane >> 5;
  const unsigned L0 = (unsigned)(uintptr_t)smem;
  (void)L0;
  if (wid >= 4) {
    u64 pc; unsigned len;
    asm volatile("s_getpc_b64 %0\n.Lpf_begin:\n\ts_mov_b32 %1, .Lpf_end-.Lpf_begin" : "=s"(pc), "=s"(len));
    const unsigned off = ((tid - 256) * 8 + (idx & 7)) * 128;
    if (off < len) asm volatile("s_mov_b32 m0, %2\n\ts_nop 0\n\tglobal_load_lds_dword %0, %1" :: "v"(off), "s"(pc), "s"(L_SINK) : "memory");
  }
  if (wid == 2) lds_st<u32x4>(L_MAX + lane * 16, u32x4{0u, 0u, 0u, 0u});
  asm volatile("s_waitcnt lgkmcnt(0)\n\ts_barrier" ::: "memory");
  const __amdgpu_buffer_rsrc_t wsr = __builtin_amdgcn_make_buffer_rsrc(ws, 0, WS_END, 0x00020000);
  asm volatile("" :: "s"(x), "s"(wqkv), "s"(wo), "s"(u), "s"(logAr), "s"(Aim), "s"(Cre), "s"(Cim), "s"(logdt), "s"(Dsk), "s"(out), "s"(ws));
  STAMP(0);
#define MAKE_TAG(e) (((u64)(unsigned)__builtin_amdgcn_readfirstlane((unsigned)((e) >> 32)) << 32 | (unsigned)__builtin_amdgcn_readfirstlane((unsigned)(e))) + 0x9E3779B97F4A7C15ull)
  const int rt = (xcd & 3) * 4 + (idx >> 3), g = (xcd >> 2) * 8 + (idx & 7);
  const int woff = (g == 15) ? 8 : 0;
  const bool has_b = xcd < 7;

  if (__builtin_expect(wid >= 4, 0)) {
    const int w = wid - 4;
    __builtin_amdgcn_s_setprio(APRIO);
    const u64 epoch = __hip_atomic_load((GAS u64*)(ws + WS_EPOCH), __ATOMIC_RELAXED, __HIP_MEMORY_SCOPE_AGENT);
    const int rt32 = rt * 4 + w;
    unsigned l16 = lane * 16, so[3], kma[3], vcst = r32 == 28 ? 0x3F803F80u : 0u; unsigned scl[3]; u64 vmask[3], pmask[3], qmask[3];
#pragma unroll
    for (int j = 0; j < 3; ++j) {
      const unsigned sl = 3 * g + j;
      unsigned mq = (unsigned)((int)(sl - 16) >> 31), mp = (unsigned)((int)(sl - 32) >> 31);
      asm volatile("" : "+s"(mq), "+s"(mp));
      const unsigned t64 = (sl & 15) * 64 + rt32;
      so[j] = ((WS_QKV + t64 * 2048) & mq) | ((WS_KV + t64 * 4096 + (2048u & ~mp)) & ~mq);
      scl[j] = (__builtin_bit_cast(unsigned, QSCALE) & mq) | (0x3F800000u & ~mq);
      const unsigned mv = 0xF0000000u & ~mp;
      vmask[j] = (u64)mv << 32 | mv; pmask[j] = (u64)mp << 32; qmask[j] = (u64)mq << 32;
      kma[j] = C_KM + j * 4;
      asm volatile("" : "+s"(so[j]), "+s"(scl[j]), "+s"(vmask[j]), "+s"(pmask[j]), "+s"(qmask[j]), "+v"(kma[j]));
    }
    PIN(l16); PIN(vcst);
    f32x16 acc[3];
#pragma unroll
    for (int j = 0; j < 3; ++j) acc[j] = f32x16{};
    {
      const unsigned xa_l = w * 4096 + r32 * 128, xsw = (r32 >> 1) & 7;
      unsigned xa0 = xa_l + (((0 + hh) ^ xsw) << 4), xa1 = xa_l + (((2 + hh) ^ xsw) << 4), xa2 = xa0 ^ 64, xa3 = xa1 ^ 64;
      unsigned wa = XCH_B + (8 * hh + ((lane & 15) >> 2)) * 192 + woff + ((lane >> 4) & 1) * 32 + (lane & 3) * 8;
      unsigned tv, ts, tn, tg, car = C_R, cla = C_L;
      asm volatile("" : "+v"(acc[0]), "+v"(acc[1]), "+v"(acc[2]), "+v"(xa0), "+v"(xa1), "+v"(xa2), "+v"(xa3), "+v"(wa), "+v"(car), "+v"(cla));
      if (has_b) {
        lds_wait_ge(C_UI, 2);
        const int oct = (lane >> 1) & 7;
        s16x8 v[2];
#pragma unroll
        for (int i = 0; i < 2; ++i) {
          const int ch = 2 * (w * 8 + i * 4 + (lane >> 4)) + (lane & 1);
          const unsigned a = L_UIMG + oct * 1024 + (((ch >> 3) ^ oct) << 4) + (ch & 7) * 2;
#pragma unroll
          for (int e = 0; e < 8; ++e) v[i][e] = lds_ld<short>(a + e * 128);
        }
#pragma unroll
        for (int i = 0; i < 2; ++i)
          st128(__builtin_bit_cast(u32x4, v[i]), wsr, ((xcd * 32 + w * 8 + i * 4 + (lane >> 4)) * 32 + (31 - idx)) * 256 + (lane & 15) * 16, WS_UT);
      }
      STAMP(9);
      QKV_LOOP();
    }
    STAMP(2);
#pragma unroll
    for (int j = 0; j < 3; ++j) {
      u32x4 f0, f1;
#pragma unroll
      for (int i = 0; i < 4; ++i) f0[i] = sel(pk_bf16(acc[j][2 * i] * u2f(scl[j]), acc[j][2 * i + 1] * u2f(scl[j])), vcst, vmask[j]);
      st128(f0, wsr, l16, so[j]);
#pragma unroll
      for (int i = 0; i < 4; ++i) f1[i] = sel(pk_bf16(acc[j][8 + 2 * i] * u2f(scl[j]), acc[j][9 + 2 * i] * u2f(scl[j])), vcst, vmask[j]);
      const unsigned n2 = f2u(frag_norm2(f0, f1, hh));
      asm volatile("ds_max_u32 %0, %1" :: "v"(kma[j]), "v"(n2) : "memory");
      f1[2] = sel(f1[2], sel(0x3F803F80u, n2, qmask[j]), pmask[j]); f1[3] = sel(f1[3], 0x00003F80u, pmask[j]);
      st128(f1, wsr, l16 + 1024, so[j]);
    }
    const u64 tag = MAKE_TAG(epoch);
    STAMP(14);
    const int h = xcd * 2 + (idx >> 4), qt = idx & 15;
    f32x16 o[4];
#pragma unroll
    for (int i = 0; i < 4; ++i) { o[i] = f32x16{}; PIN(o[i]); }
    if (w == 3) lds_wait_ge(C_BTAG, 4);
    drain_vm();
    STAMP(15);
    if (w == 3) {
      lds_wait_ge(C_APUB, 3);
      const u32x4 km = lds_ld<u32x4>(C_KM);
      const u32x4 rec = {(unsigned)tag, ((km[0] + 0xffffu) >> 16) | ((km[1] + 0xffffu) & 0xffff0000u), (unsigned)(tag >> 32), km[2] + 0xffffu};
      if (lane == 0) st128(rec, wsr, (g * 16 + rt) * 128, WS_QFLAG);
    } else {
      lds_arrive_nw(C_APUB);
      if (w == 0) {
        rec_wait(wsr, WS_QFLAG + ((h / 3) * 16 + qt) * 128, tag);
        lds_wait_ge(C_R + 24, 4);
        const char* qs = ws + WS_QKV + (h * 64 + qt * 4) * 2048;
        glds_dev<0>(l16, qs, L_RING); glds_dev<1024>(l16, qs + 1024, L_RING); glds_dev<2048>(l16, qs + 2048, L_RING); glds_dev<3072>(l16, qs + 3072, L_RING);
        glds_dev<4096>(l16, qs + 4096, L_RING); glds_dev<5120>(l16, qs + 5120, L_RING); glds_dev<6144>(l16, qs + 6144, L_RING); glds_dev<7168>(l16, qs + 7168, L_RING);
        drain_vm();
        lds_arrive_nw(C_Q);
      } else if (w == 1) {
        const u32x4 rec = rec_wait(wsr, WS_QFLAG + ((hh ? (32 + h) / 3 : (16 + h) / 3) * 16 + (lane & 15)) * 128, tag);
        const int j = (16 + h) % 3;
        unsigned v = j == 0 ? rec[1] << 16 : ((j == 1 ? rec[1] : rec[3]) & 0xffff0000u);
        v = dpp_max<0x111>(v); v = dpp_max<0x112>(v); v = dpp_max<0x114>(v); v = dpp_max<0x118>(v);
        lds_st<unsigned>(C_GO, __builtin_amdgcn_readlane(v, 15) + 1u);
      }
    }
    STAMP(3);
    s16x8 qf[4][2], kf[4][2], vf[4][2];
    const unsigned kbase = WS_KV + (h * 64 + w * 16) * 4096;
#define LDR(rs_, soff, voff) __builtin_bit_cast(s16x8, __builtin_amdgcn_raw_buffer_load_b128(rs_, l16 + (voff), (soff), AUX_SC1))
#define LDF(soff, voff) LDR(wsr, soff, voff)
#define LDKV(rs_, B, soff) do { SBAR(); kf[B][0] = LDR(rs_, soff, 0); kf[B][1] = LDR(rs_, soff, 1024); vf[B][0] = LDR(rs_, soff, 2048); vf[B][1] = LDR(rs_, soff, 3072); SBAR(); } while (0)
    lds_wait_ge_slow(C_Q, 1);
    STAMP(1);
#pragma unroll
    for (int i = 0; i < 4; ++i) { const unsigned a = L_RING + ((w + 1 + i) & 3) * 2048 + l16; qf[i][0] = lds_ld<s16x8>(a); qf[i][1] = lds_ld<s16x8>(a + 1024); }
    lds_arrive(C_QR);
    const unsigned km2 = lds_wait_ge_slow(C_GO, 1);
    STAMP(4);
    LDKV(wsr, 0, kbase);
    LDKV(wsr, 1, kbase + 4096); LDKV(wsr, 2, kbase + 8192);
    {
      const float kmax2 = u2f(km2) * 1.02f;
      unsigned pad2[4], pad3[4]; bool unsafe = false;
#pragma unroll
      for (int i = 0; i < 4; ++i) {
        const float m = __builtin_amdgcn_sqrtf(u2f(__builtin_bit_cast(u32x4, qf[i][1])[2]) * kmax2);
        unsafe |= hh && !(m <= SAFE_BOUND);
        pad2[i] = pk_bf16(-m, 0.f); pad3[i] = 0u;
      }
      if (__builtin_expect(__builtin_amdgcn_ballot_w64(unsafe) != 0ull, 0)) {
        float mx[4];
#pragma unroll
        for (int i = 0; i < 4; ++i) { mx[i] = -INFINITY; if (hh) { u32x4 t_ = __builtin_bit_cast(u32x4, qf[i][1]); t_[2] = 0u; t_[3] = 0u; qf[i][1] = __builtin_bit_cast(s16x8, t_); } }
#pragma unroll 1
        for (int t = 0; t < 16; ++t) {
          const s16x8 k0 = LDF(kbase + t * 4096, 0), k1 = LDF(kbase + t * 4096, 1024);
#pragma unroll
          for (int i = 0; i < 4; ++i) {
            f32x16 s = MFMA32(k0, qf[i][0], f32x16{}); s = MFMA32(k1, qf[i][1], s);
#pragma unroll
            for (int e = 0; e < 16; ++e) mx[i] = __builtin_fmaxf(mx[i], s[e]);
          }
        }
#pragma unroll
        for (int i = 0; i < 4; ++i) {
          const float v = half_swap_max(mx[i]);
          const unsigned k_ = f2u(v) ^ ((unsigned)((int)f2u(v) >> 31) | 0x80000000u);
          __hip_atomic_fetch_max((LAS unsigned*)(uintptr_t)(L_MAX + (((w + 1 + i) & 3) * 32 + r32) * 4), k_, __ATOMIC_RELAXED, __HIP_MEMORY_SCOPE_WORKGROUP);
        }
        role_sync(C_FB, 1);
#pragma unroll
        for (int i = 0; i < 4; ++i) {
          const unsigned k_ = lds_ld<unsigned>(L_MAX + (((w + 1 + i) & 3) * 32 + r32) * 4);
          const float m = u2f(k_ ^ ((unsigned)((int)~k_ >> 31) | 0x80000000u));
          const float m0 = bf_round(-m), r1 = -m - m0, m1 = bf_round(r1);
          pad2[i] = pk_bf16(m0, m1); pad3[i] = pk_bf16(r1 - m1, 0.f);
        }
      }
#pragma unroll
      for (int i = 0; i < 4; ++i) {
        u32x4 t_ = __builtin_bit_cast(u32x4, qf[i][1]);
        t_[2] = hh ? pad2[i] : t_[2]; t_[3] = hh ? pad3[i] : t_[3]; qf[i][1] = __builtin_bit_cast(s16x8, t_);
      }
    }
    f32x16 sA, sB; u32x4 p0, p1 = u32x4{0u, 0u, 0u, 0u};
    sA = MFMA32(kf[0][0], qf[0][0], f32x16{}); sA = MFMA32(kf[0][1], qf[0][1], sA);
    __builtin_amdgcn_s_setprio(1);
    STAMP(5);
    unsigned redw = L_B + l16; PIN(redw);
#define EX(S, i) S[i] = __builtin_amdgcn_exp2f(S[i])
#define PK(S, i) pk_bf16(S[i], S[(i) + 1])
#define BF(P) __builtin_bit_cast(s16x8, P)
#define STEP(C, N, KN, QN, VP, OP, VC, OC) do { \
      N = MFMA32(KN[0], QN[0], f32x16{}); SBAR(); EX(C, 0); EX(C, 1); EX(C, 2); EX(C, 3); EX(C, 4); SBAR(); \
      OP = MFMA32(VP[1], BF(p1), OP); SBAR(); EX(C, 5); EX(C, 6); EX(C, 7); p0[0] = PK(C, 0); p0[1] = PK(C, 2); p0[2] = PK(C, 4); p0[3] = PK(C, 6); PIN(p0); SBAR(); \
      N = MFMA32(KN[1], QN[1], N); SBAR(); EX(C, 8); EX(C, 9); EX(C, 10); EX(C, 11); EX(C, 12); SBAR(); \
      OC = MFMA32(VC[0], BF(p0), OC); SBAR(); EX(C, 13); EX(C, 14); EX(C, 15); p1[0] = PK(C, 8); p1[1] = PK(C, 10); p1[2] = PK(C, 12); p1[3] = PK(C, 14); PIN(p1); SBAR(); \
    } while (0)
#define RED_WRITE(i) do { const unsigned a_ = redw + (((w + 1 + (i)) & 3) * 3 + (2 - (i))) * 4096; \
      _Pragma("unroll") for (int q_ = 0; q_ < 4; ++q_) lds_st<f32x4>(a_ + q_ * 1024, f32x4{o[i][4 * q_], o[i][4 * q_ + 1], o[i][4 * q_ + 2], o[i][4 * q_ + 3]}); } while (0)
#define TILE(T, TP, TN, RS, LAST) do { \
      STEP(sA, sB, kf[T], qf[1], vf[TP], o[3], vf[T], o[0]); \
      if (!LAST) LDKV(RS, TP, kcur + (T + 3) * 4096); \
      STEP(sB, sA, kf[T], qf[2], vf[T], o[0], vf[T], o[1]); \
      if (LAST) RED_WRITE(0); \
      STEP(sA, sB, kf[T], qf[3], vf[T], o[1], vf[T], o[2]); \
      if (LAST) RED_WRITE(1); \
      STEP(sB, sA, kf[TN], qf[0], vf[T], o[2], vf[T], o[3]); \
      if (LAST) RED_WRITE(2); \
    } while (0)
    vf[3][1] = s16x8{};
    unsigned kcur = kbase;
#pragma unroll 1
    for (int tb = 0;; tb += 4) {
      const __amdgpu_buffer_rsrc_t wsr_next = __builtin_amdgcn_make_buffer_rsrc(ws, 0, tb < 12 ? WS_END : 0u, 0x00020000);
      TILE(0, 3, 1, wsr, false);
      TILE(1, 0, 2, wsr_next, false);
      TILE(2, 1, 3, wsr_next, false);
      if (tb == 12) break;
      TILE(3, 2, 0, wsr, false);
      kcur += 16384;
    }
    lds_wait_ge(C_BFREE, 4);
    TILE(3, 2, 0, wsr, true);
    o[3] = MFMA32(vf[3][1], BF(p1), o[3]);
    STAMP(6);
    role_sync(C_A, 1);
    {
      f32x4 pv[12];
#pragma unroll
      for (int k = 0; k < 12; ++k) pv[k] = lds_ld<f32x4>(redw + w * 12288 + k * 1024);
      SBAR();
      f32x16 a = o[3];
#pragma unroll
      for (int k = 0; k < 12; ++k) { const int q = k & 3; a[4 * q] += pv[k][0]; a[4 * q + 1] += pv[k][1]; a[4 * q + 2] += pv[k][2]; a[4 * q + 3] += pv[k][3]; }
      auto lr = __builtin_amdgcn_permlane32_swap(f2u(a[12]), f2u(a[12]), false, false);
      const float inv = __builtin_amdgcn_rcpf(u2f(lr[1]));
      u32x4 f0, f1;
#pragma unroll
      for (int i = 0; i < 4; ++i) { f0[i] = pk_bf16(a[2 * i] * inv, a[2 * i + 1] * inv); f1[i] = pk_bf16(a[8 + 2 * i] * inv, a[9 + 2 * i] * inv); }
      if (hh) { f1[2] = 0u; f1[3] = 0u; }
      const unsigned so = WS_O + ((qt * 4 + w) * 16 + h) * 2048;
      st128(f0, wsr, l16, so);
      st128(f1, wsr, l16 + 1024, so);
      lds_arrive(C_A);
      drain_vm();
      if (lane == 0) flag_store(ws, WS_OFLAG + ((qt * 4 + w) * 16 + h) * 128, tag);
    }
    STAMP(7);
    const int rs = xcd * 8 + (idx >> 2), cg = idx & 3, n0 = w < 3 ? 32 * w : 80;
    lds_wait_ge(C_A, 2 * 4);
    lds_wait_ge(C_WO, 4);
    flag_wait(ws, WS_OFLAG + (rs * 16 + w * 4 + (lane & 3)) * 128, tag);
    STAMP(8);
    {
      const char* os = ws + WS_O + (rs * 16 + w * 4) * 2048;
      const unsigned od = L_B + w * 8192;
      glds_dev<0>(l16, os, od); glds_dev<1024>(l16, os + 1024, od); glds_dev<2048>(l16, os + 2048, od); glds_dev<3072>(l16, os + 3072, od);
      glds_dev<4096>(l16, os + 4096, od); glds_dev<5120>(l16, os + 5120, od); glds_dev<6144>(l16, os + 6144, od); glds_dev<7168>(l16, os + 7168, od);
    }
    s16x8 wf[32];
    unsigned oa[4];
#pragma unroll
    for (int q = 0; q < 4; ++q) {
      const int gq = (w + q) & 3;
      unsigned wa = L_RING + (gq * 112 + 4 * hh + ((lane & 15) >> 2)) * 224 + n0 * 2 + ((lane >> 4) & 1) * 32 + (lane & 3) * 8;
      unsigned wb2 = wa + (hh ? 16 * 224 : 24 * 224);
      oa[q] = redw + gq * 8192;
      PIN(wa); PIN(wb2); PIN(oa[q]);
#pragma unroll
      for (int hd = 0; hd < 4; ++hd) {
        wf[q * 8 + hd * 2] = cat8(tr_read(wa + hd * 28 * 224), tr_read(wa + hd * 28 * 224 + 8 * 224));
        wf[q * 8 + hd * 2 + 1] = cat8(tr_read(wa + hd * 28 * 224 + 16 * 224), tr_read(wb2 + hd * 28 * 224));
      }
    }
    drain_vm();
    lds_arrive_nw(C_A);
    STAMP(13);
    f32x16 d = f32x16{};
    {
      s16x8 ob[8];
#pragma unroll
      for (int k = 0; k < 8; ++k) ob[k] = lds_ld<s16x8>(oa[0] + k * 1024);
      SBAR();
#pragma unroll
      for (int k = 0; k < 4; ++k) { d = MFMA32(wf[k], ob[k], d); SBAR(); }
      lds_wait_ge(C_A, 3 * 4);
#pragma unroll
      for (int k = 0; k < 4; ++k) ob[k] = lds_ld<s16x8>(oa[1] + k * 1024);
      SBAR();
#pragma unroll
      for (int k = 4; k < 32; ++k) {
        d = MFMA32(wf[k], ob[k & 7], d);
        if (k + 8 < 32) ob[k & 7] = lds_ld<s16x8>(oa[(k + 8) >> 3] + (k & 7) * 1024);
        SBAR();
      }
    }
    {
      char* op = out + (size_t)(rs * 32 * 1792 + (cg * 112 + n0) * 2) + (unsigned)(r32 * 1792 + hh * 16);
#pragma unroll
      for (int k = 0; k < 2; ++k) {
        const unsigned a0 = pk_bf16(d[8 * k], d[8 * k + 1]), a1 = pk_bf16(d[8 * k + 2], d[8 * k + 3]);
        const unsigned b0 = pk_bf16(d[8 * k + 4], d[8 * k + 5]), b1 = pk_bf16(d[8 * k + 6], d[8 * k + 7]);
        auto s0 = __builtin_amdgcn_permlane32_swap(a0, b0, false, false);
        auto s1 = __builtin_amdgcn_permlane32_swap(a1, b1, false, false);
        if (k || w < 3) *(GAS u32x4*)(op + k * 32) = u32x4{s0[0], s1[0], s0[1], s1[1]};
      }
    }
    STAMP(10);
    STAMP_FLUSH();
    asm volatile(".Lpf_end:");
  } else {
    const int wb = wid ^ 2;
    const int c = wb & 1, half = wb >> 1;
    const int d = (has_b ? xcd * 64 : 0) + idx * 2 + c;
    const unsigned po = d * 512 + lane * 4;
    unsigned pdt, p0[4], p1[4];
    u64 tag;
#define LOAD_P(P, O) do { P[0] = *(const GAS unsigned*)(logAr + (O) + po); P[1] = *(const GAS unsigned*)(Aim + (O) + po); \
      P[2] = *(const GAS unsigned*)(Cre + (O) + po); P[3] = *(const GAS unsigned*)(Cim + (O) + po); } while (0)
    const unsigned T = L_T + c * T_B;
    unsigned ga[2][11], wv[7], za;
    auto gather_addr = [&]() __attribute__((always_inline)) NOPK {
#pragma unroll
      for (int i = 0; i < 7; ++i) {
        const unsigned p = i * 64 + lane, row = (p * 37450u) >> 19; wv[i] = row * 896 + (p - row * 14) * 16; PIN(wv[i]);
      }
      za = L_B + c * CH_B + (lane < 28 ? 4 * KC_S + half * UC_S + 4096 + lane * 16 : (half * 2 + (lane > 30)) * KC_S + (lane - (lane > 30 ? 31 : 28)) * 16); PIN(za);
#pragma unroll
      for (int it = 0; it < 2; ++it) {
        const int l0 = 8 * (3 + it * 128 + half * 64 + lane) - 27;
        const float f0 = (float)l0;
#pragma unroll
        for (int k = 0; k < 11; k += 2) {
          const unsigned pk = pk_bf16(f0 + (float)k, f0 + (float)(k + 1));
          asm volatile("v_mad_u32_u16 %0, %1, 2, %2" : "=v"(ga[it][k]) : "v"(pk), "s"(T - 2u * 17024u));
          if (k < 10) asm volatile("v_mad_u32_u16 %0, %1, 2, %2 op_sel:[1,0,0,0]" : "=v"(ga[it][k + 1]) : "v"(pk), "s"(T - 2u * 17024u));
        }
        if (it == 0 && !half) {
#pragma unroll
          for (int k = 0; k < 11; ++k) { const int l = l0 + k; ga[0][k] = l < 128 ? T + 2 * ((k < 3 && l < 0) ? 641 : l) : ga[0][k]; PIN(ga[0][k]); }
        }
      }
    };
    if (half) {
      unsigned xo[4], wo_[6];
#pragma unroll
      for (int q = 0; q < 4; ++q) xo[q] = (q * 8 + (lane >> 3)) * 896 + (((lane & 7) ^ ((q & 1) * 4 + (lane >> 4))) << 4);
      const char* xb = x + (size_t)(rt * 128 + c * 64) * 896;
      const char* wq = wqkv + g * 168 - woff;
      int kc = ROT ? ((idx & 7) + 2 * (idx >> 3)) % 7 : 0;
      u64 epoch;
      asm volatile("global_load_dwordx2 %0, %1, %2 sc1" : "=v"(epoch) : "v"(0u), "s"(ws + WS_EPOCH) : "memory");
#define X_PIECES(B, D) do { glds<0>(xo[0], B, D); glds<1024>(xo[1], B, D); glds<2048>(xo[2], B, D); glds<3072>(xo[3], B, D); \
        glds<4096>(xo[0], (B) + 32 * 896, D); glds<5120>(xo[1], (B) + 32 * 896, D); glds<6144>(xo[2], (B) + 32 * 896, D); glds<7168>(xo[3], (B) + 32 * 896, D); } while (0)
#define W_PIECES(B, D) do { glds<0>(wo_[0], B, D); glds<1024>(wo_[1], B, D); glds<2048>(wo_[2], B, D); glds<3072>(wo_[3], B, D); glds<4096>(wo_[4], B, D); glds<5120>(wo_[5], B, D); } while (0)
      {
        const char* up = u + (size_t)(idx * 64 + 56 - 32 * c) * 896 + (has_b ? xcd * 128 : 0);
        const unsigned uo = (7 - (lane >> 3)) * 896, k4 = (lane & 7) ^ (4 * c);
        glds<0>(uo + (k4 << 4), up, L_UIMG + c * 4096); glds<1024>(uo + ((k4 ^ 1) << 4), up - 8 * 896, L_UIMG + c * 4096);
        glds<2048>(uo + ((k4 ^ 2) << 4), up - 16 * 896, L_UIMG + c * 4096); glds<3072>(uo + ((k4 ^ 3) << 4), up - 24 * 896, L_UIMG + c * 4096);
      }
      X_PIECES(xb + kc * 128, L_RING + c * 8192);
#pragma unroll
      for (int i = 0; i < 6; ++i) {
        const unsigned p = (6 * c + i) * 64 + lane, row = (p * 43691u) >> 19; unsigned e = p - row * 12; e = e > 10 ? 10 : e;
        wo_[i] = row * 2688 + e * 16;
      }
      W_PIECES(wq + kc * 64 * 2688, L_RING + XCH_B + c * 6144);
      asm volatile("s_waitcnt vmcnt(14)" : "+v"(epoch) :: "memory");
      lds_arrive_nw(C_UI);
      tag = MAKE_TAG(epoch); asm volatile("" :: "s"(tag)); lds_arrive_nw(C_BTAG);
#pragma unroll 1
      for (int n = 1; n < 7; ++n) {
        if (n >= 4) lds_wait_ge(C_R + 4 * (n - 4), 4);
        const unsigned slot = (n & 3) * SLOT_B;
        kc = kc == 6 ? 0 : kc + 1;
        X_PIECES(xb + kc * 128, L_RING + c * 8192 + slot);
        W_PIECES(wq + kc * 64 * 2688, L_RING + XCH_B + c * 6144 + slot);
        asm volatile("s_waitcnt vmcnt(14)" ::: "memory"); lds_arrive_nw(C_L + 4 * (n - 1));
        if (n == 1) STAMP(1);
      }
      pdt = *(const GAS unsigned short*)(logdt + d * 2);
      LOAD_P(p0, 256);
      asm volatile("s_waitcnt vmcnt(5)" ::: "memory");
      lds_arrive_nw(C_L + 24);
      if (c == 0 && has_b && lane == 0) flag_store(ws, WS_UFLAG + (xcd * 32 + idx) * 128, tag);
      STAMP(12);
      gather_addr();
#pragma unroll
      for (int i = 0; i < 4; ++i) { p0[i] <<= 16; p1[i] = 0u; }
      lds_wait_ge(C_R + 12, 4);
    } else {
      pdt = *(const GAS unsigned short*)(logdt + d * 2);
      LOAD_P(p0, 0); LOAD_P(p1, 256);
      const u64 epoch = __hip_atomic_load((GAS u64*)(ws + WS_EPOCH), __ATOMIC_RELAXED, __HIP_MEMORY_SCOPE_AGENT);
      gather_addr();
      asm volatile("" : "+v"(pdt), "+v"(p0[0]), "+v"(p1[0]), "+v"(p1[1]), "+v"(p1[2]), "+v"(p1[3]));
      tag = MAKE_TAG(epoch); asm volatile("" :: "s"(tag)); lds_arrive_nw(C_BTAG);
#pragma unroll
      for (int i = 0; i < 4; ++i) p1[i] &= 0xffff0000u;
    }
    __builtin_amdgcn_s_setprio(1);
    STAMP(9);
    if (has_b) {
      const float dt = bf_round(fexp(bf_lo(pdt)));
      const unsigned cm = umax(umax(umax(p0[2] << 16 & 0x7fff0000u, p0[2] & 0x7fff0000u), umax(p0[3] << 16 & 0x7fff0000u, p0[3] & 0x7fff0000u)),
                               umax(p1[2] & 0x7fff0000u, p1[3] & 0x7fff0000u));
      unsigned eb = wave_umax(cm) >> 23; eb = eb < 1u ? 1u : (eb > 253u ? 253u : eb);
      const float csc = u2f((254u - eb) << 23), cinv = u2f(eb << 23);
      const unsigned tb_ = (half ? 3 * SLOT_B : 4 * SLOT_B) + c * TAB_W;
      const unsigned lw = tb_ + (lane >> 3) * 1056 + ((lane >> 2) & 1) * 528 + (lane & 3) * 4;
      const unsigned hw = tb_ + TAB_HI + (lane >> 3) * 672 + ((lane >> 2) & 1) * 336 + (lane & 3) * 4;
      const unsigned lr = tb_ + hh * 528 + r32 * 16, hr = tb_ + TAB_HI + hh * 336 + r32 * 16;
      f32x16 kd = f32x16{};
      u64 uflag = 0;
#pragma unroll 1
      for (int r = half ? 2 : 0; r < 3; ++r) {
        const float rho = -bf_round(fexp(bf_hi(p0[0]))) * dt * LOG2E, th = bf_hi(p0[1]) * dt * 0.15915494309189535f;
        f32x2 hA, hB; hA.x = bf_hi(p0[2]) * csc; hA.y = bf_hi(p0[3]) * -csc;
#pragma unroll
        for (int i = 0; i < 4; ++i) p0[i] = r == 0 ? p0[i] << 16 : p1[i];
        const f32x2 z8 = zpow(rho, th, 8.f), z32 = zpow(rho, th, 32.f), z1 = zpow(rho, -th, 1.f), z256 = zpow(rho, -th, 256.f);
        const f32x2 z16 = cmul(z8, z8), z24 = cmul(z8, z16);
        const unsigned q1 = pk_f16(z8.x, z8.y), q2 = pk_f16(z16.x, z16.y), q3 = pk_f16(z24.x, z24.y);
        lds_st<unsigned>(lw, 0x00003C00u); lds_st<unsigned>(lw + 16, q1); lds_st<unsigned>(lw + 32, q2); lds_st<unsigned>(lw + 48, q3);
        f32x2 e = z32;
#pragma unroll
        for (int a = 1; a < 8; ++a) {
          const unsigned p_ = pk_f16(e.x, e.y); unsigned m1, m2, m3;
          if (a < 7) e = cmul(e, z32);
          hmul3(p_, q1, q2, q3, m1, m2, m3);
          lds_st<unsigned>(lw + 64 * a, p_); lds_st<unsigned>(lw + 64 * a + 16, m1); lds_st<unsigned>(lw + 64 * a + 32, m2); lds_st<unsigned>(lw + 64 * a + 48, m3);
        }
        uflag = __hip_atomic_load((GAS u64*)(ws + WS_UFLAG + (xcd * 32 + r32) * 128), __ATOMIC_RELAXED, __HIP_MEMORY_SCOPE_AGENT);
#define PUTH(i, v) lds_st<unsigned>(hw + (i) * 16, pk_f16((v).x, (v).y))
        PUTH(0, hA); f32x2 g0 = cmul(hA, z256); PUTH(8, g0);
        hB = cmul(hA, z1); PUTH(1, hB); hA = cmul(hB, z1); PUTH(2, hA); { f32x2 t_ = cmul(hA, z256); PUTH(9, t_); }
        hB = cmul(hA, z1); PUTH(3, hB); hA = cmul(hB, z1); PUTH(4, hA); f32x2 g4 = cmul(hA, z256); PUTH(10, g4);
        hB = cmul(hA, z1); PUTH(5, hB); hA = cmul(hB, z1); PUTH(6, hA); { f32x2 t_ = cmul(hA, z256); PUTH(11, t_); }
        hB = cmul(hA, z1); PUTH(7, hB);
        g0 = cmul(g0, z256); PUTH(12, g0); g4 = cmul(g4, z256); PUTH(13, g4);
        g0 = cmul(g0, z256); PUTH(14, g0); g4 = cmul(g4, z256); PUTH(15, g4);
#pragma unroll
        for (int i = 16; i < 21; ++i) { g0 = cmul(g0, z256); PUTH(i, g0); }
        h16x8 fa[8], fb[8];
#pragma unroll
        for (int s = 0; s < 8; ++s) { fa[s] = lds_ld<h16x8>(hr + s * 672); fb[s] = lds_ld<h16x8>(lr + s * 1056); }
        SBAR();
#pragma unroll
        for (int s = 0; s < 8; ++s) kd = MFMA32H(fa[s], fb[s], kd);
      }
      STAMP(10);
      if (__builtin_amdgcn_ballot_w64(uflag != tag) != 0ull) flag_wait(ws, WS_UFLAG + (xcd * 32 + r32) * 128, tag);
      asm volatile("" ::: "memory");
      u32x4 uv[2];
#pragma unroll
      for (int i = 0; i < 2; ++i) uv[i] = __builtin_amdgcn_raw_buffer_load_b128(wsr, wb * 2048 + i * 1024 + lane * 16, WS_UT + (xcd * 32 + idx) * 8192, AUX_SC1);
      STAMP(11);
      {
        const float ks = cinv * 2.f * dt;
        float kv[12];
#pragma unroll
        for (int i = 0; i < 12; ++i) kv[i] = kd[i] * ks;
        const unsigned kx = (c ? L_KX : L_T - 3072) + lane * 16;
        unsigned old = 0;
        if (lane == 0) old = __hip_atomic_fetch_add((LAS unsigned*)(uintptr_t)(C_KX + c * 8), 1u, __ATOMIC_RELAXED, __HIP_MEMORY_SCOPE_WORKGROUP);
        if (__builtin_amdgcn_readfirstlane(old) == 0) {
#pragma unroll
          for (int q = 0; q < 3; ++q) lds_st<f32x4>(kx + q * 1024, f32x4{kv[4 * q], kv[4 * q + 1], kv[4 * q + 2], kv[4 * q + 3]});
          lds_arrive(C_KX + c * 8 + 4);
        } else {
          lds_wait_ge(C_KX + c * 8 + 4, 1);
          const f32x4 v0 = lds_ld<f32x4>(kx), v1 = lds_ld<f32x4>(kx + 1024), v2 = lds_ld<f32x4>(kx + 2048);
          const unsigned i1 = T + (hh ? 384 + 2 * r32 : 256 + 4 * r32) * 2;
          const unsigned i2 = T + (hh ? (r32 == 0 ? 640 : 643) : 512 + r32) * 2, i3 = T + (hh ? 641 : 512 + r32) * 2, st3 = hh ? 0 : 64;
          lds_st<u32x2>(T + (8 * r32 + 4 * hh) * 2, u32x2{pk_bf16(kv[0] + v0[0], kv[1] + v0[1]), pk_bf16(kv[2] + v0[2], kv[3] + v0[3])});
          lds_st<unsigned>(i1, pk_bf16(kv[4] + v1[0], kv[5] + v1[1])); lds_st<unsigned>(i1 + (hh ? 128 : 4), pk_bf16(kv[6] + v1[2], kv[7] + v1[3]));
          const unsigned w0 = pk_bf16(kv[8] + v2[0], kv[9] + v2[1]), w1 = hh ? 0u : pk_bf16(kv[10] + v2[2], kv[11] + v2[3]);
          lds_st<short>(i2, (short)w0); lds_st<short>(i3 + st3, hh ? (short)0 : (short)(w0 >> 16)); lds_st<short>(i3 + 2 * st3, (short)w1); lds_st<short>(i3 + 3 * st3, (short)(w1 >> 16));
        }
        role_sync(C_B, 1);
      }
      STAMP(2);
      const unsigned cb = L_B + c * CH_B;
      {
        s16x2 P[2][6];
#pragma unroll
        for (int it = 0; it < 2; ++it)
#pragma unroll
          for (int k = 0; k < 11; ++k) P[it][k >> 1][k & 1] = lds_ld<short>(ga[it][k]);
        if (lane < 34) lds_st<u32x4>(za, u32x4{0u, 0u, 0u, 0u});
        if (half && lane == 0) lds_st<u32x2>(cb + 4 * KC_S + UC_S + 4088, u32x2{0u, 0u});
#pragma unroll
        for (int it = 0; it < 2; ++it) {
          unsigned Q[6], A[5];
#pragma unroll
          for (int k = 0; k < 6; ++k) Q[k] = __builtin_bit_cast(unsigned, P[it][k]);
#pragma unroll
          for (int k = 0; k < 5; ++k) A[k] = __builtin_amdgcn_alignbit(Q[k + 1], Q[k], 16);
          const unsigned a = cb + (3 + it * 128 + half * 64 + lane) * 16;
          lds_st<u32x4>(a, u32x4{Q[0], Q[1], Q[2], Q[3]}); lds_st<u32x4>(a + KC_S, u32x4{A[0], A[1], A[2], A[3]});
          lds_st<u32x4>(a + 2 * KC_S, u32x4{Q[1], Q[2], Q[3], Q[4]}); lds_st<u32x4>(a + 3 * KC_S, u32x4{A[1], A[2], A[3], A[4]});
        }
      }
      STAMP(3);
#pragma unroll
      for (int i = 0; i < 2; ++i) {
        const int p = wb * 128 + i * 64 + lane;
        const unsigned a = L_B + (p & 1) * CH_B + 4 * KC_S + (p >> 1) * 16;
        lds_st<u32x4>(a, uv[i]); lds_st<u32x2>(a + UC_S - 8, u32x2{uv[i][0], uv[i][1]}); lds_st<u32x2>(a + UC_S, u32x2{uv[i][2], uv[i][3]});
      }
      role_sync(C_B, 2);
      __builtin_amdgcn_s_setprio(BCONV);
      STAMP(5);
      f32x4 y[8];
#pragma unroll
      for (int i = 0; i < 8; ++i) y[i] = f32x4{0.f, 0.f, 0.f, 0.f};
      {
        const int r16 = lane & 15, kq = lane >> 4;
        unsigned ka = cb + (r16 & 3) * KC_S + ((r16 >> 2) + kq) * 16 + half * 256;
        unsigned ua = cb + 4 * KC_S + (1 - (r16 & 1)) * UC_S + (2016 - 32 * (r16 >> 1) + 8 * kq) * 2 - 7 * 512 + half * 256;
        PIN(ka); PIN(ua);
        s16x8 af[2][8], bf[2][8];
#pragma unroll
        for (int m = 0; m < 8; ++m) { bf[0][7 - m] = lds_ld<s16x8>(ua + m * 512); af[0][m] = lds_ld<s16x8>(ka + m * 512); }
        auto conv_class = [&](auto sc, auto oc) __attribute__((always_inline)) NOPK {
          constexpr int S = decltype(sc)::value, O = decltype(oc)::value;
          int k = 0;
#pragma unroll
          for (int n = 7; n >= 0; --n)
#pragma unroll
            for (int m = 0; m + n < 8; ++m) {
              y[m + n] = MFMA16(af[S][m], bf[S][n], y[m + n]);
              if (k >= 4 && k < 36 && (k & 1) == 0) {
                const int q = (k - 4) >> 2;
                if (k & 2) af[1 - S][q] = lds_ld<s16x8>(ka + O + q * 512); else bf[1 - S][7 - q] = lds_ld<s16x8>(ua + O + q * 512);
              }
              SBAR();
              ++k;
            }
        };
#pragma unroll 1
        for (int it = 0; it < 2; ++it) {
          conv_class(IC<0>{}, IC<64>{});
          conv_class(IC<1>{}, IC<128>{});
          ka += 128; ua += 128;
        }
      }
      PIN(y[7]); STAMP(13);
      role_sync_slow(C_B, 3);
      __builtin_amdgcn_s_setprio(2);
#pragma unroll
      for (int i = 0; i < 4; ++i) lds_st<f32x4>(L_B + wb * 4096 + i * 1024 + lane * 16, half ? y[i] : y[4 + i]);
      role_sync_slow(C_B, 4);
      STAMP(14);
      {
        const int cc = lane & 15, kq = lane >> 4;
#pragma unroll
        for (int i = 0; i < 4; ++i) {
          const f32x4 v = lds_ld<f32x4>(L_B + (wb ^ 2) * 4096 + i * 1024 + lane * 16) + (half ? y[4 + i] : y[i]);
          const int t64 = 4 * (half * 4 + i) + (cc >> 2);
          __builtin_amdgcn_raw_buffer_store_b64(u32x2{pk_bf16(v[0], v[1]), pk_bf16(v[2], v[3])}, wsr,
                                                ((xcd * 32 + t64) * 64 + idx * 2 + c) * 128 + (32 * ((cc >> 1) & 1) + 8 * kq + 4 * (cc & 1)) * 2, WS_YT, AUX_SC1);
        }
      }
      lds_arrive(C_BFREE);
      __builtin_amdgcn_s_setprio(0);
      STAMP(15);
    } else {
      lds_arrive(C_BFREE);
      __builtin_amdgcn_s_setprio(0);
    }
    lds_wait_ge_slow(C_QR, 4);
#pragma unroll 1
    for (int i = wb; i < 14; i += 4) {
      const char* sb = wo + i * 32 * 896 + (idx & 3) * 224;
      const unsigned dst = L_RING + i * 7168;
#define WO_PIECE(k) glds<(k) * 1024>(wv[k], sb, dst); asm volatile("s_waitcnt vmcnt(2)" ::: "memory")
      WO_PIECE(0); WO_PIECE(1); WO_PIECE(2);
      if (i < 4 && has_b) {
        if (last_of_four(C_BPUB, lane, 0)) { if (lane == 0) flag_store(ws, WS_YFLAG + (xcd * 32 + idx) * 128, tag); }
        STAMP(6);
      }
      WO_PIECE(3); WO_PIECE(4); WO_PIECE(5); WO_PIECE(6);
    }
    drain_vm();
    lds_arrive(C_WO);
    STAMP(7);
    if (has_b) {
      const int tl = wb * 16 + (lane & 15);
      u32x4 uu[2], ds[2];
#pragma unroll
      for (int i = 0; i < 2; ++i) {
        const int oct = (lane >> 4) + 4 * i;
        uu[i] = *(const GAS u32x4*)(u + (size_t)(idx * 64 * 896 + xcd * 128) + (unsigned)(tl * 896 + oct * 16));
        ds[i] = *(const GAS u32x4*)(Dsk + xcd * 128 + (unsigned)(oct * 16));
      }
      if (wb == 0) flag_wait(ws, WS_YFLAG + (xcd * 32 + r32) * 128, tag);
      role_sync_idle(C_B, 5);
      u32x4 v[2];
#pragma unroll
      for (int i = 0; i < 2; ++i) v[i] = __builtin_amdgcn_raw_buffer_load_b128(wsr, wb * 2048 + i * 1024 + lane * 16, WS_YT + (xcd * 32 + idx) * 8192, AUX_SC1);
#pragma unroll
      for (int i = 0; i < 2; ++i) lds_st<u32x4>(L_YIMG + wb * 2048 + i * 1024 + lane * 16, v[i]);
      role_sync_idle(C_B, 6);
#pragma unroll
      for (int i = 0; i < 2; ++i) {
        const int oct = (lane >> 4) + 4 * i;
        const unsigned a = L_YIMG + (8 * oct + ((lane & 15) >> 2)) * 128 + (wb * 16 + (lane & 3) * 4) * 2;
        const u32x4 yy = __builtin_bit_cast(u32x4, cat8(tr_read(a), tr_read(a + 512)));
        u32x4 r;
#pragma unroll
        for (int k = 0; k < 4; ++k) {
          const unsigned sk = pk_bf16(bf_lo(uu[i][k]) * bf_lo(ds[i][k]), bf_hi(uu[i][k]) * bf_hi(ds[i][k]));
          r[k] = pk_bf16(bf_lo(yy[k]) + bf_lo(sk), bf_hi(yy[k]) + bf_hi(sk));
        }
        *(GAS u32x4*)(out + (size_t)(idx * 64 * 1792 + 896 + xcd * 128) + (unsigned)(tl * 1792 + oct * 16)) = r;
      }
      STAMP(8);
    } else if (b == 7 && wb == 0) {
#pragma unroll 1
      for (int i = 0; i < 4; ++i) rec_wait(wsr, WS_QFLAG + (i * 64 + lane) * 128, tag);
      if (lane == 0) flag_store(ws, WS_EPOCH, tag);
    }
    STAMP_FLUSH();
  }
}

extern "C" void kernel_launch(void* const* d_in, const int* in_sizes, int n_in, void* d_out, int out_size, void* d_ws, size_t ws_size,
                              hipStream_t stream) {
  (void)in_sizes; (void)n_in; (void)out_size; (void)ws_size;
  (void)hipFuncSetAttribute((const void*)fused_mha_s4d, hipFuncAttributeMaxDynamicSharedMemorySize, LDS_BYTES);
  fused_mha_s4d<<<256, 512, LDS_BYTES, stream>>>((const char*)d_in[0], (const char*)d_in[1], (const char*)d_in[2], (const char*)d_in[3],
                                                 (const char*)d_in[4], (const char*)d_in[5], (const char*)d_in[6], (const char*)d_in[7],
                                                 (const char*)d_in[8], (const char*)d_in[9], (char*)d_out,
                                                 (char*)d_ws + (WSEND ? ((ws_size - WS_END) & ~(size_t)0xfffff) : 0));
}
```

```cpp
#include <hip/hip_runtime.h>
#include <hip/hip_bf16.h>
#include <stdint.h>

typedef short s16x8 __attribute__((ext_vector_type(8)));
typedef short s16x4 __attribute__((ext_vector_type(4)));
typedef short s16x2 __attribute__((ext_vector_type(2)));
typedef _Float16 h16x8 __attribute__((ext_vector_type(8)));
typedef _Float16 h16x2 __attribute__((ext_vector_type(2)));
typedef __bf16 b16x2 __attribute__((ext_vector_type(2)));
typedef float f32x16 __attribute__((ext_vector_type(16)));
typedef float f32x4 __attribute__((ext_vector_type(4)));
typedef float f32x2 __attribute__((ext_vector_type(2)));
typedef unsigned u32x4 __attribute__((ext_vector_type(4)));
typedef unsigned u32x16 __attribute__((ext_vector_type(16)));
typedef unsigned u32x2 __attribute__((ext_vector_type(2)));
typedef unsigned long long u64;
#define LAS __attribute__((address_space(3)))
#define GAS __attribute__((address_space(1)))
#if defined(__HIP_DEVICE_COMPILE__)
#define NOPK __attribute__((target("no-packed-fp32-ops")))
#else
#define NOPK
#endif
#define DI NOPK __device__ __forceinline__
#define SBAR() __builtin_amdgcn_sched_barrier(0)
#define PIN(x) asm volatile("" : "+v"(x))
#define MFMA32(a, b, c) __builtin_amdgcn_mfma_f32_32x32x16_bf16((a), (b), (c), 0, 0, 0)
#define MFMA32H(a, b, c) __builtin_amdgcn_mfma_f32_32x32x16_f16((a), (b), (c), 0, 0, 0)
#define MFMA16(a, b, c) __builtin_amdgcn_mfma_f32_16x16x32_bf16((a), (b), (c), 0, 0, 0)
#define AUX_SC1 16

#define PREFETCH 1
#define WSEND 0
#define APRIO 3
#define BCONV 0
#define ROT 0
#define AT_ALIGN "5"
#define STAMPS 0

constexpr unsigned WS_EPOCH = 0;
constexpr unsigned WS_QFLAG = 4096;
constexpr unsigned WS_OFLAG = WS_QFLAG + 256 * 128;
constexpr unsigned WS_UFLAG = WS_OFLAG + 1024 * 128;
constexpr unsigned WS_YFLAG = WS_UFLAG + 256 * 128;
constexpr unsigned WS_QKV = 393216;
constexpr unsigned WS_KV = WS_QKV + 16 * 64 * 2048;
constexpr unsigned WS_O = WS_QKV + 48 * 64 * 2048;
constexpr unsigned WS_UT = WS_O + 64 * 16 * 2048;
constexpr unsigned WS_YT = WS_UT + 256 * 8192;
constexpr unsigned WS_DBG = WS_YT + 224 * 8192;
constexpr unsigned WS_END = WS_DBG + 256 * 8 * 16 * 8;
static_assert(WS_YFLAG + 256 * 128 <= WS_QKV, "ws map");

constexpr unsigned L_RING = 0, SLOT_B = 28672, XCH_B = 16384;
constexpr unsigned L_B = 100352;
constexpr unsigned TAB_W = 14000, TAB_HI = 8448;
constexpr unsigned L_UIMG = 4 * SLOT_B + 2 * TAB_W;
constexpr unsigned L_T = 156352, T_B = 1296, L_KX = L_T + 2 * T_B;
constexpr unsigned KC_S = 4160, UC_S = 4640, CH_B = 4 * KC_S + 2 * UC_S;
constexpr unsigned L_YIMG = L_B + 49152;
constexpr unsigned L_CTL = 163584;
constexpr unsigned LDS_BYTES = 163840;
constexpr unsigned C_A = L_CTL, C_B = L_CTL + 4, C_GO = L_CTL + 8, C_APUB = L_CTL + 12, C_BPUB = L_CTL + 16, C_WO = L_CTL + 20, C_BFREE = L_CTL + 24, C_BTAG = L_CTL + 28, C_U = L_CTL + 32, C_Q = L_CTL + 36, C_QR = L_CTL + 40, C_FB = L_CTL + 44, C_KM = L_CTL + 48, C_UI = L_CTL + 60, C_KX = L_CTL + 128;
constexpr unsigned C_L = L_CTL + 64, C_R = L_CTL + 96;
constexpr unsigned L_MAX = 162816;

constexpr unsigned L_SINK = L_MAX + 512;
constexpr unsigned SPIN_CAP = 1u << 22;
constexpr float LOG2E = 1.4426950408889634f;
constexpr float QSCALE = 0.18898223650461362f * LOG2E;
constexpr float SAFE_BOUND = 48.0f;

template <int V> struct IC { static constexpr int value = V; };
DI unsigned f2u(float f) { return __builtin_bit_cast(unsigned, f); }
DI float u2f(unsigned u) { return __builtin_bit_cast(float, u); }
DI unsigned umax(unsigned a, unsigned b) { return a > b ? a : b; }
DI unsigned sel(unsigned a, unsigned b, u64 mask) { return __builtin_amdgcn_inverse_ballot_w64(mask) ? b : a; }
DI float fexp(float x) { return __builtin_amdgcn_exp2f(x * LOG2E); }
DI float bf_lo(unsigned v) { return u2f(v << 16); }
DI float bf_hi(unsigned v) { return u2f(v & 0xffff0000u); }
DI unsigned pk_bf16(float a, float b) { f32x2 v = {a, b}; return __builtin_bit_cast(unsigned, __builtin_convertvector(v, b16x2)); }
DI unsigned pk_f16(float a, float b) { f32x2 v = {a, b}; return __builtin_bit_cast(unsigned, __builtin_convertvector(v, h16x2)); }
DI float bf_round(float a) { return bf_lo(pk_bf16(a, a)); }
template <int OFF> DI void glds(unsigned voff, const char* sbase, unsigned lds) {
  asm volatile("s_add_u32 m0, %2, %3\n\ts_nop 0\n\tglobal_load_lds_dwordx4 %0, %1" :: "v"(voff), "s"(sbase), "s"(lds), "i"(OFF) : "memory", "scc");
}
DI float half_swap_add(float v) {
  auto r = __builtin_amdgcn_permlane32_swap(f2u(v), f2u(v), false, false);
  return u2f(r[0]) + u2f(r[1]);
}
template <int OFF> DI void glds_dev(unsigned voff, const char* sbase, unsigned lds) {
  asm volatile("s_add_u32 m0, %2, %3\n\ts_nop 0\n\tglobal_load_lds_dwordx4 %0, %1 sc1" :: "v"(voff), "s"(sbase), "s"(lds), "i"(OFF) : "memory", "scc");
}
DI float dot2(unsigned a, float c) { return __builtin_amdgcn_fdot2_f32_bf16(__builtin_bit_cast(b16x2, a), __builtin_bit_cast(b16x2, a), c, false); }
DI float frag_norm2(u32x4 f0, u32x4 f1, int hh) {
  float a = dot2(f0[0], 0.f), b = dot2(f0[1], 0.f), c = dot2(f1[2], 0.f);
  a = dot2(f0[2], a); b = dot2(f0[3], b); c = dot2(f1[3], c); a = dot2(f1[0], a); b = dot2(f1[1], b);
  return half_swap_add(a + b + (hh ? 0.f : c));
}
DI s16x4 tr_read(unsigned a) { return __builtin_bit_cast(s16x4, __builtin_amdgcn_ds_read_tr16_b64_v4i16((LAS s16x4*)(uintptr_t)a)); }
DI s16x8 cat8(s16x4 lo, s16x4 hi) { return __builtin_shufflevector(lo, hi, 0, 1, 2, 3, 4, 5, 6, 7); }
template <class T> DI T lds_ld(unsigned a) { return *(LAS T*)(uintptr_t)a; }
template <class T> DI void lds_st(unsigned a, T v) { *(LAS T*)(uintptr_t)a = v; }
#define LDS_POLL(V, A, S, N, T, EXTRA) "s_mov_b32 " N ", 0\n1:\n\t" EXTRA "ds_read_b32 " V ", " A "\n\ts_add_u32 " N ", " N ", 1024\n\t" \
  "s_waitcnt lgkmcnt(0)\n\tv_readfirstlane_b32 " S ", " V "\n\ts_cbranch_scc1 4f\n\ts_cmp_lt_u32 " S ", " T "\n\ts_cbranch_scc1 1b\n4:\n\t"
DI unsigned lds_wait_ge(unsigned a, unsigned target) {
  unsigned v, sv, n;
  asm volatile(LDS_POLL("%0", "%3", "%1", "%2", "%4", "") : "=&v"(v), "=&s"(sv), "=&s"(n) : "v"(a), "s"(target) : "memory", "scc");
  return sv;
}
DI unsigned lds_wait_ge_slow(unsigned a, unsigned target) {
  unsigned v, sv, n;
  asm volatile(LDS_POLL("%0", "%3", "%1", "%2", "%4", "s_sleep 1\n\t") : "=&v"(v), "=&s"(sv), "=&s"(n) : "v"(a), "s"(target) : "memory", "scc");
  return sv;
}
DI void lds_arrive(unsigned a) {
  asm volatile("s_waitcnt lgkmcnt(0)\n\ts_mov_b64 exec, 1\n\tds_add_u32 %0, %1\n\ts_mov_b64 exec, -1" :: "v"(a), "v"(1u) : "memory");
}
DI void lds_arrive_nw(unsigned a) {
  asm volatile("s_mov_b64 exec, 1\n\tds_add_u32 %0, %1\n\ts_mov_b64 exec, -1" :: "v"(a), "v"(1u) : "memory");
}
DI void role_sync(unsigned a, unsigned nth) { lds_arrive(a); lds_wait_ge(a, nth * 4u); }
DI void role_sync_slow(unsigned a, unsigned nth) { lds_arrive(a); lds_wait_ge_slow(a, nth * 4u); }
DI void role_sync_idle(unsigned a, unsigned nth) {
  lds_arrive(a);
  unsigned v, sv, n;
  asm volatile(LDS_POLL("%0", "%3", "%1", "%2", "%4", "s_sleep 6\n\t") : "=&v"(v), "=&s"(sv), "=&s"(n) : "v"(a), "s"(nth * 4u) : "memory", "scc");
}
DI bool last_of_four(unsigned a, int lane, unsigned round) {
  unsigned old = 0;
  if (lane == 0) old = __hip_atomic_fetch_add((LAS unsigned*)(uintptr_t)a, 1u, __ATOMIC_RELAXED, __HIP_MEMORY_SCOPE_WORKGROUP);
  return (unsigned)__builtin_amdgcn_readfirstlane(old) == round * 4u + 3u;
}
DI void st128(u32x4 v, __amdgpu_buffer_rsrc_t r, unsigned voff, unsigned soff) {
  __builtin_amdgcn_raw_buffer_store_b128(v, r, voff, soff, AUX_SC1);
  asm volatile("s_nop 1" :: "v"(v) : "memory");
}
DI void drain_vm() { asm volatile("s_waitcnt vmcnt(0)" ::: "memory"); }
DI void flag_store(char* ws, unsigned off, u64 tag) { __hip_atomic_store((GAS u64*)(ws + off), tag, __ATOMIC_RELAXED, __HIP_MEMORY_SCOPE_AGENT); }
DI void flag_wait(char* ws, unsigned off, u64 tag) {
#pragma unroll 1
  for (unsigned n = 0; n < SPIN_CAP; ++n) {
    u64 v = __hip_atomic_load((GAS u64*)(ws + off), __ATOMIC_RELAXED, __HIP_MEMORY_SCOPE_AGENT);
    if (__builtin_amdgcn_ballot_w64(v != tag) == 0ull) break;
    __builtin_amdgcn_s_sleep(2);
  }
  asm volatile("" ::: "memory");
}
DI u32x4 rec_wait(__amdgpu_buffer_rsrc_t r, unsigned off, u64 tag) {
  u32x4 v;
#pragma unroll 1
  for (unsigned n = 0; n < SPIN_CAP; ++n) {
    v = __builtin_amdgcn_raw_buffer_load_b128(r, off, 0, AUX_SC1);
    if (__builtin_amdgcn_ballot_w64(v[0] != (unsigned)tag || v[2] != (unsigned)(tag >> 32)) == 0ull) break;
    asm volatile("s_sleep 1" ::: "memory");
  }
  asm volatile("" ::: "memory");
  return v;
}
template <int CTRL> DI unsigned dpp_max(unsigned v) { unsigned t = __builtin_amdgcn_update_dpp(0u, v, CTRL, 0xf, 0xf, false); return v > t ? v : t; }
DI unsigned wave_umax(unsigned v) {
  v = dpp_max<0x111>(v); v = dpp_max<0x112>(v); v = dpp_max<0x114>(v); v = dpp_max<0x118>(v);
  v = dpp_max<0x142>(v); v = dpp_max<0x143>(v);
  return __builtin_amdgcn_readlane(v, 63);
}
DI float half_swap_max(float v) {
  auto r = __builtin_amdgcn_permlane32_swap(f2u(v), f2u(v), false, false);
  return __builtin_fmaxf(u2f(r[0]), u2f(r[1]));
}
DI f32x2 cmul(f32x2 a, f32x2 b) { f32x2 r; r.x = __builtin_fmaf(-a.y, b.y, a.x * b.x); r.y = __builtin_fmaf(a.y, b.x, a.x * b.y); return r; }
DI void hmul3(unsigned a, unsigned b1, unsigned b2, unsigned b3, unsigned& r1, unsigned& r2, unsigned& r3) {
  asm("v_pk_mul_f16 %0, %3, %4 op_sel:[0,0] op_sel_hi:[0,1]\n\tv_pk_mul_f16 %1, %3, %5 op_sel:[0,0] op_sel_hi:[0,1]\n\tv_pk_mul_f16 %2, %3, %6 op_sel:[0,0] op_sel_hi:[0,1]\n\t"
      "v_pk_fma_f16 %0, %3, %4, %0 op_sel:[1,1,0] op_sel_hi:[1,0,1] neg_lo:[1,0,0]\n\tv_pk_fma_f16 %1, %3, %5, %1 op_sel:[1,1,0] op_sel_hi:[1,0,1] neg_lo:[1,0,0]\n\t"
      "v_pk_fma_f16 %2, %3, %6, %2 op_sel:[1,1,0] op_sel_hi:[1,0,1] neg_lo:[1,0,0]"
      : "=&v"(r1), "=&v"(r2), "=&v"(r3) : "v"(a), "v"(b1), "v"(b2), "v"(b3));
}
DI f32x2 zpow(float rho, float th, float p) {
  const float e = __builtin_amdgcn_exp2f(rho * p), f = __builtin_amdgcn_fractf(th * p);
  f32x2 r; r.x = e * __builtin_amdgcn_cosf(f); r.y = e * __builtin_amdgcn_sinf(f); return r;
}
#define STAMP(k) do { } while (0)
#define STAMP_FLUSH() do { } while (0)

#define QR(a, n) "v[" #a "+" #n ":" #a "+" #n "+1]"
#define Q_READ(F, XA, S) "ds_read_b128 v[" #F ":" #F "+3], " XA "\n\t" \
  "ds_read_b64_tr_b16 " QR(F, 4) ", %[wa] offset:" #S "*3072\n\tds_read_b64_tr_b16 " QR(F, 6) ", %[wa] offset:" #S "*3072+768\n\t" \
  "ds_read_b64_tr_b16 " QR(F, 8) ", %[wa] offset:" #S "*3072+56\n\tds_read_b64_tr_b16 " QR(F, 10) ", %[wa] offset:" #S "*3072+824\n\t" \
  "ds_read_b64_tr_b16 " QR(F, 12) ", %[wa] offset:" #S "*3072+112\n\tds_read_b64_tr_b16 " QR(F, 14) ", %[wa] offset:" #S "*3072+880\n\t"
#define Q_T(ACC, F, n) "v_mfma_f32_32x32x16_bf16 " ACC ", v[" #F "+" #n ":" #F "+" #n "+3], v[" #F ":" #F "+3], " ACC "\n\t"
#define Q_N(ACC, F, n) "v_mfma_f32_32x32x16_bf16 " ACC ", v[" #F ":" #F "+3], v[" #F "+" #n ":" #F "+" #n "+3], " ACC "\n\t"
#define Q_MMA(F, M0, M1, M2) M0("%[a0]", F, 4) M1("%[a1]", F, 8) M2("%[a2]", F, 12)
#define Q_ARRIVE "s_mov_b64 exec, 1\n\tds_add_u32 %[ca], %[one]\n\ts_mov_b64 exec, -1\n\t"
#define Q_TR(F, n, S, O) "ds_read_b64_tr_b16 " QR(F, n) ", %[wa] offset:" #S "*3072+" #O "\n\t"
#define Q_GROUP(G, F, XA, S, M0, M1, M2) \
  M0("%[a0]", G, 4) "ds_read_b128 v[" #F ":" #F "+3], " XA "\n\t" Q_TR(F, 4, S, 0) Q_TR(F, 6, S, 768) \
  M1("%[a1]", G, 8) Q_TR(F, 8, S, 56) Q_TR(F, 10, S, 824) \
  M2("%[a2]", G, 12) Q_TR(F, 12, S, 112) Q_TR(F, 14, S, 880)
#define QKV_BODY(M0, M1, M2) \
  "2:\n\t" \
  "v_add_u32 %[cl], 4, %[cl]\n\ts_waitcnt lgkmcnt(7)\n\t" \
  Q_GROUP(100, 132, "%[x2]", 2, M0, M1, M2) "ds_read_b32 %[tv], %[cl]\n\t" \
  "s_waitcnt lgkmcnt(8)\n\t" \
  Q_GROUP(116, 148, "%[x3]", 3, M0, M1, M2) \
  "s_cmp_eq_u32 %[tg], 28\n\ts_cbranch_scc1 5f\n\t" \
  "s_cmp_eq_u32 %[tg], 16\n\ts_cselect_b32 %[ts], %[wrap], %[step]\n\ts_add_u32 %[tg], %[tg], 4\n\t" \
  "v_add_u32 %[x0], %[ts], %[x0]\n\tv_add_u32 %[x1], %[ts], %[x1]\n\tv_add_u32 %[x2], %[ts], %[x2]\n\tv_add_u32 %[x3], %[ts], %[x3]\n\tv_add_u32 %[wa], %[ts], %[wa]\n\t" \
  "s_waitcnt lgkmcnt(7)\n\tv_readfirstlane_b32 %[ts], %[tv]\n\ts_cmp_ge_u32 %[ts], 2\n\ts_cbranch_scc1 0f\n\t" \
  Q_MMA(132, M0, M1, M2) \
  "s_cmp_lt_u32 %[tg], 16\n\ts_cbranch_scc1 3f\n\ts_waitcnt vmcnt(0)\n3:\n\t" \
  "s_waitcnt lgkmcnt(0)\n\t" Q_ARRIVE "s_nop 0\n\t" Q_MMA(148, M0, M1, M2) "v_add_u32 %[ca], 4, %[ca]\n\t" \
  LDS_POLL("%[tv]", "%[cl]", "%[ts]", "%[tn]", "2", "") \
  Q_READ(100, "%[x0]", 0) Q_READ(116, "%[x1]", 1) "s_branch 2b\n" \
  "0:\n\t" \
  Q_GROUP(132, 100, "%[x0]", 0, M0, M1, M2) \
  "s_cmp_lt_u32 %[tg], 16\n\ts_cbranch_scc1 3f\n\ts_waitcnt vmcnt(0)\n3:\n\t" \
  "s_waitcnt lgkmcnt(7)\n\t" Q_ARRIVE \
  Q_GROUP(148, 116, "%[x1]", 1, M0, M1, M2) \
  "v_add_u32 %[ca], 4, %[ca]\n\ts_branch 2b\n" \
  "5:\n\t" \
  "s_waitcnt lgkmcnt(7)\n\t" Q_MMA(132, M0, M1, M2) "s_waitcnt lgkmcnt(0)\n\t" Q_ARRIVE "s_nop 0\n\t" Q_MMA(148, M0, M1, M2)
#define QKV_LOOP() asm volatile( \
  "s_mov_b32 %[tg], 4\n\t" LDS_POLL("%[tv]", "%[cl]", "%[ts]", "%[tn]", "2", "") \
  Q_READ(100, "%[x0]", 0) Q_READ(116, "%[x1]", 1) \
  "s_cmp_gt_u32 %[g], 10\n\ts_cbranch_scc1 7f\n\ts_cmp_eq_u32 %[g], 10\n\ts_cbranch_scc1 6f\n" \
  QKV_BODY(Q_T, Q_T, Q_T) "s_branch 8f\n6:\n" QKV_BODY(Q_T, Q_T, Q_N) "s_branch 8f\n7:\n" QKV_BODY(Q_N, Q_N, Q_N) \
  "8:\n\ts_nop 7\n\ts_nop 7" \
  : [a0] "+v"(acc[0]), [a1] "+v"(acc[1]), [a2] "+v"(acc[2]), [x0] "+v"(xa0), [x1] "+v"(xa1), [x2] "+v"(xa2), [x3] "+v"(xa3), [wa] "+v"(wa), \
    [tv] "=&v"(tv), [ts] "=&s"(ts), [tn] "=&s"(tn), [tg] "=&s"(tg), [ca] "+v"(car), [cl] "+v"(cla) \
  : [one] "v"(1u), [step] "s"(SLOT_B), [wrap] "s"(0u - 3u * SLOT_B), [g] "s"(g) \
  : "memory", "scc", V100_163)
#define V10(p) "v" #p "0", "v" #p "1", "v" #p "2", "v" #p "3", "v" #p "4", "v" #p "5", "v" #p "6", "v" #p "7", "v" #p "8", "v" #p "9"
#define V100_163 V10(10), V10(11), V10(12), V10(13), V10(14), V10(15), "v160", "v161", "v162", "v163"

#define AT_M(D, A, B, C) "v_mfma_f32_32x32x16_bf16 v[" #D ":" #D "+15], v[" A ":" A "+3], v[" B ":" B "+3], " C "\n\t"
#define AT_ACC(D) "v[" #D ":" #D "+15]"
#define AT_E(C, i) "v_exp_f32 v[" #C "+" #i "], v[" #C "+" #i "]\n\t"
#define AT_P(P, C, i) "v_cvt_pk_bf16_f32 v[" #P "], v[" #C "+" #i "], v[" #C "+" #i "+1]\n\t"
#define AT_STEP(C, N, KN, QN, VP, OP, VC, OC, X) \
  AT_M(N, #KN, #QN, "0") AT_E(C, 0) AT_E(C, 1) AT_E(C, 2) AT_E(C, 3) AT_E(C, 4) \
  AT_M(OP, #VP "+12", "100", AT_ACC(OP)) X AT_E(C, 5) AT_E(C, 6) AT_E(C, 7) AT_P(96, C, 0) AT_P(97, C, 2) AT_P(98, C, 4) AT_P(99, C, 6) \
  AT_M(N, #KN "+4", #QN "+4", AT_ACC(N)) AT_E(C, 8) AT_E(C, 9) AT_E(C, 10) AT_E(C, 11) AT_E(C, 12) \
  AT_M(OC, #VC "+8", "96", AT_ACC(OC)) AT_E(C, 13) AT_E(C, 14) AT_E(C, 15) AT_P(100, C, 8) AT_P(101, C, 10) AT_P(102, C, 12) AT_P(103, C, 14)
#define AT_LD(B) "global_load_dwordx4 v[" #B ":" #B "+3], %[vo], s[40:41] sc1\n\tglobal_load_dwordx4 v[" #B "+4:" #B "+7], %[vo], s[40:41] offset:1024 sc1\n\t" \
  "global_load_dwordx4 v[" #B "+8:" #B "+11], %[vo], s[40:41] offset:2048 sc1\n\tglobal_load_dwordx4 v[" #B "+12:" #B "+15], %[vo], s[40:41] offset:3072 sc1\n\t" \
  "s_add_u32 %[kl], %[kl], 4096\n\ts_addc_u32 %[kh], %[kh], 0\n\t"
#define AT_RED(A, O) "ds_write_b128 " A ", v[" #O ":" #O "+3]\n\tds_write_b128 " A ", v[" #O "+4:" #O "+7] offset:1024\n\t" \
  "ds_write_b128 " A ", v[" #O "+8:" #O "+11] offset:2048\n\tds_write_b128 " A ", v[" #O "+12:" #O "+15] offset:3072\n\t"
#define AT_TILE(T, TP, TN, LD, W, R1, R2, R3) \
  AT_STEP(64, 80, T, 112, TP, 48, T, 0, "") LD AT_STEP(80, 64, T, 120, T, 0, T, 16, "") AT_STEP(64, 80, T, 128, T, 16, T, 32, R1) W \
  AT_STEP(80, 64, TN, 104, T, 32, T, 48, R2) R3
#define AT_W(n) "s_waitcnt vmcnt(" #n ")\n\t"

NOPK __global__ void __launch_bounds__(512, 2)
fused_mha_s4d(const char* __restrict__ x, const char* __restrict__ wqkv, const char* __restrict__ wo, const char* __restrict__ u,
              const char* __restrict__ logAr, const char* __restrict__ Aim, const char* __restrict__ Cre, const char* __restrict__ Cim,
              const char* __restrict__ logdt, const char* __restrict__ Dsk, char* __restrict__ out, char* __restrict__ ws) {
  extern __shared__ __attribute__((aligned(16))) char smem[];
  const int tid = __builtin_amdgcn_workitem_id_x(), lane = tid & 63;
  const int wid = __builtin_amdgcn_readfirstlane(tid >> 6);
  const int b = __builtin_amdgcn_workgroup_id_x(), xcd = b & 7, idx = b >> 3;
  const int r32 = lane & 31, hh = lane >> 5;
  const unsigned L0 = (unsigned)(uintptr_t)smem;
  (void)L0;
  if (wid >= 4) {
    u64 pc; unsigned len;
    asm volatile("s_getpc_b64 %0\n.Lpf_begin:\n\ts_mov_b32 %1, .Lpf_end-.Lpf_begin" : "=s"(pc), "=s"(len));
    const unsigned off = ((tid - 256) * 8 + (idx & 7)) * 128;
    if (off < len) asm volatile("s_mov_b32 m0, %2\n\ts_nop 0\n\tglobal_load_lds_dword %0, %1" :: "v"(off), "s"(pc), "s"(L_SINK) : "memory");
  }
  if (wid == 2) lds_st<u32x4>(L_MAX + lane * 16, u32x4{0u, 0u, 0u, 0u});
  asm volatile("s_waitcnt lgkmcnt(0)\n\ts_barrier" ::: "memory");
  const __amdgpu_buffer_rsrc_t wsr = __builtin_amdgcn_make_buffer_rsrc(ws, 0, WS_END, 0x00020000);
  asm volatile("" :: "s"(x), "s"(wqkv), "s"(wo), "s"(u), "s"(logAr), "s"(Aim), "s"(Cre), "s"(Cim), "s"(logdt), "s"(Dsk), "s"(out), "s"(ws));
  STAMP(0);
#define MAKE_TAG(e) (((u64)(unsigned)__builtin_amdgcn_readfirstlane((unsigned)((e) >> 32)) << 32 | (unsigned)__builtin_amdgcn_readfirstlane((unsigned)(e))) + 0x9E3779B97F4A7C15ull)
  const int rt = (xcd & 3) * 4 + (idx >> 3), g = (xcd >> 2) * 8 + (idx & 7);
  const int woff = (g == 15) ? 8 : 0;
  const bool has_b = xcd < 7;

  if (__builtin_expect(wid >= 4, 0)) {
    const int w = wid - 4;
    __builtin_amdgcn_s_setprio(APRIO);
    const u64 epoch = __hip_atomic_load((GAS u64*)(ws + WS_EPOCH), __ATOMIC_RELAXED, __HIP_MEMORY_SCOPE_AGENT);
    const int rt32 = rt * 4 + w;
    unsigned l16 = lane * 16, so[3], kma[3], vcst = r32 == 28 ? 0x3F803F80u : 0u; unsigned scl[3]; u64 vmask[3], pmask[3], qmask[3];
#pragma unroll
    for (int j = 0; j < 3; ++j) {
      const unsigned sl = 3 * g + j;
      unsigned mq = (unsigned)((int)(sl - 16) >> 31), mp = (unsigned)((int)(sl - 32) >> 31);
      asm volatile("" : "+s"(mq), "+s"(mp));
      const unsigned t64 = (sl & 15) * 64 + rt32;
      so[j] = ((WS_QKV + t64 * 2048) & mq) | ((WS_KV + t64 * 4096 + (2048u & ~mp)) & ~mq);
      scl[j] = (__builtin_bit_cast(unsigned, QSCALE) & mq) | (0x3F800000u & ~mq);
      const unsigned mv = 0xF0000000u & ~mp;
      vmask[j] = (u64)mv << 32 | mv; pmask[j] = (u64)mp << 32; qmask[j] = (u64)mq << 32;
      kma[j] = C_KM + j * 4;
      asm volatile("" : "+s"(so[j]), "+s"(scl[j]), "+s"(vmask[j]), "+s"(pmask[j]), "+s"(qmask[j]), "+v"(kma[j]));
    }
    PIN(l16); PIN(vcst);
    f32x16 acc[3];
#pragma unroll
    for (int j = 0; j < 3; ++j) acc[j] = f32x16{};
    {
      const unsigned xa_l = w * 4096 + r32 * 128, xsw = (r32 >> 1) & 7;
      unsigned xa0 = xa_l + (((0 + hh) ^ xsw) << 4), xa1 = xa_l + (((2 + hh) ^ xsw) << 4), xa2 = xa0 ^ 64, xa3 = xa1 ^ 64;
      unsigned wa = XCH_B + (8 * hh + ((lane & 15) >> 2)) * 192 + woff + ((lane >> 4) & 1) * 32 + (lane & 3) * 8;
      unsigned tv, ts, tn, tg, car = C_R, cla = C_L;
      asm volatile("" : "+v"(acc[0]), "+v"(acc[1]), "+v"(acc[2]), "+v"(xa0), "+v"(xa1), "+v"(xa2), "+v"(xa3), "+v"(wa), "+v"(car), "+v"(cla));
      if (has_b) {
        lds_wait_ge(C_UI, 2);
        const int oct = (lane >> 1) & 7;
        s16x8 v[2];
#pragma unroll
        for (int i = 0; i < 2; ++i) {
          const int ch = 2 * (w * 8 + i * 4 + (lane >> 4)) + (lane & 1);
          const unsigned a = L_UIMG + oct * 1024 + (((ch >> 3) ^ oct) << 4) + (ch & 7) * 2;
#pragma unroll
          for (int e = 0; e < 8; ++e) v[i][e] = lds_ld<short>(a + e * 128);
        }
#pragma unroll
        for (int i = 0; i < 2; ++i)
          st128(__builtin_bit_cast(u32x4, v[i]), wsr, ((xcd * 32 + w * 8 + i * 4 + (lane >> 4)) * 32 + (31 - idx)) * 256 + (lane & 15) * 16, WS_UT);
      }
      STAMP(9);
      QKV_LOOP();
    }
    STAMP(2);
#pragma unroll
    for (int j = 0; j < 3; ++j) {
      u32x4 f0, f1;
#pragma unroll
      for (int i = 0; i < 4; ++i) f0[i] = sel(pk_bf16(acc[j][2 * i] * u2f(scl[j]), acc[j][2 * i + 1] * u2f(scl[j])), vcst, vmask[j]);
      st128(f0, wsr, l16, so[j]);
#pragma unroll
      for (int i = 0; i < 4; ++i) f1[i] = sel(pk_bf16(acc[j][8 + 2 * i] * u2f(scl[j]), acc[j][9 + 2 * i] * u2f(scl[j])), vcst, vmask[j]);
      const unsigned n2 = f2u(frag_norm2(f0, f1, hh));
      asm volatile("ds_max_u32 %0, %1" :: "v"(kma[j]), "v"(n2) : "memory");
      f1[2] = sel(f1[2], sel(0x3F803F80u, n2, qmask[j]), pmask[j]); f1[3] = sel(f1[3], 0x00003F80u, pmask[j]);
      st128(f1, wsr, l16 + 1024, so[j]);
    }
    const u64 tag = MAKE_TAG(epoch);
    STAMP(14);
    const int h = xcd * 2 + (idx >> 4), qt = idx & 15;
    f32x16 o[4];
#pragma unroll
    for (int i = 0; i < 4; ++i) { o[i] = f32x16{}; PIN(o[i]); }
    if (w == 3) lds_wait_ge(C_BTAG, 4);
    drain_vm();
    STAMP(15);
    if (w == 3) {
      lds_wait_ge(C_APUB, 3);
      const u32x4 km = lds_ld<u32x4>(C_KM);
      const u32x4 rec = {(unsigned)tag, ((km[0] + 0xffffu) >> 16) | ((km[1] + 0xffffu) & 0xffff0000u), (unsigned)(tag >> 32), km[2] + 0xffffu};
      if (lane == 0) st128(rec, wsr, (g * 16 + rt) * 128, WS_QFLAG);
    } else {
      lds_arrive_nw(C_APUB);
      if (w == 0) {
        rec_wait(wsr, WS_QFLAG + ((h / 3) * 16 + qt) * 128, tag);
        lds_wait_ge(C_R + 24, 4);
        const char* qs = ws + WS_QKV + (h * 64 + qt * 4) * 2048;
        glds_dev<0>(l16, qs, L_RING); glds_dev<1024>(l16, qs + 1024, L_RING); glds_dev<2048>(l16, qs + 2048, L_RING); glds_dev<3072>(l16, qs + 3072, L_RING);
        glds_dev<4096>(l16, qs + 4096, L_RING); glds_dev<5120>(l16, qs + 5120, L_RING); glds_dev<6144>(l16, qs + 6144, L_RING); glds_dev<7168>(l16, qs + 7168, L_RING);
        drain_vm();
        lds_arrive_nw(C_Q);
      } else if (w == 1) {
        const u32x4 rec = rec_wait(wsr, WS_QFLAG + ((hh ? (32 + h) / 3 : (16 + h) / 3) * 16 + (lane & 15)) * 128, tag);
        const int j = (16 + h) % 3;
        unsigned v = j == 0 ? rec[1] << 16 : ((j == 1 ? rec[1] : rec[3]) & 0xffff0000u);
        v = dpp_max<0x111>(v); v = dpp_max<0x112>(v); v = dpp_max<0x114>(v); v = dpp_max<0x118>(v);
        lds_st<unsigned>(C_GO, __builtin_amdgcn_readlane(v, 15) + 1u);
      }
    }
    STAMP(3);
    s16x8 qf[4][2];
    const unsigned kbase = WS_KV + (h * 64 + w * 16) * 4096;
#define LDF(soff, voff) __builtin_bit_cast(s16x8, __builtin_amdgcn_raw_buffer_load_b128(wsr, l16 + (voff), (soff), AUX_SC1))
    const unsigned km2 = lds_wait_ge_slow(C_GO, 1);
    STAMP(4);
    u32x16 kv0, kv1, kv2;
    {
      const char* kb = ws + kbase;
      asm volatile("global_load_dwordx4 v[136:139], %3, %4 sc1\n\tglobal_load_dwordx4 v[140:143], %3, %4 offset:1024 sc1\n\tglobal_load_dwordx4 v[144:147], %3, %4 offset:2048 sc1\n\t"
                   "global_load_dwordx4 v[148:151], %3, %4 offset:3072 sc1\n\tglobal_load_dwordx4 v[152:155], %3, %5 sc1\n\tglobal_load_dwordx4 v[156:159], %3, %5 offset:1024 sc1\n\t"
                   "global_load_dwordx4 v[160:163], %3, %5 offset:2048 sc1\n\tglobal_load_dwordx4 v[164:167], %3, %5 offset:3072 sc1\n\tglobal_load_dwordx4 v[168:171], %3, %6 sc1\n\t"
                   "global_load_dwordx4 v[172:175], %3, %6 offset:1024 sc1\n\tglobal_load_dwordx4 v[176:179], %3, %6 offset:2048 sc1\n\tglobal_load_dwordx4 v[180:183], %3, %6 offset:3072 sc1"
                   : "=&{v[136:151]}"(kv0), "=&{v[152:167]}"(kv1), "=&{v[168:183]}"(kv2) : "v"(l16), "s"(kb), "s"(kb + 4096), "s"(kb + 8192) : "memory");
    }
    lds_wait_ge(C_Q, 1);
    STAMP(1);
#pragma unroll
    for (int i = 0; i < 4; ++i) { const unsigned a = L_RING + ((w + 1 + i) & 3) * 2048 + l16; qf[i][0] = lds_ld<s16x8>(a); qf[i][1] = lds_ld<s16x8>(a + 1024); }
    lds_arrive(C_QR);
    {
      const float kmax2 = u2f(km2) * 1.02f;
      unsigned pad2[4], pad3[4]; bool unsafe = false;
#pragma unroll
      for (int i = 0; i < 4; ++i) {
        const float m = __builtin_amdgcn_sqrtf(u2f(__builtin_bit_cast(u32x4, qf[i][1])[2]) * kmax2);
        unsafe |= hh && !(m <= SAFE_BOUND);
        pad2[i] = pk_bf16(-m, 0.f); pad3[i] = 0u;
      }
      if (__builtin_expect(__builtin_amdgcn_ballot_w64(unsafe) != 0ull, 0)) {
        float mx[4];
#pragma unroll
        for (int i = 0; i < 4; ++i) { mx[i] = -INFINITY; if (hh) { u32x4 t_ = __builtin_bit_cast(u32x4, qf[i][1]); t_[2] = 0u; t_[3] = 0u; qf[i][1] = __builtin_bit_cast(s16x8, t_); } }
#pragma unroll 1
        for (int t = 0; t < 16; ++t) {
          const s16x8 k0 = LDF(kbase + t * 4096, 0), k1 = LDF(kbase + t * 4096, 1024);
#pragma unroll
          for (int i = 0; i < 4; ++i) {
            f32x16 s = MFMA32(k0, qf[i][0], f32x16{}); s = MFMA32(k1, qf[i][1], s);
#pragma unroll
            for (int e = 0; e < 16; ++e) mx[i] = __builtin_fmaxf(mx[i], s[e]);
          }
        }
#pragma unroll
        for (int i = 0; i < 4; ++i) {
          const float v = half_swap_max(mx[i]);
          const unsigned k_ = f2u(v) ^ ((unsigned)((int)f2u(v) >> 31) | 0x80000000u);
          __hip_atomic_fetch_max((LAS unsigned*)(uintptr_t)(L_MAX + (((w + 1 + i) & 3) * 32 + r32) * 4), k_, __ATOMIC_RELAXED, __HIP_MEMORY_SCOPE_WORKGROUP);
        }
        role_sync(C_FB, 1);
#pragma unroll
        for (int i = 0; i < 4; ++i) {
          const unsigned k_ = lds_ld<unsigned>(L_MAX + (((w + 1 + i) & 3) * 32 + r32) * 4);
          const float m = u2f(k_ ^ ((unsigned)((int)~k_ >> 31) | 0x80000000u));
          const float m0 = bf_round(-m), r1 = -m - m0, m1 = bf_round(r1);
          pad2[i] = pk_bf16(m0, m1); pad3[i] = pk_bf16(r1 - m1, 0.f);
        }
      }
#pragma unroll
      for (int i = 0; i < 4; ++i) {
        u32x4 t_ = __builtin_bit_cast(u32x4, qf[i][1]);
        t_[2] = hh ? pad2[i] : t_[2]; t_[3] = hh ? pad3[i] : t_[3]; qf[i][1] = __builtin_bit_cast(s16x8, t_);
      }
    }
    unsigned redw = L_B + l16; PIN(redw);
    __builtin_amdgcn_s_setprio(1);
    STAMP(5);
    {
      const u64 kn = (u64)(ws + kbase + 3 * 4096);
      unsigned kl = (unsigned)kn, kh = (unsigned)(kn >> 32), tv, ts, tn, it;
      asm volatile(
        "s_mov_b32 %[it], 3\n\tv_mov_b32 v100, 0\n\tv_mov_b32 v101, 0\n\tv_mov_b32 v102, 0\n\tv_mov_b32 v103, 0\n\t"
        "v_mov_b32 v196, 0\n\tv_mov_b32 v197, 0\n\tv_mov_b32 v198, 0\n\tv_mov_b32 v199, 0\n\t"
        AT_W(10) AT_M(64, "136", "104", "0") AT_M(64, "140", "108", AT_ACC(64)) AT_W(8) "s_nop 7\n\ts_nop 3\n\t.p2align " AT_ALIGN "\n"
        "2:\n\t"
        AT_TILE(136, 184, 152, AT_LD(184), AT_W(8), "", "", "") AT_TILE(152, 136, 168, AT_LD(136), AT_W(8), "", "", "")
        AT_TILE(168, 152, 184, AT_LD(152), AT_W(8), "", "", "") AT_TILE(184, 168, 136, AT_LD(168), AT_W(8), "", "", "s_sub_u32 %[it], %[it], 1\n\t")
        "s_cmp_lg_u32 %[it], 0\n\ts_cbranch_scc1 2b\n\t"
        AT_TILE(136, 184, 152, AT_LD(184), AT_W(8), "", "", "") AT_TILE(152, 136, 168, "", AT_W(4), "", "", "")
        AT_TILE(168, 152, 184, "", AT_W(0), "", "", "")
        LDS_POLL("%[tv]", "%[bf]", "%[ts]", "%[tn]", "4", "")
        AT_TILE(184, 168, 136, "", "", AT_RED("%[r0]", 0), AT_RED("%[r1]", 16), "")
        "s_nop 1\n\t" AT_M(48, "184+12", "100", AT_ACC(48)) AT_RED("%[r2]", 32) "s_nop 7"
        : "+{v[0:15]}"(o[0]), "+{v[16:31]}"(o[1]), "+{v[32:47]}"(o[2]), "+{v[48:63]}"(o[3]), "+{v[136:151]}"(kv0), "+{v[152:167]}"(kv1), "+{v[168:183]}"(kv2),
          [kl] "+{s40}"(kl), [kh] "+{s41}"(kh), [tv] "=&v"(tv), [ts] "=&s"(ts), [tn] "=&s"(tn), [it] "=&s"(it)
        : "{v[104:107]}"(qf[0][0]), "{v[108:111]}"(qf[0][1]), "{v[112:115]}"(qf[1][0]), "{v[116:119]}"(qf[1][1]), "{v[120:123]}"(qf[2][0]), "{v[124:127]}"(qf[2][1]),
          "{v[128:131]}"(qf[3][0]), "{v[132:135]}"(qf[3][1]), [vo] "v"(l16), [bf] "v"(C_BFREE),
          [r0] "v"(redw + (((w + 1) & 3) * 3 + 2) * 4096), [r1] "v"(redw + (((w + 2) & 3) * 3 + 1) * 4096), [r2] "v"(redw + (((w + 3) & 3) * 3) * 4096)
        : "memory", "scc", "v64", "v65", "v66", "v67", "v68", "v69", V10(7), V10(8), V10(9), "v100", "v101", "v102", "v103",
          "v184", "v185", "v186", "v187", "v188", "v189", V10(19));
    }
    STAMP(6);
    role_sync(C_A, 1);
    {
      f32x4 pv[12];
#pragma unroll
      for (int k = 0; k < 12; ++k) pv[k] = lds_ld<f32x4>(redw + w * 12288 + k * 1024);
      SBAR();
      f32x16 a = o[3];
#pragma unroll
      for (int k = 0; k < 12; ++k) { const int q = k & 3; a[4 * q] += pv[k][0]; a[4 * q + 1] += pv[k][1]; a[4 * q + 2] += pv[k][2]; a[4 * q + 3] += pv[k][3]; }
      auto lr = __builtin_amdgcn_permlane32_swap(f2u(a[12]), f2u(a[12]), false, false);
      const float inv = __builtin_amdgcn_rcpf(u2f(lr[1]));
      u32x4 f0, f1;
#pragma unroll
      for (int i = 0; i < 4; ++i) { f0[i] = pk_bf16(a[2 * i] * inv, a[2 * i + 1] * inv); f1[i] = pk_bf16(a[8 + 2 * i] * inv, a[9 + 2 * i] * inv); }
      if (hh) { f1[2] = 0u; f1[3] = 0u; }
      const unsigned so = WS_O + ((qt * 4 + w) * 16 + h) * 2048;
      st128(f0, wsr, l16, so);
      st128(f1, wsr, l16 + 1024, so);
      lds_arrive(C_A);
      drain_vm();
      if (lane == 0) flag_store(ws, WS_OFLAG + ((qt * 4 + w) * 16 + h) * 128, tag);
    }
    STAMP(7);
    const int rs = xcd * 8 + (idx >> 2), cg = idx & 3, n0 = w < 3 ? 32 * w : 80;
    lds_wait_ge(C_A, 2 * 4);
    lds_wait_ge(C_WO, 4);
    flag_wait(ws, WS_OFLAG + (rs * 16 + w * 4 + (lane & 3)) * 128, tag);
    STAMP(8);
    {
      const char* os = ws + WS_O + (rs * 16 + w * 4) * 2048;
      const unsigned od = L_B + w * 8192;
      glds_dev<0>(l16, os, od); glds_dev<1024>(l16, os + 1024, od); glds_dev<2048>(l16, os + 2048, od); glds_dev<3072>(l16, os + 3072, od);
      glds_dev<4096>(l16, os + 4096, od); glds_dev<5120>(l16, os + 5120, od); glds_dev<6144>(l16, os + 6144, od); glds_dev<7168>(l16, os + 7168, od);
    }
    s16x8 wf[32];
    unsigned oa[4];
#pragma unroll
    for (int q = 0; q < 4; ++q) {
      const int gq = (w + q) & 3;
      unsigned wa = L_RING + (gq * 112 + 4 * hh + ((lane & 15) >> 2)) * 224 + n0 * 2 + ((lane >> 4) & 1) * 32 + (lane & 3) * 8;
      unsigned wb2 = wa + (hh ? 16 * 224 : 24 * 224);
      oa[q] = redw + gq * 8192;
      PIN(wa); PIN(wb2); PIN(oa[q]);
#pragma unroll
      for (int hd = 0; hd < 4; ++hd) {
        wf[q * 8 + hd * 2] = cat8(tr_read(wa + hd * 28 * 224), tr_read(wa + hd * 28 * 224 + 8 * 224));
        wf[q * 8 + hd * 2 + 1] = cat8(tr_read(wa + hd * 28 * 224 + 16 * 224), tr_read(wb2 + hd * 28 * 224));
      }
    }
    drain_vm();
    lds_arrive_nw(C_A);
    STAMP(13);
    f32x16 d = f32x16{};
    {
      s16x8 ob[8];
#pragma unroll
      for (int k = 0; k < 8; ++k) ob[k] = lds_ld<s16x8>(oa[0] + k * 1024);
      SBAR();
#pragma unroll
      for (int k = 0; k < 4; ++k) { d = MFMA32(wf[k], ob[k], d); SBAR(); }
      lds_wait_ge(C_A, 3 * 4);
#pragma unroll
      for (int k = 0; k < 4; ++k) ob[k] = lds_ld<s16x8>(oa[1] + k * 1024);
      SBAR();
#pragma unroll
      for (int k = 4; k < 32; ++k) {
        d = MFMA32(wf[k], ob[k & 7], d);
        if (k + 8 < 32) ob[k & 7] = lds_ld<s16x8>(oa[(k + 8) >> 3] + (k & 7) * 1024);
        SBAR();
      }
    }
    {
      char* op = out + (size_t)(rs * 32 * 1792 + (cg * 112 + n0) * 2) + (unsigned)(r32 * 1792 + hh * 16);
#pragma unroll
      for (int k = 0; k < 2; ++k) {
        const unsigned a0 = pk_bf16(d[8 * k], d[8 * k + 1]), a1 = pk_bf16(d[8 * k + 2], d[8 * k + 3]);
        const unsigned b0 = pk_bf16(d[8 * k + 4], d[8 * k + 5]), b1 = pk_bf16(d[8 * k + 6], d[8 * k + 7]);
        auto s0 = __builtin_amdgcn_permlane32_swap(a0, b0, false, false);
        auto s1 = __builtin_amdgcn_permlane32_swap(a1, b1, false, false);
        if (k || w < 3) *(GAS u32x4*)(op + k * 32) = u32x4{s0[0], s1[0], s0[1], s1[1]};
      }
    }
    STAMP(10);
    STAMP_FLUSH();
    asm volatile(".Lpf_end:");
  } else {
    const int wb = wid ^ 2;
    const int c = wb & 1, half = wb >> 1;
    const int d = (has_b ? xcd * 64 : 0) + idx * 2 + c;
    const unsigned po = d * 512 + lane * 4;
    unsigned pdt, p0[4], p1[4];
    u64 tag;
#define LOAD_P(P, O) do { P[0] = *(const GAS unsigned*)(logAr + (O) + po); P[1] = *(const GAS unsigned*)(Aim + (O) + po); \
      P[2] = *(const GAS unsigned*)(Cre + (O) + po); P[3] = *(const GAS unsigned*)(Cim + (O) + po); } while (0)
    const unsigned T = L_T + c * T_B;
    unsigned ga[2][11], wv[7], za;
    auto gather_addr = [&]() __attribute__((always_inline)) NOPK {
#pragma unroll
      for (int i = 0; i < 7; ++i) {
        const unsigned p = i * 64 + lane, row = (p * 37450u) >> 19; wv[i] = row * 896 + (p - row * 14) * 16; PIN(wv[i]);
      }
      za = L_B + c * CH_B + (lane < 28 ? 4 * KC_S + half * UC_S + 4096 + lane * 16 : (half * 2 + (lane > 30)) * KC_S + (lane - (lane > 30 ? 31 : 28)) * 16); PIN(za);
#pragma unroll
      for (int it = 0; it < 2; ++it) {
        const int l0 = 8 * (3 + it * 128 + half * 64 + lane) - 27;
        const float f0 = (float)l0;
#pragma unroll
        for (int k = 0; k < 11; k += 2) {
          const unsigned pk = pk_bf16(f0 + (float)k, f0 + (float)(k + 1));
          asm volatile("v_mad_u32_u16 %0, %1, 2, %2" : "=v"(ga[it][k]) : "v"(pk), "s"(T - 2u * 17024u));
          if (k < 10) asm volatile("v_mad_u32_u16 %0, %1, 2, %2 op_sel:[1,0,0,0]" : "=v"(ga[it][k + 1]) : "v"(pk), "s"(T - 2u * 17024u));
        }
        if (it == 0 && !half) {
#pragma unroll
          for (int k = 0; k < 11; ++k) { const int l = l0 + k; ga[0][k] = l < 128 ? T + 2 * ((k < 3 && l < 0) ? 641 : l) : ga[0][k]; PIN(ga[0][k]); }
        }
      }
    };
    if (half) {
      unsigned xo[4], wo_[6];
#pragma unroll
      for (int q = 0; q < 4; ++q) xo[q] = (q * 8 + (lane >> 3)) * 896 + (((lane & 7) ^ ((q & 1) * 4 + (lane >> 4))) << 4);
      const char* xb = x + (size_t)(rt * 128 + c * 64) * 896;
      const char* wq = wqkv + g * 168 - woff;
      int kc = ROT ? ((idx & 7) + 2 * (idx >> 3)) % 7 : 0;
      u64 epoch;
      asm volatile("global_load_dwordx2 %0, %1, %2 sc1" : "=v"(epoch) : "v"(0u), "s"(ws + WS_EPOCH) : "memory");
#define X_PIECES(B, D) do { glds<0>(xo[0], B, D); glds<1024>(xo[1], B, D); glds<2048>(xo[2], B, D); glds<3072>(xo[3], B, D); \
        glds<4096>(xo[0], (B) + 32 * 896, D); glds<5120>(xo[1], (B) + 32 * 896, D); glds<6144>(xo[2], (B) + 32 * 896, D); glds<7168>(xo[3], (B) + 32 * 896, D); } while (0)
#define W_PIECES(B, D) do { glds<0>(wo_[0], B, D); glds<1024>(wo_[1], B, D); glds<2048>(wo_[2], B, D); glds<3072>(wo_[3], B, D); glds<4096>(wo_[4], B, D); glds<5120>(wo_[5], B, D); } while (0)
      {
        const char* up = u + (size_t)(idx * 64 + 56 - 32 * c) * 896 + (has_b ? xcd * 128 : 0);
        const unsigned uo = (7 - (lane >> 3)) * 896, k4 = (lane & 7) ^ (4 * c);
        glds<0>(uo + (k4 << 4), up, L_UIMG + c * 4096); glds<1024>(uo + ((k4 ^ 1) << 4), up - 8 * 896, L_UIMG + c * 4096);
        glds<2048>(uo + ((k4 ^ 2) << 4), up - 16 * 896, L_UIMG + c * 4096); glds<3072>(uo + ((k4 ^ 3) << 4), up - 24 * 896, L_UIMG + c * 4096);
      }
      X_PIECES(xb + kc * 128, L_RING + c * 8192);
#pragma unroll
      for (int i = 0; i < 6; ++i) {
        const unsigned p = (6 * c + i) * 64 + lane, row = (p * 43691u) >> 19; unsigned e = p - row * 12; e = e > 10 ? 10 : e;
        wo_[i] = row * 2688 + e * 16;
      }
      W_PIECES(wq + kc * 64 * 2688, L_RING + XCH_B + c * 6144);
      asm volatile("s_waitcnt vmcnt(14)" : "+v"(epoch) :: "memory");
      lds_arrive_nw(C_UI);
      tag = MAKE_TAG(epoch); asm volatile("" :: "s"(tag)); lds_arrive_nw(C_BTAG);
#pragma unroll 1
      for (int n = 1; n < 7; ++n) {
        if (n >= 4) lds_wait_ge(C_R + 4 * (n - 4), 4);
        const unsigned slot = (n & 3) * SLOT_B;
        kc = kc == 6 ? 0 : kc + 1;
        X_PIECES(xb + kc * 128, L_RING + c * 8192 + slot);
        W_PIECES(wq + kc * 64 * 2688, L_RING + XCH_B + c * 6144 + slot);
        asm volatile("s_waitcnt vmcnt(14)" ::: "memory"); lds_arrive_nw(C_L + 4 * (n - 1));
        if (n == 1) STAMP(1);
      }
      pdt = *(const GAS unsigned short*)(logdt + d * 2);
      LOAD_P(p0, 256);
      asm volatile("s_waitcnt vmcnt(5)" ::: "memory");
      lds_arrive_nw(C_L + 24);
      if (c == 0 && has_b && lane == 0) flag_store(ws, WS_UFLAG + (xcd * 32 + idx) * 128, tag);
      STAMP(12);
      gather_addr();
#pragma unroll
      for (int i = 0; i < 4; ++i) { p0[i] <<= 16; p1[i] = 0u; }
      lds_wait_ge(C_R + 12, 4);
    } else {
      pdt = *(const GAS unsigned short*)(logdt + d * 2);
      LOAD_P(p0, 0); LOAD_P(p1, 256);
      const u64 epoch = __hip_atomic_load((GAS u64*)(ws + WS_EPOCH), __ATOMIC_RELAXED, __HIP_MEMORY_SCOPE_AGENT);
      gather_addr();
      asm volatile("" : "+v"(pdt), "+v"(p0[0]), "+v"(p1[0]), "+v"(p1[1]), "+v"(p1[2]), "+v"(p1[3]));
      tag = MAKE_TAG(epoch); asm volatile("" :: "s"(tag)); lds_arrive_nw(C_BTAG);
#pragma unroll
      for (int i = 0; i < 4; ++i) p1[i] &= 0xffff0000u;
    }
    __builtin_amdgcn_s_setprio(1);
    STAMP(9);
    if (has_b) {
      const float dt = bf_round(fexp(bf_lo(pdt)));
      const unsigned cm = umax(umax(umax(p0[2] << 16 & 0x7fff0000u, p0[2] & 0x7fff0000u), umax(p0[3] << 16 & 0x7fff0000u, p0[3] & 0x7fff0000u)),
                               umax(p1[2] & 0x7fff0000u, p1[3] & 0x7fff0000u));
      unsigned eb = wave_umax(cm) >> 23; eb = eb < 1u ? 1u : (eb > 253u ? 253u : eb);
      const float csc = u2f((254u - eb) << 23), cinv = u2f(eb << 23);
      const unsigned tb_ = (half ? 3 * SLOT_B : 4 * SLOT_B) + c * TAB_W;
      const unsigned lw = tb_ + (lane >> 3) * 1056 + ((lane >> 2) & 1) * 528 + (lane & 3) * 4;
      const unsigned hw = tb_ + TAB_HI + (lane >> 3) * 672 + ((lane >> 2) & 1) * 336 + (lane & 3) * 4;
      const unsigned lr = tb_ + hh * 528 + r32 * 16, hr = tb_ + TAB_HI + hh * 336 + r32 * 16;
      f32x16 kd = f32x16{};
      u64 uflag = 0;
#pragma unroll 1
      for (int r = half ? 2 : 0; r < 3; ++r) {
        const float rho = -bf_round(fexp(bf_hi(p0[0]))) * dt * LOG2E, th = bf_hi(p0[1]) * dt * 0.15915494309189535f;
        f32x2 hA, hB; hA.x = bf_hi(p0[2]) * csc; hA.y = bf_hi(p0[3]) * -csc;
#pragma unroll
        for (int i = 0; i < 4; ++i) p0[i] = r == 0 ? p0[i] << 16 : p1[i];
        const f32x2 z8 = zpow(rho, th, 8.f), z32 = zpow(rho, th, 32.f), z1 = zpow(rho, -th, 1.f), z256 = zpow(rho, -th, 256.f);
        const f32x2 z16 = cmul(z8, z8), z24 = cmul(z8, z16);
        const unsigned q1 = pk_f16(z8.x, z8.y), q2 = pk_f16(z16.x, z16.y), q3 = pk_f16(z24.x, z24.y);
        lds_st<unsigned>(lw, 0x00003C00u); lds_st<unsigned>(lw + 16, q1); lds_st<unsigned>(lw + 32, q2); lds_st<unsigned>(lw + 48, q3);
        f32x2 e = z32;
#pragma unroll
        for (int a = 1; a < 8; ++a) {
          const unsigned p_ = pk_f16(e.x, e.y); unsigned m1, m2, m3;
          if (a < 7) e = cmul(e, z32);
          hmul3(p_, q1, q2, q3, m1, m2, m3);
          lds_st<unsigned>(lw + 64 * a, p_); lds_st<unsigned>(lw + 64 * a + 16, m1); lds_st<unsigned>(lw + 64 * a + 32, m2); lds_st<unsigned>(lw + 64 * a + 48, m3);
        }
        uflag = __hip_atomic_load((GAS u64*)(ws + WS_UFLAG + (xcd * 32 + r32) * 128), __ATOMIC_RELAXED, __HIP_MEMORY_SCOPE_AGENT);
#define PUTH(i, v) lds_st<unsigned>(hw + (i) * 16, pk_f16((v).x, (v).y))
        PUTH(0, hA); f32x2 g0 = cmul(hA, z256); PUTH(8, g0);
        hB = cmul(hA, z1); PUTH(1, hB); hA = cmul(hB, z1); PUTH(2, hA); { f32x2 t_ = cmul(hA, z256); PUTH(9, t_); }
        hB = cmul(hA, z1); PUTH(3, hB); hA = cmul(hB, z1); PUTH(4, hA); f32x2 g4 = cmul(hA, z256); PUTH(10, g4);
        hB = cmul(hA, z1); PUTH(5, hB); hA = cmul(hB, z1); PUTH(6, hA); { f32x2 t_ = cmul(hA, z256); PUTH(11, t_); }
        hB = cmul(hA, z1); PUTH(7, hB);
        g0 = cmul(g0, z256); PUTH(12, g0); g4 = cmul(g4, z256); PUTH(13, g4);
        g0 = cmul(g0, z256); PUTH(14, g0); g4 = cmul(g4, z256); PUTH(15, g4);
#pragma unroll
        for (int i = 16; i < 21; ++i) { g0 = cmul(g0, z256); PUTH(i, g0); }
        h16x8 fa[8], fb[8];
#pragma unroll
        for (int s = 0; s < 8; ++s) { fa[s] = lds_ld<h16x8>(hr + s * 672); fb[s] = lds_ld<h16x8>(lr + s * 1056); }
        SBAR();
#pragma unroll
        for (int s = 0; s < 8; ++s) kd = MFMA32H(fa[s], fb[s], kd);
      }
      STAMP(10);
      if (__builtin_amdgcn_ballot_w64(uflag != tag) != 0ull) flag_wait(ws, WS_UFLAG + (xcd * 32 + r32) * 128, tag);
      asm volatile("" ::: "memory");
      u32x4 uv[2];
#pragma unroll
      for (int i = 0; i < 2; ++i) uv[i] = __builtin_amdgcn_raw_buffer_load_b128(wsr, wb * 2048 + i * 1024 + lane * 16, WS_UT + (xcd * 32 + idx) * 8192, AUX_SC1);
      STAMP(11);
      {
        const float ks = cinv * 2.f * dt;
        float kv[12];
#pragma unroll
        for (int i = 0; i < 12; ++i) kv[i] = kd[i] * ks;
        const unsigned kx = (c ? L_KX : L_T - 3072) + lane * 16;
        unsigned old = 0;
        if (lane == 0) old = __hip_atomic_fetch_add((LAS unsigned*)(uintptr_t)(C_KX + c * 8), 1u, __ATOMIC_RELAXED, __HIP_MEMORY_SCOPE_WORKGROUP);
        if (__builtin_amdgcn_readfirstlane(old) == 0) {
#pragma unroll
          for (int q = 0; q < 3; ++q) lds_st<f32x4>(kx + q * 1024, f32x4{kv[4 * q], kv[4 * q + 1], kv[4 * q + 2], kv[4 * q + 3]});
          lds_arrive(C_KX + c * 8 + 4);
        } else {
          lds_wait_ge(C_KX + c * 8 + 4, 1);
          const f32x4 v0 = lds_ld<f32x4>(kx), v1 = lds_ld<f32x4>(kx + 1024), v2 = lds_ld<f32x4>(kx + 2048);
          const unsigned i1 = T + (hh ? 384 + 2 * r32 : 256 + 4 * r32) * 2;
          const unsigned i2 = T + (hh ? (r32 == 0 ? 640 : 643) : 512 + r32) * 2, i3 = T + (hh ? 641 : 512 + r32) * 2, st3 = hh ? 0 : 64;
          lds_st<u32x2>(T + (8 * r32 + 4 * hh) * 2, u32x2{pk_bf16(kv[0] + v0[0], kv[1] + v0[1]), pk_bf16(kv[2] + v0[2], kv[3] + v0[3])});
          lds_st<unsigned>(i1, pk_bf16(kv[4] + v1[0], kv[5] + v1[1])); lds_st<unsigned>(i1 + (hh ? 128 : 4), pk_bf16(kv[6] + v1[2], kv[7] + v1[3]));
          const unsigned w0 = pk_bf16(kv[8] + v2[0], kv[9] + v2[1]), w1 = hh ? 0u : pk_bf16(kv[10] + v2[2], kv[11] + v2[3]);
          lds_st<short>(i2, (short)w0); lds_st<short>(i3 + st3, hh ? (short)0 : (short)(w0 >> 16)); lds_st<short>(i3 + 2 * st3, (short)w1); lds_st<short>(i3 + 3 * st3, (short)(w1 >> 16));
        }
        role_sync(C_B, 1);
      }
      STAMP(2);
      const unsigned cb = L_B + c * CH_B;
      {
        s16x2 P[2][6];
#pragma unroll
        for (int it = 0; it < 2; ++it)
#pragma unroll
          for (int k = 0; k < 11; ++k) P[it][k >> 1][k & 1] = lds_ld<short>(ga[it][k]);
        if (lane < 34) lds_st<u32x4>(za, u32x4{0u, 0u, 0u, 0u});
        if (half && lane == 0) lds_st<u32x2>(cb + 4 * KC_S + UC_S + 4088, u32x2{0u, 0u});
#pragma unroll
        for (int it = 0; it < 2; ++it) {
          unsigned Q[6], A[5];
#pragma unroll
          for (int k = 0; k < 6; ++k) Q[k] = __builtin_bit_cast(unsigned, P[it][k]);
#pragma unroll
          for (int k = 0; k < 5; ++k) A[k] = __builtin_amdgcn_alignbit(Q[k + 1], Q[k], 16);
          const unsigned a = cb + (3 + it * 128 + half * 64 + lane) * 16;
          lds_st<u32x4>(a, u32x4{Q[0], Q[1], Q[2], Q[3]}); lds_st<u32x4>(a + KC_S, u32x4{A[0], A[1], A[2], A[3]});
          lds_st<u32x4>(a + 2 * KC_S, u32x4{Q[1], Q[2], Q[3], Q[4]}); lds_st<u32x4>(a + 3 * KC_S, u32x4{A[1], A[2], A[3], A[4]});
        }
      }
      STAMP(3);
#pragma unroll
      for (int i = 0; i < 2; ++i) {
        const int p = wb * 128 + i * 64 + lane;
        const unsigned a = L_B + (p & 1) * CH_B + 4 * KC_S + (p >> 1) * 16;
        lds_st<u32x4>(a, uv[i]); lds_st<u32x2>(a + UC_S - 8, u32x2{uv[i][0], uv[i][1]}); lds_st<u32x2>(a + UC_S, u32x2{uv[i][2], uv[i][3]});
      }
      role_sync(C_B, 2);
      __builtin_amdgcn_s_setprio(BCONV);
      STAMP(5);
      f32x4 y[4];
#pragma unroll
      for (int i = 0; i < 4; ++i) y[i] = f32x4{0.f, 0.f, 0.f, 0.f};
      const int cc = lane & 15, kq = lane >> 4;
      {
        unsigned ka = cb + (cc & 3) * KC_S + ((cc >> 2) + kq) * 16;
        unsigned ua = cb + 4 * KC_S + (1 - (cc & 1)) * UC_S + (2016 - 32 * (cc >> 1) + 8 * kq) * 2 - 7 * 512;
        PIN(ka); PIN(ua);
        auto conv = [&](auto hc) __attribute__((always_inline)) NOPK {
          constexpr int H = decltype(hc)::value, NF = H ? 6 : 8;
          s16x8 af[2][NF], bf[2][NF];
#pragma unroll
          for (int m = 0; m < NF; ++m) { bf[0][m] = lds_ld<s16x8>(ua + (7 - m) * 512); af[0][m] = lds_ld<s16x8>(ka + m * 512); }
          auto conv_class = [&](auto sc, auto oc) __attribute__((always_inline)) NOPK {
            constexpr int S = decltype(sc)::value, O = decltype(oc)::value;
            int k = 0;
#pragma unroll
            for (int m = 0; m < NF; ++m)
#pragma unroll
              for (int i = 0; i < 4; ++i) {
                const int n = (H ? 2 + i : (i < 2 ? i : 4 + i)) - m;
                if (n >= 0) {
                  y[i] = MFMA16(af[S][m], bf[S][n], y[i]);
                  if (k < 2 * NF) {
                    constexpr int o0[16] = {0, 8, 9, 14, 15, 1, 13, 2, 12, 3, 11, 4, 10, 5, 6, 7}, o1[16] = {0, 10, 11, 12, 13, 1, 9, 2, 8, 3, 4, 5, 0, 0, 0, 0};
                    const int q = H ? o1[k] : o0[k];
                    if (q < 8) af[1 - S][q] = lds_ld<s16x8>(ka + O + q * 512); else bf[1 - S][q - 8] = lds_ld<s16x8>(ua + O + (15 - q) * 512);
                  }
                  SBAR();
                  ++k;
                }
              }
          };
#pragma unroll 1
          for (int it = 0; it < 4; ++it) {
            conv_class(IC<0>{}, IC<64>{});
            conv_class(IC<1>{}, IC<128>{});
            ka += 128; ua += 128;
          }
        };
        if (half) conv(IC<1>{}); else conv(IC<0>{});
      }
      PIN(y[3]); STAMP(13);
      __builtin_amdgcn_s_setprio(2);
#pragma unroll
      for (int i = 0; i < 4; ++i) {
        const int t64 = 4 * (half ? 2 + i : (i < 2 ? i : 4 + i)) + (cc >> 2);
        __builtin_amdgcn_raw_buffer_store_b64(u32x2{pk_bf16(y[i][0], y[i][1]), pk_bf16(y[i][2], y[i][3])}, wsr,
                                              ((xcd * 32 + t64) * 64 + idx * 2 + c) * 128 + (32 * ((cc >> 1) & 1) + 8 * kq + 4 * (cc & 1)) * 2, WS_YT, AUX_SC1);
      }
      lds_arrive(C_BFREE);
      __builtin_amdgcn_s_setprio(0);
      STAMP(15);
    } else {
      lds_arrive(C_BFREE);
      __builtin_amdgcn_s_setprio(0);
    }
    lds_wait_ge_slow(C_QR, 4);
#pragma unroll 1
    for (int i = wb; i < 14; i += 4) {
      const char* sb = wo + i * 32 * 896 + (idx & 3) * 224;
      const unsigned dst = L_RING + i * 7168;
#define WO_PIECE(k) glds<(k) * 1024>(wv[k], sb, dst); asm volatile("s_waitcnt vmcnt(2)" ::: "memory")
      WO_PIECE(0); WO_PIECE(1); WO_PIECE(2);
      if (i < 4 && has_b) {
        if (last_of_four(C_BPUB, lane, 0)) { if (lane == 0) flag_store(ws, WS_YFLAG + (xcd * 32 + idx) * 128, tag); }
        STAMP(6);
      }
      WO_PIECE(3); WO_PIECE(4); WO_PIECE(5); WO_PIECE(6);
    }
    drain_vm();
    lds_arrive(C_WO);
    STAMP(7);
    if (has_b) {
      const int tl = wb * 16 + (lane & 15);
      u32x4 uu[2], ds[2];
#pragma unroll
      for (int i = 0; i < 2; ++i) {
        const int oct = (lane >> 4) + 4 * i;
        uu[i] = *(const GAS u32x4*)(u + (size_t)(idx * 64 * 896 + xcd * 128) + (unsigned)(tl * 896 + oct * 16));
        ds[i] = *(const GAS u32x4*)(Dsk + xcd * 128 + (unsigned)(oct * 16));
      }
      if (wb == 0) flag_wait(ws, WS_YFLAG + (xcd * 32 + r32) * 128, tag);
      role_sync_idle(C_B, 3);
      u32x4 v[2];
#pragma unroll
      for (int i = 0; i < 2; ++i) v[i] = __builtin_amdgcn_raw_buffer_load_b128(wsr, wb * 2048 + i * 1024 + lane * 16, WS_YT + (xcd * 32 + idx) * 8192, AUX_SC1);
#pragma unroll
      for (int i = 0; i < 2; ++i) lds_st<u32x4>(L_YIMG + wb * 2048 + i * 1024 + lane * 16, v[i]);
      role_sync_idle(C_B, 4);
#pragma unroll
      for (int i = 0; i < 2; ++i) {
        const int oct = (lane >> 4) + 4 * i;
        const unsigned a = L_YIMG + (8 * oct + ((lane & 15) >> 2)) * 128 + (wb * 16 + (lane & 3) * 4) * 2;
        const u32x4 yy = __builtin_bit_cast(u32x4, cat8(tr_read(a), tr_read(a + 512)));
        u32x4 r;
#pragma unroll
        for (int k = 0; k < 4; ++k) {
          const unsigned sk = pk_bf16(bf_lo(uu[i][k]) * bf_lo(ds[i][k]), bf_hi(uu[i][k]) * bf_hi(ds[i][k]));
          r[k] = pk_bf16(bf_lo(yy[k]) + bf_lo(sk), bf_hi(yy[k]) + bf_hi(sk));
        }
        *(GAS u32x4*)(out + (size_t)(idx * 64 * 1792 + 896 + xcd * 128) + (unsigned)(tl * 1792 + oct * 16)) = r;
      }
      STAMP(8);
    } else if (b == 7 && wb == 0) {
#pragma unroll 1
      for (int i = 0; i < 4; ++i) rec_wait(wsr, WS_QFLAG + (i * 64 + lane) * 128, tag);
      if (lane == 0) flag_store(ws, WS_EPOCH, tag);
    }
    STAMP_FLUSH();
  }
}

extern "C" void kernel_launch(void* const* d_in, const int* in_sizes, int n_in, void* d_out, int out_size, void* d_ws, size_t ws_size,
                              hipStream_t stream) {
  (void)in_sizes; (void)n_in; (void)out_size; (void)ws_size;
  (void)hipFuncSetAttribute((const void*)fused_mha_s4d, hipFuncAttributeMaxDynamicSharedMemorySize, LDS_BYTES);
  fused_mha_s4d<<<256, 512, LDS_BYTES, stream>>>((const char*)d_in[0], (const char*)d_in[1], (const char*)d_in[2], (const char*)d_in[3],
                                                 (const char*)d_in[4], (const char*)d_in[5], (const char*)d_in[6], (const char*)d_in[7],
                                                 (const char*)d_in[8], (const char*)d_in[9], (char*)d_out,
                                                 (char*)d_ws + (WSEND ? ((ws_size - WS_END) & ~(size_t)0xfffff) : 0));
}
```

```cpp
#include <hip/hip_runtime.h>
#include <hip/hip_bf16.h>
#include <stdint.h>

typedef short s16x8 __attribute__((ext_vector_type(8)));
typedef short s16x4 __attribute__((ext_vector_type(4)));
typedef short s16x2 __attribute__((ext_vector_type(2)));
typedef _Float16 h16x8 __attribute__((ext_vector_type(8)));
typedef _Float16 h16x2 __attribute__((ext_vector_type(2)));
typedef __bf16 b16x2 __attribute__((ext_vector_type(2)));
typedef float f32x16 __attribute__((ext_vector_type(16)));
typedef float f32x4 __attribute__((ext_vector_type(4)));
typedef float f32x2 __attribute__((ext_vector_type(2)));
typedef unsigned u32x4 __attribute__((ext_vector_type(4)));
typedef unsigned u32x16 __attribute__((ext_vector_type(16)));
typedef unsigned u32x2 __attribute__((ext_vector_type(2)));
typedef unsigned long long u64;
#define LAS __attribute__((address_space(3)))
#define GAS __attribute__((address_space(1)))
#if defined(__HIP_DEVICE_COMPILE__)
#define NOPK __attribute__((target("no-packed-fp32-ops")))
#else
#define NOPK
#endif
#define DI NOPK __device__ __forceinline__
#define SBAR() __builtin_amdgcn_sched_barrier(0)
#define PIN(x) asm volatile("" : "+v"(x))
#define MFMA32(a, b, c) __builtin_amdgcn_mfma_f32_32x32x16_bf16((a), (b), (c), 0, 0, 0)
#define MFMA32H(a, b, c) __builtin_amdgcn_mfma_f32_32x32x16_f16((a), (b), (c), 0, 0, 0)
#define MFMA16(a, b, c) __builtin_amdgcn_mfma_f32_16x16x32_bf16((a), (b), (c), 0, 0, 0)
#define AUX_SC1 16

#define PREFETCH 1
#define WSEND 0
#define APRIO 3
#define BCONV 0
#define ROT 0
#define AT_ALIGN "5"
#define TWOPOLL 0
#define STAMPS 0

constexpr unsigned WS_EPOCH = 0;
constexpr unsigned WS_QFLAG = 4096;
constexpr unsigned WS_OFLAG = WS_QFLAG + 256 * 128;
constexpr unsigned WS_UFLAG = WS_OFLAG + 1024 * 128;
constexpr unsigned WS_YFLAG = WS_UFLAG + 256 * 128;
constexpr unsigned WS_QKV = 393216;
constexpr unsigned WS_KV = WS_QKV + 16 * 64 * 2048;
constexpr unsigned WS_O = WS_QKV + 48 * 64 * 2048;
constexpr unsigned WS_UT = WS_O + 64 * 16 * 2048;
constexpr unsigned WS_YT = WS_UT + 256 * 8192;
constexpr unsigned WS_DBG = WS_YT + 224 * 8192;
constexpr unsigned WS_END = WS_DBG + 256 * 8 * 16 * 8;
static_assert(WS_YFLAG + 256 * 128 <= WS_QKV, "ws map");

constexpr unsigned L_RING = 0, SLOT_B = 28672, XCH_B = 16384;
constexpr unsigned L_B = 100352;
constexpr unsigned TAB_W = 14000, TAB_HI = 8448;
constexpr unsigned L_UIMG = 4 * SLOT_B + 2 * TAB_W;
constexpr unsigned L_T = 156352, T_B = 1296;
constexpr unsigned L_CV = 8192;
constexpr unsigned KC_S = 4160, UC_S = 4640, CH_B = 4 * KC_S + 2 * UC_S;
constexpr unsigned L_YIMG = L_B + 49152;
constexpr unsigned L_CTL = 163584;
constexpr unsigned LDS_BYTES = 163840;
constexpr unsigned C_A = L_CTL, C_B = L_CTL + 4, C_GO = L_CTL + 8, C_APUB = L_CTL + 12, C_BPUB = L_CTL + 16, C_WO = L_CTL + 20, C_BFREE = L_CTL + 24, C_BTAG = L_CTL + 28, C_U = L_CTL + 32, C_Q = L_CTL + 36, C_QR = L_CTL + 40, C_FB = L_CTL + 44, C_KM = L_CTL + 48, C_UI = L_CTL + 60, C_CH = L_CTL + 128;
constexpr unsigned C_L = L_CTL + 64, C_R = L_CTL + 96;
constexpr unsigned L_MAX = 162816;

constexpr unsigned L_SINK = L_MAX + 512;
constexpr unsigned SPIN_CAP = 1u << 22;
constexpr float LOG2E = 1.4426950408889634f;
constexpr float QSCALE = 0.18898223650461362f * LOG2E;
constexpr float SAFE_BOUND = 48.0f;

template <int V> struct IC { static constexpr int value = V; };
DI unsigned f2u(float f) { return __builtin_bit_cast(unsigned, f); }
DI float u2f(unsigned u) { return __builtin_bit_cast(float, u); }
DI unsigned umax(unsigned a, unsigned b) { return a > b ? a : b; }
DI unsigned sel(unsigned a, unsigned b, u64 mask) { return __builtin_amdgcn_inverse_ballot_w64(mask) ? b : a; }
DI float fexp(float x) { return __builtin_amdgcn_exp2f(x * LOG2E); }
DI float bf_lo(unsigned v) { return u2f(v << 16); }
DI float bf_hi(unsigned v) { return u2f(v & 0xffff0000u); }
DI unsigned pk_bf16(float a, float b) { f32x2 v = {a, b}; return __builtin_bit_cast(unsigned, __builtin_convertvector(v, b16x2)); }
DI unsigned pk_f16(float a, float b) { f32x2 v = {a, b}; return __builtin_bit_cast(unsigned, __builtin_convertvector(v, h16x2)); }
DI float bf_round(float a) { return bf_lo(pk_bf16(a, a)); }
template <int OFF> DI void glds(unsigned voff, const char* sbase, unsigned lds) {
  asm volatile("s_add_u32 m0, %2, %3\n\ts_nop 0\n\tglobal_load_lds_dwordx4 %0, %1" :: "v"(voff), "s"(sbase), "s"(lds), "i"(OFF) : "memory", "scc");
}
DI float half_swap_add(float v) {
  auto r = __builtin_amdgcn_permlane32_swap(f2u(v), f2u(v), false, false);
  return u2f(r[0]) + u2f(r[1]);
}
template <int OFF> DI void glds_dev(unsigned voff, const char* sbase, unsigned lds) {
  asm volatile("s_add_u32 m0, %2, %3\n\ts_nop 0\n\tglobal_load_lds_dwordx4 %0, %1 sc1" :: "v"(voff), "s"(sbase), "s"(lds), "i"(OFF) : "memory", "scc");
}
DI float dot2(unsigned a, float c) { return __builtin_amdgcn_fdot2_f32_bf16(__builtin_bit_cast(b16x2, a), __builtin_bit_cast(b16x2, a), c, false); }
DI float frag_norm2(u32x4 f0, u32x4 f1, int hh) {
  float a = dot2(f0[0], 0.f), b = dot2(f0[1], 0.f), c = dot2(f1[2], 0.f);
  a = dot2(f0[2], a); b = dot2(f0[3], b); c = dot2(f1[3], c); a = dot2(f1[0], a); b = dot2(f1[1], b);
  return half_swap_add(a + b + (hh ? 0.f : c));
}
DI s16x4 tr_read(unsigned a) { return __builtin_bit_cast(s16x4, __builtin_amdgcn_ds_read_tr16_b64_v4i16((LAS s16x4*)(uintptr_t)a)); }
DI s16x8 cat8(s16x4 lo, s16x4 hi) { return __builtin_shufflevector(lo, hi, 0, 1, 2, 3, 4, 5, 6, 7); }
template <class T> DI T lds_ld(unsigned a) { return *(LAS T*)(uintptr_t)a; }
template <class T> DI void lds_st(unsigned a, T v) { *(LAS T*)(uintptr_t)a = v; }
#define LDS_POLL(V, A, S, N, T, EXTRA) "s_mov_b32 " N ", 0\n1:\n\t" EXTRA "ds_read_b32 " V ", " A "\n\ts_add_u32 " N ", " N ", 1024\n\t" \
  "s_waitcnt lgkmcnt(0)\n\tv_readfirstlane_b32 " S ", " V "\n\ts_cbranch_scc1 4f\n\ts_cmp_lt_u32 " S ", " T "\n\ts_cbranch_scc1 1b\n4:\n\t"
DI unsigned lds_wait_ge(unsigned a, unsigned target) {
  unsigned v, sv, n;
  asm volatile(LDS_POLL("%0", "%3", "%1", "%2", "%4", "") : "=&v"(v), "=&s"(sv), "=&s"(n) : "v"(a), "s"(target) : "memory", "scc");
  return sv;
}
DI unsigned lds_wait_ge_slow(unsigned a, unsigned target) {
  unsigned v, sv, n;
  asm volatile(LDS_POLL("%0", "%3", "%1", "%2", "%4", "s_sleep 1\n\t") : "=&v"(v), "=&s"(sv), "=&s"(n) : "v"(a), "s"(target) : "memory", "scc");
  return sv;
}
DI void lds_arrive(unsigned a) {
  asm volatile("s_waitcnt lgkmcnt(0)\n\ts_mov_b64 exec, 1\n\tds_add_u32 %0, %1\n\ts_mov_b64 exec, -1" :: "v"(a), "v"(1u) : "memory");
}
DI void lds_arrive_nw(unsigned a) {
  asm volatile("s_mov_b64 exec, 1\n\tds_add_u32 %0, %1\n\ts_mov_b64 exec, -1" :: "v"(a), "v"(1u) : "memory");
}
DI void role_sync(unsigned a, unsigned nth) { lds_arrive(a); lds_wait_ge(a, nth * 4u); }
DI void role_sync_slow(unsigned a, unsigned nth) { lds_arrive(a); lds_wait_ge_slow(a, nth * 4u); }
DI void role_sync_idle(unsigned a, unsigned nth) {
  lds_arrive(a);
  unsigned v, sv, n;
  asm volatile(LDS_POLL("%0", "%3", "%1", "%2", "%4", "s_sleep 6\n\t") : "=&v"(v), "=&s"(sv), "=&s"(n) : "v"(a), "s"(nth * 4u) : "memory", "scc");
}
DI bool last_of_four(unsigned a, int lane, unsigned round) {
  unsigned old = 0;
  if (lane == 0) old = __hip_atomic_fetch_add((LAS unsigned*)(uintptr_t)a, 1u, __ATOMIC_RELAXED, __HIP_MEMORY_SCOPE_WORKGROUP);
  return (unsigned)__builtin_amdgcn_readfirstlane(old) == round * 4u + 3u;
}
DI void st128(u32x4 v, __amdgpu_buffer_rsrc_t r, unsigned voff, unsigned soff) {
  __builtin_amdgcn_raw_buffer_store_b128(v, r, voff, soff, AUX_SC1);
  asm volatile("s_nop 1" :: "v"(v) : "memory");
}
DI void drain_vm() { asm volatile("s_waitcnt vmcnt(0)" ::: "memory"); }
DI void flag_store(char* ws, unsigned off, u64 tag) { __hip_atomic_store((GAS u64*)(ws + off), tag, __ATOMIC_RELAXED, __HIP_MEMORY_SCOPE_AGENT); }
DI void flag_wait(char* ws, unsigned off, u64 tag) {
#pragma unroll 1
  for (unsigned n = 0; n < SPIN_CAP; ++n) {
    u64 v = __hip_atomic_load((GAS u64*)(ws + off), __ATOMIC_RELAXED, __HIP_MEMORY_SCOPE_AGENT);
    if (__builtin_amdgcn_ballot_w64(v != tag) == 0ull) break;
    __builtin_amdgcn_s_sleep(2);
  }
  asm volatile("" ::: "memory");
}
DI u32x4 rec_wait(__amdgpu_buffer_rsrc_t r, unsigned off, u64 tag) {
  u32x4 v;
#pragma unroll 1
  for (unsigned n = 0; n < SPIN_CAP; ++n) {
    v = __builtin_amdgcn_raw_buffer_load_b128(r, off, 0, AUX_SC1);
    if (__builtin_amdgcn_ballot_w64(v[0] != (unsigned)tag || v[2] != (unsigned)(tag >> 32)) == 0ull) break;
    asm volatile("s_sleep 1" ::: "memory");
  }
  asm volatile("" ::: "memory");
  return v;
}
template <int CTRL> DI unsigned dpp_max(unsigned v) { unsigned t = __builtin_amdgcn_update_dpp(0u, v, CTRL, 0xf, 0xf, false); return v > t ? v : t; }
DI unsigned wave_umax(unsigned v) {
  v = dpp_max<0x111>(v); v = dpp_max<0x112>(v); v = dpp_max<0x114>(v); v = dpp_max<0x118>(v);
  v = dpp_max<0x142>(v); v = dpp_max<0x143>(v);
  return __builtin_amdgcn_readlane(v, 63);
}
DI float half_swap_max(float v) {
  auto r = __builtin_amdgcn_permlane32_swap(f2u(v), f2u(v), false, false);
  return __builtin_fmaxf(u2f(r[0]), u2f(r[1]));
}
DI f32x2 cmul(f32x2 a, f32x2 b) { f32x2 r; r.x = __builtin_fmaf(-a.y, b.y, a.x * b.x); r.y = __builtin_fmaf(a.y, b.x, a.x * b.y); return r; }
DI void hmul3(unsigned a, unsigned b1, unsigned b2, unsigned b3, unsigned& r1, unsigned& r2, unsigned& r3) {
  asm("v_pk_mul_f16 %0, %3, %4 op_sel:[0,0] op_sel_hi:[0,1]\n\tv_pk_mul_f16 %1, %3, %5 op_sel:[0,0] op_sel_hi:[0,1]\n\tv_pk_mul_f16 %2, %3, %6 op_sel:[0,0] op_sel_hi:[0,1]\n\t"
      "v_pk_fma_f16 %0, %3, %4, %0 op_sel:[1,1,0] op_sel_hi:[1,0,1] neg_lo:[1,0,0]\n\tv_pk_fma_f16 %1, %3, %5, %1 op_sel:[1,1,0] op_sel_hi:[1,0,1] neg_lo:[1,0,0]\n\t"
      "v_pk_fma_f16 %2, %3, %6, %2 op_sel:[1,1,0] op_sel_hi:[1,0,1] neg_lo:[1,0,0]"
      : "=&v"(r1), "=&v"(r2), "=&v"(r3) : "v"(a), "v"(b1), "v"(b2), "v"(b3));
}
DI void hmul3x(unsigned a1, unsigned b1, unsigned a2, unsigned b2, unsigned a3, unsigned b3, unsigned& r1, unsigned& r2, unsigned& r3) {
  asm("v_pk_mul_f16 %0, %3, %4 op_sel:[0,0] op_sel_hi:[0,1]\n\tv_pk_mul_f16 %1, %5, %6 op_sel:[0,0] op_sel_hi:[0,1]\n\tv_pk_mul_f16 %2, %7, %8 op_sel:[0,0] op_sel_hi:[0,1]\n\t"
      "v_pk_fma_f16 %0, %3, %4, %0 op_sel:[1,1,0] op_sel_hi:[1,0,1] neg_lo:[1,0,0]\n\tv_pk_fma_f16 %1, %5, %6, %1 op_sel:[1,1,0] op_sel_hi:[1,0,1] neg_lo:[1,0,0]\n\t"
      "v_pk_fma_f16 %2, %7, %8, %2 op_sel:[1,1,0] op_sel_hi:[1,0,1] neg_lo:[1,0,0]"
      : "=&v"(r1), "=&v"(r2), "=&v"(r3) : "v"(a1), "v"(b1), "v"(a2), "v"(b2), "v"(a3), "v"(b3));
}
DI f32x2 zpow(float rho, float th, float p) {
  const float e = __builtin_amdgcn_exp2f(rho * p), f = __builtin_amdgcn_fractf(th * p);
  f32x2 r; r.x = e * __builtin_amdgcn_cosf(f); r.y = e * __builtin_amdgcn_sinf(f); return r;
}
#define STAMP(k) do { } while (0)
#define STAMP_FLUSH() do { } while (0)

#define QR(a, n) "v[" #a "+" #n ":" #a "+" #n "+1]"
#define Q_READ(F, XA, S) "ds_read_b128 v[" #F ":" #F "+3], " XA "\n\t" \
  "ds_read_b64_tr_b16 " QR(F, 4) ", %[wa] offset:" #S "*3072\n\tds_read_b64_tr_b16 " QR(F, 6) ", %[wa] offset:" #S "*3072+768\n\t" \
  "ds_read_b64_tr_b16 " QR(F, 8) ", %[wa] offset:" #S "*3072+56\n\tds_read_b64_tr_b16 " QR(F, 10) ", %[wa] offset:" #S "*3072+824\n\t" \
  "ds_read_b64_tr_b16 " QR(F, 12) ", %[wa] offset:" #S "*3072+112\n\tds_read_b64_tr_b16 " QR(F, 14) ", %[wa] offset:" #S "*3072+880\n\t"
#define Q_T(ACC, F, n) "v_mfma_f32_32x32x16_bf16 " ACC ", v[" #F "+" #n ":" #F "+" #n "+3], v[" #F ":" #F "+3], " ACC "\n\t"
#define Q_N(ACC, F, n) "v_mfma_f32_32x32x16_bf16 " ACC ", v[" #F ":" #F "+3], v[" #F "+" #n ":" #F "+" #n "+3], " ACC "\n\t"
#define Q_MMA(F, M0, M1, M2) M0("%[a0]", F, 4) M1("%[a1]", F, 8) M2("%[a2]", F, 12)
#define Q_ARRIVE "s_mov_b64 exec, 1\n\tds_add_u32 %[ca], %[one]\n\ts_mov_b64 exec, -1\n\t"
#define Q_TR(F, n, S, O) "ds_read_b64_tr_b16 " QR(F, n) ", %[wa] offset:" #S "*3072+" #O "\n\t"
#define Q_GROUP(G, F, XA, S, M0, M1, M2) \
  M0("%[a0]", G, 4) "ds_read_b128 v[" #F ":" #F "+3], " XA "\n\t" Q_TR(F, 4, S, 0) Q_TR(F, 6, S, 768) \
  M1("%[a1]", G, 8) Q_TR(F, 8, S, 56) Q_TR(F, 10, S, 824) \
  M2("%[a2]", G, 12) Q_TR(F, 12, S, 112) Q_TR(F, 14, S, 880)
#define QKV_BODY(M0, M1, M2) \
  "2:\n\t" \
  "v_add_u32 %[cl], 4, %[cl]\n\ts_waitcnt lgkmcnt(7)\n\t" \
  Q_GROUP(100, 132, "%[x2]", 2, M0, M1, M2) "ds_read_b32 %[tv], %[cl]\n\t" \
  "s_waitcnt lgkmcnt(8)\n\t" \
  Q_GROUP(116, 148, "%[x3]", 3, M0, M1, M2) \
  "s_cmp_eq_u32 %[tg], 28\n\ts_cbranch_scc1 5f\n\t" \
  "s_cmp_eq_u32 %[tg], 16\n\ts_cselect_b32 %[ts], %[wrap], %[step]\n\ts_add_u32 %[tg], %[tg], 4\n\t" \
  "v_add_u32 %[x0], %[ts], %[x0]\n\tv_add_u32 %[x1], %[ts], %[x1]\n\tv_add_u32 %[x2], %[ts], %[x2]\n\tv_add_u32 %[x3], %[ts], %[x3]\n\tv_add_u32 %[wa], %[ts], %[wa]\n\t" \
  "s_waitcnt lgkmcnt(7)\n\tv_readfirstlane_b32 %[ts], %[tv]\n\ts_cmp_ge_u32 %[ts], 2\n\ts_cbranch_scc1 0f\n\t" \
  Q_MMA(132, M0, M1, M2) \
  "s_cmp_lt_u32 %[tg], 16\n\ts_cbranch_scc1 3f\n\ts_waitcnt vmcnt(0)\n3:\n\t" \
  "s_waitcnt lgkmcnt(0)\n\t" Q_ARRIVE "s_nop 0\n\t" Q_MMA(148, M0, M1, M2) "v_add_u32 %[ca], 4, %[ca]\n\t" \
  LDS_POLL("%[tv]", "%[cl]", "%[ts]", "%[tn]", "2", "") \
  Q_READ(100, "%[x0]", 0) Q_READ(116, "%[x1]", 1) "s_branch 2b\n" \
  "0:\n\t" \
  Q_GROUP(132, 100, "%[x0]", 0, M0, M1, M2) \
  "s_cmp_lt_u32 %[tg], 16\n\ts_cbranch_scc1 3f\n\ts_waitcnt vmcnt(0)\n3:\n\t" \
  "s_waitcnt lgkmcnt(7)\n\t" Q_ARRIVE \
  Q_GROUP(148, 116, "%[x1]", 1, M0, M1, M2) \
  "v_add_u32 %[ca], 4, %[ca]\n\ts_branch 2b\n" \
  "5:\n\t" \
  "s_waitcnt lgkmcnt(7)\n\t" Q_MMA(132, M0, M1, M2) "s_waitcnt lgkmcnt(0)\n\t" Q_ARRIVE "s_nop 0\n\t" Q_MMA(148, M0, M1, M2)
#define QKV_LOOP() asm volatile( \
  "s_mov_b32 %[tg], 4\n\t" LDS_POLL("%[tv]", "%[cl]", "%[ts]", "%[tn]", "2", "") \
  Q_READ(100, "%[x0]", 0) Q_READ(116, "%[x1]", 1) \
  "s_cmp_gt_u32 %[g], 10\n\ts_cbranch_scc1 7f\n\ts_cmp_eq_u32 %[g], 10\n\ts_cbranch_scc1 6f\n" \
  QKV_BODY(Q_T, Q_T, Q_T) "s_branch 8f\n6:\n" QKV_BODY(Q_T, Q_T, Q_N) "s_branch 8f\n7:\n" QKV_BODY(Q_N, Q_N, Q_N) \
  "8:\n\ts_nop 7\n\ts_nop 7" \
  : [a0] "+v"(acc[0]), [a1] "+v"(acc[1]), [a2] "+v"(acc[2]), [x0] "+v"(xa0), [x1] "+v"(xa1), [x2] "+v"(xa2), [x3] "+v"(xa3), [wa] "+v"(wa), \
    [tv] "=&v"(tv), [ts] "=&s"(ts), [tn] "=&s"(tn), [tg] "=&s"(tg), [ca] "+v"(car), [cl] "+v"(cla) \
  : [one] "v"(1u), [step] "s"(SLOT_B), [wrap] "s"(0u - 3u * SLOT_B), [g] "s"(g) \
  : "memory", "scc", V100_163)
#define V10(p) "v" #p "0", "v" #p "1", "v" #p "2", "v" #p "3", "v" #p "4", "v" #p "5", "v" #p "6", "v" #p "7", "v" #p "8", "v" #p "9"
#define V100_163 V10(10), V10(11), V10(12), V10(13), V10(14), V10(15), "v160", "v161", "v162", "v163"

#define AT_M(D, A, B, C) "v_mfma_f32_32x32x16_bf16 v[" #D ":" #D "+15], v[" A ":" A "+3], v[" B ":" B "+3], " C "\n\t"
#define AT_ACC(D) "v[" #D ":" #D "+15]"
#define AT_E(C, i) "v_exp_f32 v[" #C "+" #i "], v[" #C "+" #i "]\n\t"
#define AT_P(P, C, i) "v_cvt_pk_bf16_f32 v[" #P "], v[" #C "+" #i "], v[" #C "+" #i "+1]\n\t"
#define AT_STEP(C, N, KN, QN, VP, OP, VC, OC, X) \
  AT_M(N, #KN, #QN, "0") AT_E(C, 0) AT_E(C, 1) AT_E(C, 2) AT_E(C, 3) AT_E(C, 4) \
  AT_M(OP, #VP "+12", "100", AT_ACC(OP)) X AT_E(C, 5) AT_E(C, 6) AT_E(C, 7) AT_P(96, C, 0) AT_P(97, C, 2) AT_P(98, C, 4) AT_P(99, C, 6) \
  AT_M(N, #KN "+4", #QN "+4", AT_ACC(N)) AT_E(C, 8) AT_E(C, 9) AT_E(C, 10) AT_E(C, 11) AT_E(C, 12) \
  AT_M(OC, #VC "+8", "96", AT_ACC(OC)) AT_E(C, 13) AT_E(C, 14) AT_E(C, 15) AT_P(100, C, 8) AT_P(101, C, 10) AT_P(102, C, 12) AT_P(103, C, 14)
#define AT_LD(B) "global_load_dwordx4 v[" #B ":" #B "+3], %[vo], s[40:41] sc1\n\tglobal_load_dwordx4 v[" #B "+4:" #B "+7], %[vo], s[40:41] offset:1024 sc1\n\t" \
  "global_load_dwordx4 v[" #B "+8:" #B "+11], %[vo], s[40:41] offset:2048 sc1\n\tglobal_load_dwordx4 v[" #B "+12:" #B "+15], %[vo], s[40:41] offset:3072 sc1\n\t" \
  "s_add_u32 %[kl], %[kl], 4096\n\ts_addc_u32 %[kh], %[kh], 0\n\t"
#define AT_RED(A, O) "ds_write_b128 " A ", v[" #O ":" #O "+3]\n\tds_write_b128 " A ", v[" #O "+4:" #O "+7] offset:1024\n\t" \
  "ds_write_b128 " A ", v[" #O "+8:" #O "+11] offset:2048\n\tds_write_b128 " A ", v[" #O "+12:" #O "+15] offset:3072\n\t"
#define AT_TILE(T, TP, TN, LD, W, R1, R2, R3) \
  AT_STEP(64, 80, T, 112, TP, 48, T, 0, "") LD AT_STEP(80, 64, T, 120, T, 0, T, 16, "") AT_STEP(64, 80, T, 128, T, 16, T, 32, R1) W \
  AT_STEP(80, 64, TN, 104, T, 32, T, 48, R2) R3
#define AT_W(n) "s_waitcnt vmcnt(" #n ")\n\t"

NOPK __global__ void __launch_bounds__(512, 2)
fused_mha_s4d(const char* __restrict__ x, const char* __restrict__ wqkv, const char* __restrict__ wo, const char* __restrict__ u,
              const char* __restrict__ logAr, const char* __restrict__ Aim, const char* __restrict__ Cre, const char* __restrict__ Cim,
              const char* __restrict__ logdt, const char* __restrict__ Dsk, char* __restrict__ out, char* __restrict__ ws) {
  extern __shared__ __attribute__((aligned(16))) char smem[];
  const int tid = __builtin_amdgcn_workitem_id_x(), lane = tid & 63;
  const int wid = __builtin_amdgcn_readfirstlane(tid >> 6);
  const int b = __builtin_amdgcn_workgroup_id_x(), xcd = b & 7, idx = b >> 3;
  const int r32 = lane & 31, hh = lane >> 5;
  const unsigned L0 = (unsigned)(uintptr_t)smem;
  (void)L0;
  if (wid >= 4) {
    u64 pc; unsigned len;
    asm volatile("s_getpc_b64 %0\n.Lpf_begin:\n\ts_mov_b32 %1, .Lpf_end-.Lpf_begin" : "=s"(pc), "=s"(len));
    const unsigned off = ((tid - 256) * 8 + (idx & 7)) * 128;
    if (off < len) asm volatile("s_mov_b32 m0, %2\n\ts_nop 0\n\tglobal_load_lds_dword %0, %1" :: "v"(off), "s"(pc), "s"(L_SINK) : "memory");
  }
  if (wid == 2) lds_st<u32x4>(L_MAX + lane * 16, u32x4{0u, 0u, 0u, 0u});
  asm volatile("s_waitcnt lgkmcnt(0)\n\ts_barrier" ::: "memory");
  const __amdgpu_buffer_rsrc_t wsr = __builtin_amdgcn_make_buffer_rsrc(ws, 0, WS_END, 0x00020000);
  asm volatile("" :: "s"(x), "s"(wqkv), "s"(wo), "s"(u), "s"(logAr), "s"(Aim), "s"(Cre), "s"(Cim), "s"(logdt), "s"(Dsk), "s"(out), "s"(ws));
  STAMP(0);
#define MAKE_TAG(e) (((u64)(unsigned)__builtin_amdgcn_readfirstlane((unsigned)((e) >> 32)) << 32 | (unsigned)__builtin_amdgcn_readfirstlane((unsigned)(e))) + 0x9E3779B97F4A7C15ull)
  const int rt = (xcd & 3) * 4 + (idx >> 3), g = (xcd >> 2) * 8 + (idx & 7);
  const int woff = (g == 15) ? 8 : 0;
  const bool has_b = xcd < 7;

  if (__builtin_expect(wid >= 4, 0)) {
    const int w = wid - 4;
    __builtin_amdgcn_s_setprio(APRIO);
    const u64 epoch = __hip_atomic_load((GAS u64*)(ws + WS_EPOCH), __ATOMIC_RELAXED, __HIP_MEMORY_SCOPE_AGENT);
    const int rt32 = rt * 4 + w;
    unsigned l16 = lane * 16, so[3], kma[3], vcst = r32 == 28 ? 0x3F803F80u : 0u; unsigned scl[3]; u64 vmask[3], pmask[3], qmask[3];
#pragma unroll
    for (int j = 0; j < 3; ++j) {
      const unsigned sl = 3 * g + j;
      unsigned mq = (unsigned)((int)(sl - 16) >> 31), mp = (unsigned)((int)(sl - 32) >> 31);
      asm volatile("" : "+s"(mq), "+s"(mp));
      const unsigned t64 = (sl & 15) * 64 + rt32;
      so[j] = ((WS_QKV + t64 * 2048) & mq) | ((WS_KV + t64 * 4096 + (2048u & ~mp)) & ~mq);
      scl[j] = (__builtin_bit_cast(unsigned, QSCALE) & mq) | (0x3F800000u & ~mq);
      const unsigned mv = 0xF0000000u & ~mp;
      vmask[j] = (u64)mv << 32 | mv; pmask[j] = (u64)mp << 32; qmask[j] = (u64)mq << 32;
      kma[j] = C_KM + j * 4;
      asm volatile("" : "+s"(so[j]), "+s"(scl[j]), "+s"(vmask[j]), "+s"(pmask[j]), "+s"(qmask[j]), "+v"(kma[j]));
    }
    PIN(l16); PIN(vcst);
    f32x16 acc[3];
#pragma unroll
    for (int j = 0; j < 3; ++j) acc[j] = f32x16{};
    {
      const unsigned xa_l = w * 4096 + r32 * 128, xsw = (r32 >> 1) & 7;
      unsigned xa0 = xa_l + (((0 + hh) ^ xsw) << 4), xa1 = xa_l + (((2 + hh) ^ xsw) << 4), xa2 = xa0 ^ 64, xa3 = xa1 ^ 64;
      unsigned wa = XCH_B + (8 * hh + ((lane & 15) >> 2)) * 192 + woff + ((lane >> 4) & 1) * 32 + (lane & 3) * 8;
      unsigned tv, ts, tn, tg, car = C_R, cla = C_L;
      asm volatile("" : "+v"(acc[0]), "+v"(acc[1]), "+v"(acc[2]), "+v"(xa0), "+v"(xa1), "+v"(xa2), "+v"(xa3), "+v"(wa), "+v"(car), "+v"(cla));
      if (has_b) {
        lds_wait_ge(C_UI, 2);
        const int oct = (lane >> 1) & 7;
        s16x8 v[2];
#pragma unroll
        for (int i = 0; i < 2; ++i) {
          const int ch = 2 * (w * 8 + i * 4 + (lane >> 4)) + (lane & 1);
          const unsigned a = L_UIMG + oct * 1024 + (((ch >> 3) ^ oct) << 4) + (ch & 7) * 2;
#pragma unroll
          for (int e = 0; e < 8; ++e) v[i][e] = lds_ld<short>(a + e * 128);
        }
#pragma unroll
        for (int i = 0; i < 2; ++i)
          st128(__builtin_bit_cast(u32x4, v[i]), wsr, ((xcd * 32 + w * 8 + i * 4 + (lane >> 4)) * 32 + (31 - idx)) * 256 + (lane & 15) * 16, WS_UT);
      }
      STAMP(9);
      QKV_LOOP();
    }
    STAMP(2);
#pragma unroll
    for (int j = 0; j < 3; ++j) {
      u32x4 f0, f1;
#pragma unroll
      for (int i = 0; i < 4; ++i) f0[i] = sel(pk_bf16(acc[j][2 * i] * u2f(scl[j]), acc[j][2 * i + 1] * u2f(scl[j])), vcst, vmask[j]);
      st128(f0, wsr, l16, so[j]);
#pragma unroll
      for (int i = 0; i < 4; ++i) f1[i] = sel(pk_bf16(acc[j][8 + 2 * i] * u2f(scl[j]), acc[j][9 + 2 * i] * u2f(scl[j])), vcst, vmask[j]);
      const unsigned n2 = f2u(frag_norm2(f0, f1, hh));
      asm volatile("ds_max_u32 %0, %1" :: "v"(kma[j]), "v"(n2) : "memory");
      f1[2] = sel(f1[2], sel(0x3F803F80u, n2, qmask[j]), pmask[j]); f1[3] = sel(f1[3], 0x00003F80u, pmask[j]);
      st128(f1, wsr, l16 + 1024, so[j]);
    }
    const u64 tag = MAKE_TAG(epoch);
    STAMP(14);
    const int h = xcd * 2 + (idx >> 4), qt = idx & 15;
    f32x16 o[4];
#pragma unroll
    for (int i = 0; i < 4; ++i) { o[i] = f32x16{}; PIN(o[i]); }
    if (w == 3) lds_wait_ge(C_BTAG, 4);
    drain_vm();
    STAMP(15);
    if (w == 3) {
      lds_wait_ge(C_APUB, 3);
      const u32x4 km = lds_ld<u32x4>(C_KM);
      const u32x4 rec = {(unsigned)tag, ((km[0] + 0xffffu) >> 16) | ((km[1] + 0xffffu) & 0xffff0000u), (unsigned)(tag >> 32), km[2] + 0xffffu};
      if (lane == 0) st128(rec, wsr, (g * 16 + rt) * 128, WS_QFLAG);
    } else {
      lds_arrive_nw(C_APUB);
      if (w == 0) {
        rec_wait(wsr, WS_QFLAG + ((h / 3) * 16 + qt) * 128, tag);
        lds_wait_ge(C_R + 24, 4);
        const char* qs = ws + WS_QKV + (h * 64 + qt * 4) * 2048;
        glds_dev<0>(l16, qs, L_RING); glds_dev<1024>(l16, qs + 1024, L_RING); glds_dev<2048>(l16, qs + 2048, L_RING); glds_dev<3072>(l16, qs + 3072, L_RING);
        glds_dev<4096>(l16, qs + 4096, L_RING); glds_dev<5120>(l16, qs + 5120, L_RING); glds_dev<6144>(l16, qs + 6144, L_RING); glds_dev<7168>(l16, qs + 7168, L_RING);
        drain_vm();
        lds_arrive_nw(C_Q);
      } else if (w == 1 || TWOPOLL) {
        if (w == 2) __builtin_amdgcn_s_sleep(7);
        const u32x4 rec = rec_wait(wsr, WS_QFLAG + ((hh ? (32 + h) / 3 : (16 + h) / 3) * 16 + (lane & 15)) * 128, tag);
        const int j = (16 + h) % 3;
        unsigned v = j == 0 ? rec[1] << 16 : ((j == 1 ? rec[1] : rec[3]) & 0xffff0000u);
        v = dpp_max<0x111>(v); v = dpp_max<0x112>(v); v = dpp_max<0x114>(v); v = dpp_max<0x118>(v);
        lds_st<unsigned>(C_GO, __builtin_amdgcn_readlane(v, 15) + 1u);
      }
    }
    STAMP(3);
    s16x8 qf[4][2];
    const unsigned kbase = WS_KV + (h * 64 + w * 16) * 4096;
#define LDF(soff, voff) __builtin_bit_cast(s16x8, __builtin_amdgcn_raw_buffer_load_b128(wsr, l16 + (voff), (soff), AUX_SC1))
    const unsigned km2 = lds_wait_ge_slow(C_GO, 1);
    STAMP(4);
    u32x16 kv0, kv1, kv2;
    {
      const char* kb = ws + kbase;
      asm volatile("global_load_dwordx4 v[136:139], %3, %4 sc1\n\tglobal_load_dwordx4 v[140:143], %3, %4 offset:1024 sc1\n\tglobal_load_dwordx4 v[144:147], %3, %4 offset:2048 sc1\n\t"
                   "global_load_dwordx4 v[148:151], %3, %4 offset:3072 sc1\n\tglobal_load_dwordx4 v[152:155], %3, %5 sc1\n\tglobal_load_dwordx4 v[156:159], %3, %5 offset:1024 sc1\n\t"
                   "global_load_dwordx4 v[160:163], %3, %5 offset:2048 sc1\n\tglobal_load_dwordx4 v[164:167], %3, %5 offset:3072 sc1\n\tglobal_load_dwordx4 v[168:171], %3, %6 sc1\n\t"
                   "global_load_dwordx4 v[172:175], %3, %6 offset:1024 sc1\n\tglobal_load_dwordx4 v[176:179], %3, %6 offset:2048 sc1\n\tglobal_load_dwordx4 v[180:183], %3, %6 offset:3072 sc1"
                   : "=&{v[136:151]}"(kv0), "=&{v[152:167]}"(kv1), "=&{v[168:183]}"(kv2) : "v"(l16), "s"(kb), "s"(kb + 4096), "s"(kb + 8192) : "memory");
    }
    lds_wait_ge(C_Q, 1);
    STAMP(1);
#pragma unroll
    for (int i = 0; i < 4; ++i) { const unsigned a = L_RING + ((w + 1 + i) & 3) * 2048 + l16; qf[i][0] = lds_ld<s16x8>(a); qf[i][1] = lds_ld<s16x8>(a + 1024); }
    lds_arrive(C_QR);
    {
      const float kmax2 = u2f(km2) * 1.02f;
      unsigned pad2[4], pad3[4]; bool unsafe = false;
#pragma unroll
      for (int i = 0; i < 4; ++i) {
        const float m = __builtin_amdgcn_sqrtf(u2f(__builtin_bit_cast(u32x4, qf[i][1])[2]) * kmax2);
        unsafe |= hh && !(m <= SAFE_BOUND);
        pad2[i] = pk_bf16(-m, 0.f); pad3[i] = 0u;
      }
      if (__builtin_expect(__builtin_amdgcn_ballot_w64(unsafe) != 0ull, 0)) {
        float mx[4];
#pragma unroll
        for (int i = 0; i < 4; ++i) { mx[i] = -INFINITY; if (hh) { u32x4 t_ = __builtin_bit_cast(u32x4, qf[i][1]); t_[2] = 0u; t_[3] = 0u; qf[i][1] = __builtin_bit_cast(s16x8, t_); } }
#pragma unroll 1
        for (int t = 0; t < 16; ++t) {
          const s16x8 k0 = LDF(kbase + t * 4096, 0), k1 = LDF(kbase + t * 4096, 1024);
#pragma unroll
          for (int i = 0; i < 4; ++i) {
            f32x16 s = MFMA32(k0, qf[i][0], f32x16{}); s = MFMA32(k1, qf[i][1], s);
#pragma unroll
            for (int e = 0; e < 16; ++e) mx[i] = __builtin_fmaxf(mx[i], s[e]);
          }
        }
#pragma unroll
        for (int i = 0; i < 4; ++i) {
          const float v = half_swap_max(mx[i]);
          const unsigned k_ = f2u(v) ^ ((unsigned)((int)f2u(v) >> 31) | 0x80000000u);
          __hip_atomic_fetch_max((LAS unsigned*)(uintptr_t)(L_MAX + (((w + 1 + i) & 3) * 32 + r32) * 4), k_, __ATOMIC_RELAXED, __HIP_MEMORY_SCOPE_WORKGROUP);
        }
        role_sync(C_FB, 1);
#pragma unroll
        for (int i = 0; i < 4; ++i) {
          const unsigned k_ = lds_ld<unsigned>(L_MAX + (((w + 1 + i) & 3) * 32 + r32) * 4);
          const float m = u2f(k_ ^ ((unsigned)((int)~k_ >> 31) | 0x80000000u));
          const float m0 = bf_round(-m), r1 = -m - m0, m1 = bf_round(r1);
          pad2[i] = pk_bf16(m0, m1); pad3[i] = pk_bf16(r1 - m1, 0.f);
        }
      }
#pragma unroll
      for (int i = 0; i < 4; ++i) {
        u32x4 t_ = __builtin_bit_cast(u32x4, qf[i][1]);
        t_[2] = hh ? pad2[i] : t_[2]; t_[3] = hh ? pad3[i] : t_[3]; qf[i][1] = __builtin_bit_cast(s16x8, t_);
      }
    }
    unsigned redw = L_B + l16; PIN(redw);
    __builtin_amdgcn_s_setprio(1);
    STAMP(5);
    {
      const u64 kn = (u64)(ws + kbase + 3 * 4096);
      unsigned kl = (unsigned)kn, kh = (unsigned)(kn >> 32), tv, ts, tn, it;
      asm volatile(
        "s_mov_b32 %[it], 3\n\tv_mov_b32 v100, 0\n\tv_mov_b32 v101, 0\n\tv_mov_b32 v102, 0\n\tv_mov_b32 v103, 0\n\t"
        "v_mov_b32 v196, 0\n\tv_mov_b32 v197, 0\n\tv_mov_b32 v198, 0\n\tv_mov_b32 v199, 0\n\t"
        AT_W(10) AT_M(64, "136", "104", "0") AT_M(64, "140", "108", AT_ACC(64)) AT_W(8) "s_nop 7\n\ts_nop 3\n\t.p2align " AT_ALIGN "\n"
        "2:\n\t"
        AT_TILE(136, 184, 152, AT_LD(184), AT_W(8), "", "", "") AT_TILE(152, 136, 168, AT_LD(136), AT_W(8), "", "", "")
        AT_TILE(168, 152, 184, AT_LD(152), AT_W(8), "", "", "") AT_TILE(184, 168, 136, AT_LD(168), AT_W(8), "", "", "s_sub_u32 %[it], %[it], 1\n\t")
        "s_cmp_lg_u32 %[it], 0\n\ts_cbranch_scc1 2b\n\t"
        AT_TILE(136, 184, 152, AT_LD(184), AT_W(8), "", "", "") AT_TILE(152, 136, 168, "", AT_W(4), "", "", "")
        AT_TILE(168, 152, 184, "", AT_W(0), "", "", "")
        LDS_POLL("%[tv]", "%[bf]", "%[ts]", "%[tn]", "4", "")
        AT_TILE(184, 168, 136, "", "", AT_RED("%[r0]", 0), AT_RED("%[r1]", 16), "")
        "s_nop 1\n\t" AT_M(48, "184+12", "100", AT_ACC(48)) AT_RED("%[r2]", 32) "s_nop 7"
        : "+{v[0:15]}"(o[0]), "+{v[16:31]}"(o[1]), "+{v[32:47]}"(o[2]), "+{v[48:63]}"(o[3]), "+{v[136:151]}"(kv0), "+{v[152:167]}"(kv1), "+{v[168:183]}"(kv2),
          [kl] "+{s40}"(kl), [kh] "+{s41}"(kh), [tv] "=&v"(tv), [ts] "=&s"(ts), [tn] "=&s"(tn), [it] "=&s"(it)
        : "{v[104:107]}"(qf[0][0]), "{v[108:111]}"(qf[0][1]), "{v[112:115]}"(qf[1][0]), "{v[116:119]}"(qf[1][1]), "{v[120:123]}"(qf[2][0]), "{v[124:127]}"(qf[2][1]),
          "{v[128:131]}"(qf[3][0]), "{v[132:135]}"(qf[3][1]), [vo] "v"(l16), [bf] "v"(C_BFREE),
          [r0] "v"(redw + (((w + 1) & 3) * 3 + 2) * 4096), [r1] "v"(redw + (((w + 2) & 3) * 3 + 1) * 4096), [r2] "v"(redw + (((w + 3) & 3) * 3) * 4096)
        : "memory", "scc", "v64", "v65", "v66", "v67", "v68", "v69", V10(7), V10(8), V10(9), "v100", "v101", "v102", "v103",
          "v184", "v185", "v186", "v187", "v188", "v189", V10(19));
    }
    STAMP(6);
    role_sync(C_A, 1);
    {
      f32x4 pv[12];
#pragma unroll
      for (int k = 0; k < 12; ++k) pv[k] = lds_ld<f32x4>(redw + w * 12288 + k * 1024);
      SBAR();
      f32x16 a = o[3];
#pragma unroll
      for (int k = 0; k < 12; ++k) { const int q = k & 3; a[4 * q] += pv[k][0]; a[4 * q + 1] += pv[k][1]; a[4 * q + 2] += pv[k][2]; a[4 * q + 3] += pv[k][3]; }
      auto lr = __builtin_amdgcn_permlane32_swap(f2u(a[12]), f2u(a[12]), false, false);
      const float inv = __builtin_amdgcn_rcpf(u2f(lr[1]));
      u32x4 f0, f1;
#pragma unroll
      for (int i = 0; i < 4; ++i) { f0[i] = pk_bf16(a[2 * i] * inv, a[2 * i + 1] * inv); f1[i] = pk_bf16(a[8 + 2 * i] * inv, a[9 + 2 * i] * inv); }
      if (hh) { f1[2] = 0u; f1[3] = 0u; }
      const unsigned so = WS_O + ((qt * 4 + w) * 16 + h) * 2048;
      st128(f0, wsr, l16, so);
      st128(f1, wsr, l16 + 1024, so);
      lds_arrive(C_A);
      drain_vm();
      if (lane == 0) flag_store(ws, WS_OFLAG + ((qt * 4 + w) * 16 + h) * 128, tag);
    }
    STAMP(7);
    const int rs = xcd * 8 + (idx >> 2), cg = idx & 3, n0 = w < 3 ? 32 * w : 80;
    lds_wait_ge(C_A, 2 * 4);
    lds_wait_ge(C_WO, 4);
    flag_wait(ws, WS_OFLAG + (rs * 16 + w * 4 + (lane & 3)) * 128, tag);
    STAMP(8);
    {
      const char* os = ws + WS_O + (rs * 16 + w * 4) * 2048;
      const unsigned od = L_B + w * 8192;
      glds_dev<0>(l16, os, od); glds_dev<1024>(l16, os + 1024, od); glds_dev<2048>(l16, os + 2048, od); glds_dev<3072>(l16, os + 3072, od);
      glds_dev<4096>(l16, os + 4096, od); glds_dev<5120>(l16, os + 5120, od); glds_dev<6144>(l16, os + 6144, od); glds_dev<7168>(l16, os + 7168, od);
    }
    s16x8 wf[32];
    unsigned oa[4];
#pragma unroll
    for (int q = 0; q < 4; ++q) {
      const int gq = (w + q) & 3;
      unsigned wa = L_RING + (gq * 112 + 4 * hh + ((lane & 15) >> 2)) * 224 + n0 * 2 + ((lane >> 4) & 1) * 32 + (lane & 3) * 8;
      unsigned wb2 = wa + (hh ? 16 * 224 : 24 * 224);
      oa[q] = redw + gq * 8192;
      PIN(wa); PIN(wb2); PIN(oa[q]);
#pragma unroll
      for (int hd = 0; hd < 4; ++hd) {
        wf[q * 8 + hd * 2] = cat8(tr_read(wa + hd * 28 * 224), tr_read(wa + hd * 28 * 224 + 8 * 224));
        wf[q * 8 + hd * 2 + 1] = cat8(tr_read(wa + hd * 28 * 224 + 16 * 224), tr_read(wb2 + hd * 28 * 224));
      }
    }
    drain_vm();
    lds_arrive_nw(C_A);
    STAMP(13);
    f32x16 d = f32x16{};
    {
      s16x8 ob[8];
#pragma unroll
      for (int k = 0; k < 8; ++k) ob[k] = lds_ld<s16x8>(oa[0] + k * 1024);
      SBAR();
#pragma unroll
      for (int k = 0; k < 4; ++k) { d = MFMA32(wf[k], ob[k], d); SBAR(); }
      lds_wait_ge(C_A, 3 * 4);
#pragma unroll
      for (int k = 0; k < 4; ++k) ob[k] = lds_ld<s16x8>(oa[1] + k * 1024);
      SBAR();
#pragma unroll
      for (int k = 4; k < 32; ++k) {
        d = MFMA32(wf[k], ob[k & 7], d);
        if (k + 8 < 32) ob[k & 7] = lds_ld<s16x8>(oa[(k + 8) >> 3] + (k & 7) * 1024);
        SBAR();
      }
    }
    {
      char* op = out + (size_t)(rs * 32 * 1792 + (cg * 112 + n0) * 2) + (unsigned)(r32 * 1792 + hh * 16);
#pragma unroll
      for (int k = 0; k < 2; ++k) {
        const unsigned a0 = pk_bf16(d[8 * k], d[8 * k + 1]), a1 = pk_bf16(d[8 * k + 2], d[8 * k + 3]);
        const unsigned b0 = pk_bf16(d[8 * k + 4], d[8 * k + 5]), b1 = pk_bf16(d[8 * k + 6], d[8 * k + 7]);
        auto s0 = __builtin_amdgcn_permlane32_swap(a0, b0, false, false);
        auto s1 = __builtin_amdgcn_permlane32_swap(a1, b1, false, false);
        if (k || w < 3) *(GAS u32x4*)(op + k * 32) = u32x4{s0[0], s1[0], s0[1], s1[1]};
      }
    }
    STAMP(10);
    STAMP_FLUSH();
    asm volatile(".Lpf_end:");
  } else {
    const int wb = wid ^ 2;
    const int c = wb & 1, half = wb >> 1;
    const int d = (has_b ? xcd * 64 : 0) + idx * 2 + c;
    const unsigned po = d * 512 + lane * 4;
    const unsigned cb = L_CV + c * CH_B, cw = C_CH + c * 16;
    const unsigned tw = (lane >> 3) * 1056 + ((lane >> 2) & 1) * 528 + (lane & 3) * 4, lo2 = 3 * SLOT_B + c * TAB_HI;
    u64 tag;
#define LOAD_P(P, O) do { P[0] = *(const GAS unsigned*)(logAr + (O) + po); P[1] = *(const GAS unsigned*)(Aim + (O) + po); \
      P[2] = *(const GAS unsigned*)(Cre + (O) + po); P[3] = *(const GAS unsigned*)(Cim + (O) + po); } while (0)
    const unsigned T = L_T + c * T_B;
    unsigned ga[2][11], wv[7];
    auto gather_addr = [&]() __attribute__((always_inline)) NOPK {
#pragma unroll
      for (int i = 0; i < 7; ++i) {
        const unsigned p = i * 64 + lane, row = (p * 37450u) >> 19; wv[i] = row * 896 + (p - row * 14) * 16; PIN(wv[i]);
      }
#pragma unroll
      for (int it = 0; it < 2; ++it) {
        const int l0 = 8 * (3 + it * 128 + half * 64 + lane) - 27;
        const float f0 = (float)l0;
#pragma unroll
        for (int k = 0; k < 11; k += 2) {
          const unsigned pk = pk_bf16(f0 + (float)k, f0 + (float)(k + 1));
          asm volatile("v_mad_u32_u16 %0, %1, 2, %2" : "=v"(ga[it][k]) : "v"(pk), "s"(T - 2u * 17024u));
          if (k < 10) asm volatile("v_mad_u32_u16 %0, %1, 2, %2 op_sel:[1,0,0,0]" : "=v"(ga[it][k + 1]) : "v"(pk), "s"(T - 2u * 17024u));
        }
        if (it == 0 && !half) {
#pragma unroll
          for (int k = 0; k < 11; ++k) { const int l = l0 + k; ga[0][k] = l < 128 ? T + 2 * ((k < 3 && l < 0) ? 641 : l) : ga[0][k]; PIN(ga[0][k]); }
        }
      }
    };
    auto lo_vals = [&](float rho, float th, unsigned (&lo)[32]) __attribute__((always_inline)) NOPK {
      const f32x2 z8 = zpow(rho, th, 8.f), z16 = cmul(z8, z8), z24 = cmul(z8, z16), z32 = cmul(z16, z16);
      lo[0] = 0x00003C00u; lo[1] = pk_f16(z8.x, z8.y); lo[2] = pk_f16(z16.x, z16.y); lo[3] = pk_f16(z24.x, z24.y);
      f32x2 e = z32;
#pragma unroll
      for (int a = 1; a < 8; ++a) {
        lo[4 * a] = pk_f16(e.x, e.y);
        if (a < 7) e = cmul(e, z32);
        hmul3(lo[4 * a], lo[1], lo[2], lo[3], lo[4 * a + 1], lo[4 * a + 2], lo[4 * a + 3]);
      }
    };
    auto hi_vals = [&](float rho, float th, f32x2 hA, unsigned (&hi)[21]) __attribute__((always_inline)) NOPK {
      const f32x2 z1 = zpow(rho, -th, 1.f), z256 = zpow(rho, -th, 256.f);
#define F16(v) pk_f16((v).x, (v).y)
      const f32x2 z2 = cmul(z1, z1), z3 = cmul(z1, z2), z4 = cmul(z2, z2), w2 = cmul(z256, z256), w3 = cmul(z256, w2), w4 = cmul(w2, w2);
      const f32x2 h4 = cmul(hA, z4), g4 = cmul(hA, w4);
      const unsigned e1 = F16(z1), e2 = F16(z2), e3 = F16(z3), v1 = F16(z256), v2 = F16(w2), v3 = F16(w3), v4 = F16(w4);
      hi[0] = F16(hA); hi[4] = F16(h4); hi[16] = F16(g4);
      hmul3(hi[0], e1, e2, e3, hi[1], hi[2], hi[3]); hmul3(hi[4], e1, e2, e3, hi[5], hi[6], hi[7]);
      hmul3(hi[0], v1, v2, v3, hi[8], hi[12], hi[14]); hmul3(hi[4], v1, v2, v3, hi[10], hi[13], hi[15]);
      hmul3(hi[16], v1, v2, v3, hi[17], hi[18], hi[19]);
      hmul3x(v1, hi[2], v1, hi[6], v4, hi[16], hi[9], hi[11], hi[20]);
    };
    if (half) {
      unsigned xo[4], wo_[6];
#pragma unroll
      for (int q = 0; q < 4; ++q) xo[q] = (q * 8 + (lane >> 3)) * 896 + (((lane & 7) ^ ((q & 1) * 4 + (lane >> 4))) << 4);
      const char* xb = x + (size_t)(rt * 128 + c * 64) * 896;
      const char* wq = wqkv + g * 168 - woff;
      int kc = ROT ? ((idx & 7) + 2 * (idx >> 3)) % 7 : 0;
      u64 epoch;
      asm volatile("global_load_dwordx2 %0, %1, %2 sc1" : "=v"(epoch) : "v"(0u), "s"(ws + WS_EPOCH) : "memory");
#define X_PIECES(B, D) do { glds<0>(xo[0], B, D); glds<1024>(xo[1], B, D); glds<2048>(xo[2], B, D); glds<3072>(xo[3], B, D); \
        glds<4096>(xo[0], (B) + 32 * 896, D); glds<5120>(xo[1], (B) + 32 * 896, D); glds<6144>(xo[2], (B) + 32 * 896, D); glds<7168>(xo[3], (B) + 32 * 896, D); } while (0)
#define W_PIECES(B, D) do { glds<0>(wo_[0], B, D); glds<1024>(wo_[1], B, D); glds<2048>(wo_[2], B, D); glds<3072>(wo_[3], B, D); glds<4096>(wo_[4], B, D); glds<5120>(wo_[5], B, D); } while (0)
      {
        const char* up = u + (size_t)(idx * 64 + 56 - 32 * c) * 896 + (has_b ? xcd * 128 : 0);
        const unsigned uo = (7 - (lane >> 3)) * 896, k4 = (lane & 7) ^ (4 * c);
        glds<0>(uo + (k4 << 4), up, L_UIMG + c * 4096); glds<1024>(uo + ((k4 ^ 1) << 4), up - 8 * 896, L_UIMG + c * 4096);
        glds<2048>(uo + ((k4 ^ 2) << 4), up - 16 * 896, L_UIMG + c * 4096); glds<3072>(uo + ((k4 ^ 3) << 4), up - 24 * 896, L_UIMG + c * 4096);
      }
      X_PIECES(xb + kc * 128, L_RING + c * 8192);
#pragma unroll
      for (int i = 0; i < 6; ++i) {
        const unsigned p = (6 * c + i) * 64 + lane, row = (p * 43691u) >> 19; unsigned e = p - row * 12; e = e > 10 ? 10 : e;
        wo_[i] = row * 2688 + e * 16;
      }
      W_PIECES(wq + kc * 64 * 2688, L_RING + XCH_B + c * 6144);
      asm volatile("s_waitcnt vmcnt(14)" : "+v"(epoch) :: "memory");
      lds_arrive_nw(C_UI);
      tag = MAKE_TAG(epoch); asm volatile("" :: "s"(tag)); lds_arrive_nw(C_BTAG);
#pragma unroll 1
      for (int n = 1; n < 7; ++n) {
        if (n >= 4) lds_wait_ge(C_R + 4 * (n - 4), 4);
        const unsigned slot = (n & 3) * SLOT_B;
        kc = kc == 6 ? 0 : kc + 1;
        X_PIECES(xb + kc * 128, L_RING + c * 8192 + slot);
        W_PIECES(wq + kc * 64 * 2688, L_RING + XCH_B + c * 6144 + slot);
        asm volatile("s_waitcnt vmcnt(14)" ::: "memory"); lds_arrive_nw(C_L + 4 * (n - 1));
        if (n == 1) STAMP(1);
      }
      const unsigned pdt = *(const GAS unsigned short*)(logdt + d * 2);
      const unsigned pl = *(const GAS unsigned*)(logAr + 256 + po), pa = *(const GAS unsigned*)(Aim + 256 + po);
      asm volatile("s_waitcnt vmcnt(3)" ::: "memory");
      lds_arrive_nw(C_L + 24);
      __builtin_amdgcn_s_setprio(1);
      STAMP(12);
      u32x4 uv[4];
      if (has_b) {
        flag_wait(ws, WS_UFLAG + (xcd * 32 + r32) * 128, tag);
#pragma unroll
        for (int i = 0; i < 4; ++i) uv[i] = __builtin_amdgcn_raw_buffer_load_b128(wsr, i * 2048 + lane * 32 + c * 16, WS_UT + (xcd * 32 + idx) * 8192, AUX_SC1);
        STAMP(4);
        const float dt = bf_round(fexp(bf_lo(pdt)));
        unsigned lo[32];
        lo_vals(-bf_round(fexp(bf_lo(pl))) * dt * LOG2E, bf_lo(pa) * dt * 0.15915494309189535f, lo);
        lds_wait_ge(C_R + 12, 4);
#pragma unroll
        for (int j = 0; j < 32; ++j) lds_st<unsigned>(lo2 + tw + j * 16, lo[j]);
        lds_arrive(cw);
        STAMP(10);
      }
      gather_addr();
      if (has_b) {
        lds_wait_ge(C_R + 24, 4);
        const u32x4 z4 = u32x4{0u, 0u, 0u, 0u};
        if (lane < 56) lds_st<u32x4>(cb + 4 * KC_S + (lane >= 28 ? UC_S + 4096 - 28 * 16 : 4096) + lane * 16, z4);
        if (lane < 12) lds_st<u32x4>(cb + (lane & 3) * KC_S + (lane >> 2) * 16, z4);
        if (lane == 0) lds_st<u32x2>(cb + 4 * KC_S + UC_S + 4088, u32x2{0u, 0u});
#pragma unroll
        for (int i = 0; i < 4; ++i) {
          const unsigned a = cb + 4 * KC_S + (i * 64 + lane) * 16;
          lds_st<u32x4>(a, uv[i]); lds_st<u32x2>(a + UC_S - 8, u32x2{uv[i][0], uv[i][1]}); lds_st<u32x2>(a + UC_S, u32x2{uv[i][2], uv[i][3]});
        }
        STAMP(11);
        lds_wait_ge(cw + 4, 1);
      }
    } else {
      unsigned pdt = *(const GAS unsigned short*)(logdt + d * 2), p0[4], p1[4];
      LOAD_P(p0, 0); LOAD_P(p1, 256);
      const u64 epoch = __hip_atomic_load((GAS u64*)(ws + WS_EPOCH), __ATOMIC_RELAXED, __HIP_MEMORY_SCOPE_AGENT);
      gather_addr();
      asm volatile("" : "+v"(pdt), "+v"(p0[0]), "+v"(p0[2]), "+v"(p0[3]), "+v"(p1[2]), "+v"(p1[3]));
      tag = MAKE_TAG(epoch); asm volatile("" :: "s"(tag)); lds_arrive_nw(C_BTAG);
      __builtin_amdgcn_s_setprio(1);
      STAMP(9);
      if (has_b) {
        const float dt = bf_round(fexp(bf_lo(pdt)));
        unsigned cm = 0;
#pragma unroll
        for (int i = 2; i < 4; ++i) cm = umax(cm, umax(umax(p0[i] << 16 & 0x7fff0000u, p0[i] & 0x7fff0000u), umax(p1[i] << 16 & 0x7fff0000u, p1[i] & 0x7fff0000u)));
        unsigned eb = wave_umax(cm) >> 23; eb = eb < 1u ? 1u : (eb > 253u ? 253u : eb);
        const float csc = u2f((254u - eb) << 23), ks = u2f(eb << 23) * 2.f * dt;
        const unsigned tb_ = 4 * SLOT_B + c * TAB_W;
        const unsigned lw = tb_ + tw, hw = tb_ + TAB_HI + (lane >> 3) * 672 + ((lane >> 2) & 1) * 336 + (lane & 3) * 4;
        const unsigned fr = hh * 528 + r32 * 16, hr = tb_ + TAB_HI + hh * 336 + r32 * 16;
        f32x16 kd = f32x16{};
        unsigned lo[3][32], hi[4][21];
        auto vals = [&](auto rc) __attribute__((always_inline)) NOPK {
          constexpr int r = decltype(rc)::value;
          const unsigned (&P)[4] = r < 2 ? p0 : p1;
#define HALF(v) ((r == 0 || r == 2) ? bf_hi(v) : bf_lo(v))
          const float rho = -bf_round(fexp(HALF(P[0]))) * dt * LOG2E, th = HALF(P[1]) * dt * 0.15915494309189535f;
          if constexpr (r < 3) {
            lo_vals(rho, th, lo[r]);
#pragma unroll
            for (int j = 1; j < 32; ++j) PIN(lo[r][j]);
          }
          hi_vals(rho, th, f32x2{HALF(P[2]) * csc, HALF(P[3]) * -csc}, hi[r]);
#pragma unroll
          for (int i = 0; i < 21; ++i) PIN(hi[r][i]);
          SBAR();
        };
        auto mma = [&](auto rc) __attribute__((always_inline)) NOPK {
          constexpr int r = decltype(rc)::value;
          if constexpr (r < 3) {
#pragma unroll
            for (int j = 0; j < 32; ++j) lds_st<unsigned>(lw + j * 16, lo[r][j]);
          }
#pragma unroll
          for (int i = 0; i < 21; ++i) lds_st<unsigned>(hw + i * 16, hi[r][i]);
          if constexpr (r == 3) lds_wait_ge(cw, 1);
          h16x8 fa[8], fb[8];
#pragma unroll
          for (int s = 0; s < 8; ++s) { fa[s] = lds_ld<h16x8>(hr + s * 672); fb[s] = lds_ld<h16x8>((r < 3 ? tb_ : lo2) + fr + s * 1056); }
          SBAR();
#pragma unroll
          for (int s = 0; s < 8; ++s) kd = MFMA32H(fa[s], fb[s], kd);
        };
        vals(IC<0>{}); mma(IC<0>{}); vals(IC<1>{});
        if (c == 0) {
          lds_wait_ge(C_R + 8, 4);
          if (lane == 0) flag_store(ws, WS_UFLAG + (xcd * 32 + idx) * 128, tag);
        }
        vals(IC<2>{}); vals(IC<3>{});
        STAMP(4);
        mma(IC<1>{}); mma(IC<2>{}); mma(IC<3>{});
        STAMP(10);
        const unsigned i1 = T + (hh ? 384 + 2 * r32 : 256 + 4 * r32) * 2;
        const unsigned i2 = T + (hh ? (r32 == 0 ? 640 : 643) : 512 + r32) * 2, i3 = T + (hh ? 641 : 512 + r32) * 2, st3 = hh ? 0 : 64;
        lds_st<u32x2>(T + (8 * r32 + 4 * hh) * 2, u32x2{pk_bf16(kd[0] * ks, kd[1] * ks), pk_bf16(kd[2] * ks, kd[3] * ks)});
        lds_st<unsigned>(i1, pk_bf16(kd[4] * ks, kd[5] * ks)); lds_st<unsigned>(i1 + (hh ? 128 : 4), pk_bf16(kd[6] * ks, kd[7] * ks));
        const unsigned w0 = pk_bf16(kd[8] * ks, kd[9] * ks), w1 = hh ? 0u : pk_bf16(kd[10] * ks, kd[11] * ks);
        lds_st<short>(i2, (short)w0); lds_st<short>(i3 + st3, hh ? (short)0 : (short)(w0 >> 16)); lds_st<short>(i3 + 2 * st3, (short)w1); lds_st<short>(i3 + 3 * st3, (short)(w1 >> 16));
        lds_arrive(cw + 4);
        lds_wait_ge(C_R + 24, 4);
      }
    }
    if (has_b) {
      STAMP(2);
      {
        s16x2 P[2][6];
#pragma unroll
        for (int it = 0; it < 2; ++it)
#pragma unroll
          for (int k = 0; k < 11; ++k) P[it][k >> 1][k & 1] = lds_ld<short>(ga[it][k]);
#pragma unroll
        for (int it = 0; it < 2; ++it) {
          unsigned Q[6], A[5];
#pragma unroll
          for (int k = 0; k < 6; ++k) Q[k] = __builtin_bit_cast(unsigned, P[it][k]);
#pragma unroll
          for (int k = 0; k < 5; ++k) A[k] = __builtin_amdgcn_alignbit(Q[k + 1], Q[k], 16);
          const unsigned a = cb + (3 + it * 128 + half * 64 + lane) * 16;
          lds_st<u32x4>(a, u32x4{Q[0], Q[1], Q[2], Q[3]}); lds_st<u32x4>(a + KC_S, u32x4{A[0], A[1], A[2], A[3]});
          lds_st<u32x4>(a + 2 * KC_S, u32x4{Q[1], Q[2], Q[3], Q[4]}); lds_st<u32x4>(a + 3 * KC_S, u32x4{A[1], A[2], A[3], A[4]});
        }
      }
      lds_arrive(cw + 8); lds_wait_ge(cw + 8, 2);
      __builtin_amdgcn_s_setprio(BCONV);
      STAMP(5);
      f32x4 y[4];
#pragma unroll
      for (int i = 0; i < 4; ++i) y[i] = f32x4{0.f, 0.f, 0.f, 0.f};
      const int cc = lane & 15, kq = lane >> 4;
      {
        unsigned ka = cb + (cc & 3) * KC_S + ((cc >> 2) + kq) * 16;
        unsigned ua = cb + 4 * KC_S + (1 - (cc & 1)) * UC_S + (2016 - 32 * (cc >> 1) + 8 * kq) * 2 - 7 * 512;
        PIN(ka); PIN(ua);
        auto conv = [&](auto hc) __attribute__((always_inline)) NOPK {
          constexpr int H = decltype(hc)::value, NF = H ? 6 : 8;
          s16x8 af[2][NF], bf[2][NF];
#pragma unroll
          for (int m = 0; m < NF; ++m) { bf[0][m] = lds_ld<s16x8>(ua + (7 - m) * 512); af[0][m] = lds_ld<s16x8>(ka + m * 512); }
          auto conv_class = [&](auto sc, auto oc) __attribute__((always_inline)) NOPK {
            constexpr int S = decltype(sc)::value, O = decltype(oc)::value;
            int k = 0;
#pragma unroll
            for (int m = 0; m < NF; ++m)
#pragma unroll
              for (int i = 0; i < 4; ++i) {
                const int n = (H ? 2 + i : (i < 2 ? i : 4 + i)) - m;
                if (n >= 0) {
                  y[i] = MFMA16(af[S][m], bf[S][n], y[i]);
                  if (k < 2 * NF) {
                    constexpr int o0[16] = {0, 8, 9, 14, 15, 1, 13, 2, 12, 3, 11, 4, 10, 5, 6, 7}, o1[16] = {0, 10, 11, 12, 13, 1, 9, 2, 8, 3, 4, 5, 0, 0, 0, 0};
                    const int q = H ? o1[k] : o0[k];
                    if (q < 8) af[1 - S][q] = lds_ld<s16x8>(ka + O + q * 512); else bf[1 - S][q - 8] = lds_ld<s16x8>(ua + O + (15 - q) * 512);
                  }
                  SBAR();
                  ++k;
                }
              }
          };
#pragma unroll 1
          for (int it = 0; it < 4; ++it) {
            conv_class(IC<0>{}, IC<64>{});
            conv_class(IC<1>{}, IC<128>{});
            ka += 128; ua += 128;
          }
        };
        if (half) conv(IC<1>{}); else conv(IC<0>{});
      }
      PIN(y[3]); STAMP(13);
      __builtin_amdgcn_s_setprio(2);
#pragma unroll
      for (int i = 0; i < 4; ++i) {
        const int t64 = 4 * (half ? 2 + i : (i < 2 ? i : 4 + i)) + (cc >> 2);
        __builtin_amdgcn_raw_buffer_store_b64(u32x2{pk_bf16(y[i][0], y[i][1]), pk_bf16(y[i][2], y[i][3])}, wsr,
                                              ((xcd * 32 + t64) * 64 + idx * 2 + c) * 128 + (32 * ((cc >> 1) & 1) + 8 * kq + 4 * (cc & 1)) * 2, WS_YT, AUX_SC1);
      }
      lds_arrive(C_BFREE);
      __builtin_amdgcn_s_setprio(0);
      STAMP(15);
    } else {
      lds_arrive(C_BFREE);
      __builtin_amdgcn_s_setprio(0);
    }
    lds_wait_ge_slow(C_QR, 4);
    lds_wait_ge_slow(C_BFREE, 4);
#pragma unroll 1
    for (int i = wb; i < 14; i += 4) {
      const char* sb = wo + i * 32 * 896 + (idx & 3) * 224;
      const unsigned dst = L_RING + i * 7168;
#define WO_PIECE(k) glds<(k) * 1024>(wv[k], sb, dst); asm volatile("s_waitcnt vmcnt(2)" ::: "memory")
      WO_PIECE(0); WO_PIECE(1); WO_PIECE(2);
      if (i < 4 && has_b) {
        if (last_of_four(C_BPUB, lane, 0)) { if (lane == 0) flag_store(ws, WS_YFLAG + (xcd * 32 + idx) * 128, tag); }
        STAMP(6);
      }
      WO_PIECE(3); WO_PIECE(4); WO_PIECE(5); WO_PIECE(6);
    }
    drain_vm();
    lds_arrive(C_WO);
    STAMP(7);
    if (has_b) {
      const int tl = wb * 16 + (lane & 15);
      u32x4 uu[2], ds[2];
#pragma unroll
      for (int i = 0; i < 2; ++i) {
        const int oct = (lane >> 4) + 4 * i;
        uu[i] = *(const GAS u32x4*)(u + (size_t)(idx * 64 * 896 + xcd * 128) + (unsigned)(tl * 896 + oct * 16));
        ds[i] = *(const GAS u32x4*)(Dsk + xcd * 128 + (unsigned)(oct * 16));
      }
      if (wb == 0) flag_wait(ws, WS_YFLAG + (xcd * 32 + r32) * 128, tag);
      role_sync_idle(C_B, 1);
      u32x4 v[2];
#pragma unroll
      for (int i = 0; i < 2; ++i) v[i] = __builtin_amdgcn_raw_buffer_load_b128(wsr, wb * 2048 + i * 1024 + lane * 16, WS_YT + (xcd * 32 + idx) * 8192, AUX_SC1);
#pragma unroll
      for (int i = 0; i < 2; ++i) lds_st<u32x4>(L_YIMG + wb * 2048 + i * 1024 + lane * 16, v[i]);
      role_sync_idle(C_B, 2);
#pragma unroll
      for (int i = 0; i < 2; ++i) {
        const int oct = (lane >> 4) + 4 * i;
        const unsigned a = L_YIMG + (8 * oct + ((lane & 15) >> 2)) * 128 + (wb * 16 + (lane & 3) * 4) * 2;
        const u32x4 yy = __builtin_bit_cast(u32x4, cat8(tr_read(a), tr_read(a + 512)));
        u32x4 r;
#pragma unroll
        for (int k = 0; k < 4; ++k) {
          const unsigned sk = pk_bf16(bf_lo(uu[i][k]) * bf_lo(ds[i][k]), bf_hi(uu[i][k]) * bf_hi(ds[i][k]));
          r[k] = pk_bf16(bf_lo(yy[k]) + bf_lo(sk), bf_hi(yy[k]) + bf_hi(sk));
        }
        *(GAS u32x4*)(out + (size_t)(idx * 64 * 1792 + 896 + xcd * 128) + (unsigned)(tl * 1792 + oct * 16)) = r;
      }
      STAMP(8);
    } else if (b == 7 && wb == 0) {
#pragma unroll 1
      for (int i = 0; i < 4; ++i) rec_wait(wsr, WS_QFLAG + (i * 64 + lane) * 128, tag);
      if (lane == 0) flag_store(ws, WS_EPOCH, tag);
    }
    STAMP_FLUSH();
  }
}

extern "C" void kernel_launch(void* const* d_in, const int* in_sizes, int n_in, void* d_out, int out_size, void* d_ws, size_t ws_size,
                              hipStream_t stream) {
  (void)in_sizes; (void)n_in; (void)out_size; (void)ws_size;
  (void)hipFuncSetAttribute((const void*)fused_mha_s4d, hipFuncAttributeMaxDynamicSharedMemorySize, LDS_BYTES);
  fused_mha_s4d<<<256, 512, LDS_BYTES, stream>>>((const char*)d_in[0], (const char*)d_in[1], (const char*)d_in[2], (const char*)d_in[3],
                                                 (const char*)d_in[4], (const char*)d_in[5], (const char*)d_in[6], (const char*)d_in[7],
                                                 (const char*)d_in[8], (const char*)d_in[9], (char*)d_out,
                                                 (char*)d_ws + (WSEND ? ((ws_size - WS_END) & ~(size_t)0xfffff) : 0));
}
```

```cpp
#include <hip/hip_runtime.h>
#include <hip/hip_bf16.h>
#include <stdint.h>

typedef short s16x8 __attribute__((ext_vector_type(8)));
typedef short s16x4 __attribute__((ext_vector_type(4)));
typedef short s16x2 __attribute__((ext_vector_type(2)));
typedef _Float16 h16x8 __attribute__((ext_vector_type(8)));
typedef _Float16 h16x2 __attribute__((ext_vector_type(2)));
typedef __bf16 b16x2 __attribute__((ext_vector_type(2)));
typedef float f32x16 __attribute__((ext_vector_type(16)));
typedef float f32x4 __attribute__((ext_vector_type(4)));
typedef float f32x2 __attribute__((ext_vector_type(2)));
typedef unsigned u32x4 __attribute__((ext_vector_type(4)));
typedef unsigned u32x16 __attribute__((ext_vector_type(16)));
typedef unsigned u32x2 __attribute__((ext_vector_type(2)));
typedef unsigned long long u64;
#define LAS __attribute__((address_space(3)))
#define GAS __attribute__((address_space(1)))
#if defined(__HIP_DEVICE_COMPILE__)
#define NOPK __attribute__((target("no-packed-fp32-ops")))
#else
#define NOPK
#endif
#define DI NOPK __device__ __forceinline__
#define SBAR() __builtin_amdgcn_sched_barrier(0)
#define PIN(x) asm volatile("" : "+v"(x))
#define MFMA32(a, b, c) __builtin_amdgcn_mfma_f32_32x32x16_bf16((a), (b), (c), 0, 0, 0)
#define MFMA32H(a, b, c) __builtin_amdgcn_mfma_f32_32x32x16_f16((a), (b), (c), 0, 0, 0)
#define MFMA16(a, b, c) __builtin_amdgcn_mfma_f32_16x16x32_bf16((a), (b), (c), 0, 0, 0)
#define AUX_SC1 16

#define PREFETCH 1
#define WSEND 0
#define APRIO 3
#define BCONV 0
#define ROT 0
#define AT_ALIGN "5"
#define TWOPOLL 0
#define GATE 20
#define STAMPS 0

constexpr unsigned WS_EPOCH = 0;
constexpr unsigned WS_QFLAG = 4096;
constexpr unsigned WS_OFLAG = WS_QFLAG + 256 * 128;
constexpr unsigned WS_UFLAG = WS_OFLAG + 1024 * 128;
constexpr unsigned WS_YFLAG = WS_UFLAG + 256 * 128;
constexpr unsigned WS_QKV = 393216;
constexpr unsigned WS_KV = WS_QKV + 16 * 64 * 2048;
constexpr unsigned WS_O = WS_QKV + 48 * 64 * 2048;
constexpr unsigned WS_UT = WS_O + 64 * 16 * 2048;
constexpr unsigned WS_YT = WS_UT + 256 * 8192;
constexpr unsigned WS_DBG = WS_YT + 224 * 8192;
constexpr unsigned WS_END = WS_DBG + 256 * 8 * 16 * 8;
static_assert(WS_YFLAG + 256 * 128 <= WS_QKV, "ws map");

constexpr unsigned L_RING = 0, SLOT_B = 28672, XCH_B = 16384;
constexpr unsigned L_B = 100352;
constexpr unsigned TAB_HI = 16896, TAB_W = 28000;
constexpr unsigned L_UIMG = 4 * SLOT_B + TAB_W;
constexpr unsigned L_T = 156352, T_B = 1296;
constexpr unsigned L_CV = 8192;
constexpr unsigned KC_S = 4160, UC_S = 4640, CH_B = 4 * KC_S + 2 * UC_S;
constexpr unsigned L_YIMG = L_B + 49152;
constexpr unsigned L_CTL = 163584;
constexpr unsigned LDS_BYTES = 163840;
constexpr unsigned C_A = L_CTL, C_B = L_CTL + 4, C_GO = L_CTL + 8, C_APUB = L_CTL + 12, C_BPUB = L_CTL + 16, C_WO = L_CTL + 20, C_BFREE = L_CTL + 24, C_BTAG = L_CTL + 28, C_U = L_CTL + 32, C_Q = L_CTL + 36, C_QR = L_CTL + 40, C_FB = L_CTL + 44, C_KM = L_CTL + 48, C_UI = L_CTL + 60, C_CH = L_CTL + 128;
constexpr unsigned C_L = L_CTL + 64, C_R = L_CTL + 96;
constexpr unsigned L_MAX = 162816;

constexpr unsigned L_SINK = L_MAX + 512;
constexpr unsigned SPIN_CAP = 1u << 22;
constexpr float LOG2E = 1.4426950408889634f;
constexpr float QSCALE = 0.18898223650461362f * LOG2E;
constexpr float SAFE_BOUND = 48.0f;

template <int V> struct IC { static constexpr int value = V; };
DI unsigned f2u(float f) { return __builtin_bit_cast(unsigned, f); }
DI float u2f(unsigned u) { return __builtin_bit_cast(float, u); }
DI unsigned umax(unsigned a, unsigned b) { return a > b ? a : b; }
DI unsigned sel(unsigned a, unsigned b, u64 mask) { return __builtin_amdgcn_inverse_ballot_w64(mask) ? b : a; }
DI float fexp(float x) { return __builtin_amdgcn_exp2f(x * LOG2E); }
DI float bf_lo(unsigned v) { return u2f(v << 16); }
DI float bf_hi(unsigned v) { return u2f(v & 0xffff0000u); }
DI unsigned pk_bf16(float a, float b) { f32x2 v = {a, b}; return __builtin_bit_cast(unsigned, __builtin_convertvector(v, b16x2)); }
DI unsigned pk_f16(float a, float b) { f32x2 v = {a, b}; return __builtin_bit_cast(unsigned, __builtin_convertvector(v, h16x2)); }
DI float bf_round(float a) { return bf_lo(pk_bf16(a, a)); }
template <int OFF> DI void glds(unsigned voff, const char* sbase, unsigned lds) {
  asm volatile("s_add_u32 m0, %2, %3\n\ts_nop 0\n\tglobal_load_lds_dwordx4 %0, %1" :: "v"(voff), "s"(sbase), "s"(lds), "i"(OFF) : "memory", "scc");
}
DI float half_swap_add(float v) {
  auto r = __builtin_amdgcn_permlane32_swap(f2u(v), f2u(v), false, false);
  return u2f(r[0]) + u2f(r[1]);
}
template <int OFF> DI void glds_dev(unsigned voff, const char* sbase, unsigned lds) {
  asm volatile("s_add_u32 m0, %2, %3\n\ts_nop 0\n\tglobal_load_lds_dwordx4 %0, %1 sc1" :: "v"(voff), "s"(sbase), "s"(lds), "i"(OFF) : "memory", "scc");
}
DI float dot2(unsigned a, float c) { return __builtin_amdgcn_fdot2_f32_bf16(__builtin_bit_cast(b16x2, a), __builtin_bit_cast(b16x2, a), c, false); }
DI float frag_norm2(u32x4 f0, u32x4 f1, int hh) {
  float a = dot2(f0[0], 0.f), b = dot2(f0[1], 0.f), c = dot2(f1[2], 0.f);
  a = dot2(f0[2], a); b = dot2(f0[3], b); c = dot2(f1[3], c); a = dot2(f1[0], a); b = dot2(f1[1], b);
  return half_swap_add(a + b + (hh ? 0.f : c));
}
DI s16x4 tr_read(unsigned a) { return __builtin_bit_cast(s16x4, __builtin_amdgcn_ds_read_tr16_b64_v4i16((LAS s16x4*)(uintptr_t)a)); }
DI s16x8 cat8(s16x4 lo, s16x4 hi) { return __builtin_shufflevector(lo, hi, 0, 1, 2, 3, 4, 5, 6, 7); }
template <class T> DI T lds_ld(unsigned a) { return *(LAS T*)(uintptr_t)a; }
template <class T> DI void lds_st(unsigned a, T v) { *(LAS T*)(uintptr_t)a = v; }
#define LDS_POLL(V, A, S, N, T, EXTRA) "s_mov_b32 " N ", 0\n1:\n\t" EXTRA "ds_read_b32 " V ", " A "\n\ts_add_u32 " N ", " N ", 1024\n\t" \
  "s_waitcnt lgkmcnt(0)\n\tv_readfirstlane_b32 " S ", " V "\n\ts_cbranch_scc1 4f\n\ts_cmp_lt_u32 " S ", " T "\n\ts_cbranch_scc1 1b\n4:\n\t"
DI unsigned lds_wait_ge(unsigned a, unsigned target) {
  unsigned v, sv, n;
  asm volatile(LDS_POLL("%0", "%3", "%1", "%2", "%4", "") : "=&v"(v), "=&s"(sv), "=&s"(n) : "v"(a), "s"(target) : "memory", "scc");
  return sv;
}
DI unsigned lds_wait_ge_slow(unsigned a, unsigned target) {
  unsigned v, sv, n;
  asm volatile(LDS_POLL("%0", "%3", "%1", "%2", "%4", "s_sleep 1\n\t") : "=&v"(v), "=&s"(sv), "=&s"(n) : "v"(a), "s"(target) : "memory", "scc");
  return sv;
}
DI void lds_arrive(unsigned a) {
  asm volatile("s_waitcnt lgkmcnt(0)\n\ts_mov_b64 exec, 1\n\tds_add_u32 %0, %1\n\ts_mov_b64 exec, -1" :: "v"(a), "v"(1u) : "memory");
}
DI void lds_arrive_nw(unsigned a) {
  asm volatile("s_mov_b64 exec, 1\n\tds_add_u32 %0, %1\n\ts_mov_b64 exec, -1" :: "v"(a), "v"(1u) : "memory");
}
DI void role_sync(unsigned a, unsigned nth) { lds_arrive(a); lds_wait_ge(a, nth * 4u); }
DI void role_sync_slow(unsigned a, unsigned nth) { lds_arrive(a); lds_wait_ge_slow(a, nth * 4u); }
DI void role_sync_idle(unsigned a, unsigned nth) {
  lds_arrive(a);
  unsigned v, sv, n;
  asm volatile(LDS_POLL("%0", "%3", "%1", "%2", "%4", "s_sleep 6\n\t") : "=&v"(v), "=&s"(sv), "=&s"(n) : "v"(a), "s"(nth * 4u) : "memory", "scc");
}
DI bool last_of_four(unsigned a, int lane, unsigned round) {
  unsigned old = 0;
  if (lane == 0) old = __hip_atomic_fetch_add((LAS unsigned*)(uintptr_t)a, 1u, __ATOMIC_RELAXED, __HIP_MEMORY_SCOPE_WORKGROUP);
  return (unsigned)__builtin_amdgcn_readfirstlane(old) == round * 4u + 3u;
}
DI void st128(u32x4 v, __amdgpu_buffer_rsrc_t r, unsigned voff, unsigned soff) {
  __builtin_amdgcn_raw_buffer_store_b128(v, r, voff, soff, AUX_SC1);
  asm volatile("s_nop 1" :: "v"(v) : "memory");
}
DI void drain_vm() { asm volatile("s_waitcnt vmcnt(0)" ::: "memory"); }
DI void flag_store(char* ws, unsigned off, u64 tag) { __hip_atomic_store((GAS u64*)(ws + off), tag, __ATOMIC_RELAXED, __HIP_MEMORY_SCOPE_AGENT); }
DI void flag_wait(char* ws, unsigned off, u64 tag) {
#pragma unroll 1
  for (unsigned n = 0; n < SPIN_CAP; ++n) {
    u64 v = __hip_atomic_load((GAS u64*)(ws + off), __ATOMIC_RELAXED, __HIP_MEMORY_SCOPE_AGENT);
    if (__builtin_amdgcn_ballot_w64(v != tag) == 0ull) break;
    __builtin_amdgcn_s_sleep(2);
  }
  asm volatile("" ::: "memory");
}
DI u32x4 rec_wait(__amdgpu_buffer_rsrc_t r, unsigned off, u64 tag) {
  u32x4 v;
#pragma unroll 1
  for (unsigned n = 0; n < SPIN_CAP; ++n) {
    v = __builtin_amdgcn_raw_buffer_load_b128(r, off, 0, AUX_SC1);
    if (__builtin_amdgcn_ballot_w64(v[0] != (unsigned)tag || v[2] != (unsigned)(tag >> 32)) == 0ull) break;
    asm volatile("s_sleep 1" ::: "memory");
  }
  asm volatile("" ::: "memory");
  return v;
}
template <int CTRL> DI unsigned dpp_max(unsigned v) { unsigned t = __builtin_amdgcn_update_dpp(0u, v, CTRL, 0xf, 0xf, false); return v > t ? v : t; }
DI unsigned wave_umax(unsigned v) {
  v = dpp_max<0x111>(v); v = dpp_max<0x112>(v); v = dpp_max<0x114>(v); v = dpp_max<0x118>(v);
  v = dpp_max<0x142>(v); v = dpp_max<0x143>(v);
  return __builtin_amdgcn_readlane(v, 63);
}
DI float half_swap_max(float v) {
  auto r = __builtin_amdgcn_permlane32_swap(f2u(v), f2u(v), false, false);
  return __builtin_fmaxf(u2f(r[0]), u2f(r[1]));
}
DI f32x2 cmul(f32x2 a, f32x2 b) { f32x2 r; r.x = __builtin_fmaf(-a.y, b.y, a.x * b.x); r.y = __builtin_fmaf(a.y, b.x, a.x * b.y); return r; }
DI void hmul3(unsigned a, unsigned b1, unsigned b2, unsigned b3, unsigned& r1, unsigned& r2, unsigned& r3) {
  asm("v_pk_mul_f16 %0, %3, %4 op_sel:[0,0] op_sel_hi:[0,1]\n\tv_pk_mul_f16 %1, %3, %5 op_sel:[0,0] op_sel_hi:[0,1]\n\tv_pk_mul_f16 %2, %3, %6 op_sel:[0,0] op_sel_hi:[0,1]\n\t"
      "v_pk_fma_f16 %0, %3, %4, %0 op_sel:[1,1,0] op_sel_hi:[1,0,1] neg_lo:[1,0,0]\n\tv_pk_fma_f16 %1, %3, %5, %1 op_sel:[1,1,0] op_sel_hi:[1,0,1] neg_lo:[1,0,0]\n\t"
      "v_pk_fma_f16 %2, %3, %6, %2 op_sel:[1,1,0] op_sel_hi:[1,0,1] neg_lo:[1,0,0]"
      : "=&v"(r1), "=&v"(r2), "=&v"(r3) : "v"(a), "v"(b1), "v"(b2), "v"(b3));
}
DI void hmul3x(unsigned a1, unsigned b1, unsigned a2, unsigned b2, unsigned a3, unsigned b3, unsigned& r1, unsigned& r2, unsigned& r3) {
  asm("v_pk_mul_f16 %0, %3, %4 op_sel:[0,0] op_sel_hi:[0,1]\n\tv_pk_mul_f16 %1, %5, %6 op_sel:[0,0] op_sel_hi:[0,1]\n\tv_pk_mul_f16 %2, %7, %8 op_sel:[0,0] op_sel_hi:[0,1]\n\t"
      "v_pk_fma_f16 %0, %3, %4, %0 op_sel:[1,1,0] op_sel_hi:[1,0,1] neg_lo:[1,0,0]\n\tv_pk_fma_f16 %1, %5, %6, %1 op_sel:[1,1,0] op_sel_hi:[1,0,1] neg_lo:[1,0,0]\n\t"
      "v_pk_fma_f16 %2, %7, %8, %2 op_sel:[1,1,0] op_sel_hi:[1,0,1] neg_lo:[1,0,0]"
      : "=&v"(r1), "=&v"(r2), "=&v"(r3) : "v"(a1), "v"(b1), "v"(a2), "v"(b2), "v"(a3), "v"(b3));
}
DI f32x2 zpow(float rho, float th, float p) {
  const float e = __builtin_amdgcn_exp2f(rho * p), f = __builtin_amdgcn_fractf(th * p);
  f32x2 r; r.x = e * __builtin_amdgcn_cosf(f); r.y = e * __builtin_amdgcn_sinf(f); return r;
}
#define STAMP(k) do { } while (0)
#define STAMP_FLUSH() do { } while (0)

#define QR(a, n) "v[" #a "+" #n ":" #a "+" #n "+1]"
#define Q_READ(F, XA, S) "ds_read_b128 v[" #F ":" #F "+3], " XA "\n\t" \
  "ds_read_b64_tr_b16 " QR(F, 4) ", %[wa] offset:" #S "*3072\n\tds_read_b64_tr_b16 " QR(F, 6) ", %[wa] offset:" #S "*3072+768\n\t" \
  "ds_read_b64_tr_b16 " QR(F, 8) ", %[wa] offset:" #S "*3072+56\n\tds_read_b64_tr_b16 " QR(F, 10) ", %[wa] offset:" #S "*3072+824\n\t" \
  "ds_read_b64_tr_b16 " QR(F, 12) ", %[wa] offset:" #S "*3072+112\n\tds_read_b64_tr_b16 " QR(F, 14) ", %[wa] offset:" #S "*3072+880\n\t"
#define Q_T(ACC, F, n) "v_mfma_f32_32x32x16_bf16 " ACC ", v[" #F "+" #n ":" #F "+" #n "+3], v[" #F ":" #F "+3], " ACC "\n\t"
#define Q_N(ACC, F, n) "v_mfma_f32_32x32x16_bf16 " ACC ", v[" #F ":" #F "+3], v[" #F "+" #n ":" #F "+" #n "+3], " ACC "\n\t"
#define Q_MMA(F, M0, M1, M2) M0("%[a0]", F, 4) M1("%[a1]", F, 8) M2("%[a2]", F, 12)
#define Q_ARRIVE "s_mov_b64 exec, 1\n\tds_add_u32 %[ca], %[one]\n\ts_mov_b64 exec, -1\n\t"
#define Q_TR(F, n, S, O) "ds_read_b64_tr_b16 " QR(F, n) ", %[wa] offset:" #S "*3072+" #O "\n\t"
#define Q_GROUP(G, F, XA, S, M0, M1, M2) \
  M0("%[a0]", G, 4) "ds_read_b128 v[" #F ":" #F "+3], " XA "\n\t" Q_TR(F, 4, S, 0) Q_TR(F, 6, S, 768) \
  M1("%[a1]", G, 8) Q_TR(F, 8, S, 56) Q_TR(F, 10, S, 824) \
  M2("%[a2]", G, 12) Q_TR(F, 12, S, 112) Q_TR(F, 14, S, 880)
#define QKV_BODY(M0, M1, M2) \
  "2:\n\t" \
  "v_add_u32 %[cl], 4, %[cl]\n\ts_waitcnt lgkmcnt(7)\n\t" \
  Q_GROUP(100, 132, "%[x2]", 2, M0, M1, M2) "ds_read_b32 %[tv], %[cl]\n\t" \
  "s_waitcnt lgkmcnt(8)\n\t" \
  Q_GROUP(116, 148, "%[x3]", 3, M0, M1, M2) \
  "s_cmp_eq_u32 %[tg], 28\n\ts_cbranch_scc1 5f\n\t" \
  "s_cmp_eq_u32 %[tg], 16\n\ts_cselect_b32 %[ts], %[wrap], %[step]\n\ts_add_u32 %[tg], %[tg], 4\n\t" \
  "v_add_u32 %[x0], %[ts], %[x0]\n\tv_add_u32 %[x1], %[ts], %[x1]\n\tv_add_u32 %[x2], %[ts], %[x2]\n\tv_add_u32 %[x3], %[ts], %[x3]\n\tv_add_u32 %[wa], %[ts], %[wa]\n\t" \
  "s_waitcnt lgkmcnt(7)\n\tv_readfirstlane_b32 %[ts], %[tv]\n\ts_cmp_ge_u32 %[ts], 2\n\ts_cbranch_scc1 0f\n\t" \
  Q_MMA(132, M0, M1, M2) \
  "s_cmp_lt_u32 %[tg], 16\n\ts_cbranch_scc1 3f\n\ts_waitcnt vmcnt(0)\n3:\n\t" \
  "s_waitcnt lgkmcnt(0)\n\t" Q_ARRIVE "s_nop 0\n\t" Q_MMA(148, M0, M1, M2) "v_add_u32 %[ca], 4, %[ca]\n\t" \
  LDS_POLL("%[tv]", "%[cl]", "%[ts]", "%[tn]", "2", "") \
  Q_READ(100, "%[x0]", 0) Q_READ(116, "%[x1]", 1) "s_branch 2b\n" \
  "0:\n\t" \
  Q_GROUP(132, 100, "%[x0]", 0, M0, M1, M2) \
  "s_cmp_lt_u32 %[tg], 16\n\ts_cbranch_scc1 3f\n\ts_waitcnt vmcnt(0)\n3:\n\t" \
  "s_waitcnt lgkmcnt(7)\n\t" Q_ARRIVE \
  Q_GROUP(148, 116, "%[x1]", 1, M0, M1, M2) \
  "v_add_u32 %[ca], 4, %[ca]\n\ts_branch 2b\n" \
  "5:\n\t" \
  "s_waitcnt lgkmcnt(7)\n\t" Q_MMA(132, M0, M1, M2) "s_waitcnt lgkmcnt(0)\n\t" Q_ARRIVE "s_nop 0\n\t" Q_MMA(148, M0, M1, M2)
#define QKV_LOOP() asm volatile( \
  "s_mov_b32 %[tg], 4\n\t" LDS_POLL("%[tv]", "%[cl]", "%[ts]", "%[tn]", "2", "") \
  Q_READ(100, "%[x0]", 0) Q_READ(116, "%[x1]", 1) \
  "s_cmp_gt_u32 %[g], 10\n\ts_cbranch_scc1 7f\n\ts_cmp_eq_u32 %[g], 10\n\ts_cbranch_scc1 6f\n" \
  QKV_BODY(Q_T, Q_T, Q_T) "s_branch 8f\n6:\n" QKV_BODY(Q_T, Q_T, Q_N) "s_branch 8f\n7:\n" QKV_BODY(Q_N, Q_N, Q_N) \
  "8:\n\ts_nop 7\n\ts_nop 7" \
  : [a0] "+v"(acc[0]), [a1] "+v"(acc[1]), [a2] "+v"(acc[2]), [x0] "+v"(xa0), [x1] "+v"(xa1), [x2] "+v"(xa2), [x3] "+v"(xa3), [wa] "+v"(wa), \
    [tv] "=&v"(tv), [ts] "=&s"(ts), [tn] "=&s"(tn), [tg] "=&s"(tg), [ca] "+v"(car), [cl] "+v"(cla) \
  : [one] "v"(1u), [step] "s"(SLOT_B), [wrap] "s"(0u - 3u * SLOT_B), [g] "s"(g) \
  : "memory", "scc", V100_163)
#define V10(p) "v" #p "0", "v" #p "1", "v" #p "2", "v" #p "3", "v" #p "4", "v" #p "5", "v" #p "6", "v" #p "7", "v" #p "8", "v" #p "9"
#define V100_163 V10(10), V10(11), V10(12), V10(13), V10(14), V10(15), "v160", "v161", "v162", "v163"

#define AT_M(D, A, B, C) "v_mfma_f32_32x32x16_bf16 v[" #D ":" #D "+15], v[" A ":" A "+3], v[" B ":" B "+3], " C "\n\t"
#define AT_ACC(D) "v[" #D ":" #D "+15]"
#define AT_E(C, i) "v_exp_f32 v[" #C "+" #i "], v[" #C "+" #i "]\n\t"
#define AT_P(P, C, i) "v_cvt_pk_bf16_f32 v[" #P "], v[" #C "+" #i "], v[" #C "+" #i "+1]\n\t"
#define AT_STEP(C, N, KN, QN, VP, OP, VC, OC, X) \
  AT_M(N, #KN, #QN, "0") AT_E(C, 0) AT_E(C, 1) AT_E(C, 2) AT_E(C, 3) AT_E(C, 4) \
  AT_M(OP, #VP "+12", "100", AT_ACC(OP)) X AT_E(C, 5) AT_E(C, 6) AT_E(C, 7) AT_P(96, C, 0) AT_P(97, C, 2) AT_P(98, C, 4) AT_P(99, C, 6) \
  AT_M(N, #KN "+4", #QN "+4", AT_ACC(N)) AT_E(C, 8) AT_E(C, 9) AT_E(C, 10) AT_E(C, 11) AT_E(C, 12) \
  AT_M(OC, #VC "+8", "96", AT_ACC(OC)) AT_E(C, 13) AT_E(C, 14) AT_E(C, 15) AT_P(100, C, 8) AT_P(101, C, 10) AT_P(102, C, 12) AT_P(103, C, 14)
#define AT_LD(B) "global_load_dwordx4 v[" #B ":" #B "+3], %[vo], s[40:41] sc1\n\tglobal_load_dwordx4 v[" #B "+4:" #B "+7], %[vo], s[40:41] offset:1024 sc1\n\t" \
  "global_load_dwordx4 v[" #B "+8:" #B "+11], %[vo], s[40:41] offset:2048 sc1\n\tglobal_load_dwordx4 v[" #B "+12:" #B "+15], %[vo], s[40:41] offset:3072 sc1\n\t" \
  "s_add_u32 %[kl], %[kl], 4096\n\ts_addc_u32 %[kh], %[kh], 0\n\t"
#define AT_RED(A, O) "ds_write_b128 " A ", v[" #O ":" #O "+3]\n\tds_write_b128 " A ", v[" #O "+4:" #O "+7] offset:1024\n\t" \
  "ds_write_b128 " A ", v[" #O "+8:" #O "+11] offset:2048\n\tds_write_b128 " A ", v[" #O "+12:" #O "+15] offset:3072\n\t"
#define AT_TILE(T, TP, TN, LD, W, R1, R2, R3) \
  AT_STEP(64, 80, T, 112, TP, 48, T, 0, "") LD AT_STEP(80, 64, T, 120, T, 0, T, 16, "") AT_STEP(64, 80, T, 128, T, 16, T, 32, R1) W \
  AT_STEP(80, 64, TN, 104, T, 32, T, 48, R2) R3
#define AT_W(n) "s_waitcnt vmcnt(" #n ")\n\t"

NOPK __global__ void __launch_bounds__(512, 2)
fused_mha_s4d(const char* __restrict__ x, const char* __restrict__ wqkv, const char* __restrict__ wo, const char* __restrict__ u,
              const char* __restrict__ logAr, const char* __restrict__ Aim, const char* __restrict__ Cre, const char* __restrict__ Cim,
              const char* __restrict__ logdt, const char* __restrict__ Dsk, char* __restrict__ out, char* __restrict__ ws) {
  extern __shared__ __attribute__((aligned(16))) char smem[];
  const int tid = __builtin_amdgcn_workitem_id_x(), lane = tid & 63;
  const int wid = __builtin_amdgcn_readfirstlane(tid >> 6);
  const int b = __builtin_amdgcn_workgroup_id_x(), xcd = b & 7, idx = b >> 3;
  const int r32 = lane & 31, hh = lane >> 5;
  const unsigned L0 = (unsigned)(uintptr_t)smem;
  (void)L0;
  if (wid >= 4) {
    u64 pc; unsigned len;
    asm volatile("s_getpc_b64 %0\n.Lpf_begin:\n\ts_mov_b32 %1, .Lpf_end-.Lpf_begin" : "=s"(pc), "=s"(len));
    const unsigned off = ((tid - 256) * 8 + (idx & 7)) * 128;
    if (off < len) asm volatile("s_mov_b32 m0, %2\n\ts_nop 0\n\tglobal_load_lds_dword %0, %1" :: "v"(off), "s"(pc), "s"(L_SINK) : "memory");
  }
  if (wid == 2) lds_st<u32x4>(L_MAX + lane * 16, u32x4{0u, 0u, 0u, 0u});
  asm volatile("s_waitcnt lgkmcnt(0)\n\ts_barrier" ::: "memory");
  const __amdgpu_buffer_rsrc_t wsr = __builtin_amdgcn_make_buffer_rsrc(ws, 0, WS_END, 0x00020000);
  asm volatile("" :: "s"(x), "s"(wqkv), "s"(wo), "s"(u), "s"(logAr), "s"(Aim), "s"(Cre), "s"(Cim), "s"(logdt), "s"(Dsk), "s"(out), "s"(ws));
  STAMP(0);
#define MAKE_TAG(e) (((u64)(unsigned)__builtin_amdgcn_readfirstlane((unsigned)((e) >> 32)) << 32 | (unsigned)__builtin_amdgcn_readfirstlane((unsigned)(e))) + 0x9E3779B97F4A7C15ull)
  const int rt = (xcd & 3) * 4 + (idx >> 3), g = (xcd >> 2) * 8 + (idx & 7);
  const int woff = (g == 15) ? 8 : 0;
  const bool has_b = xcd < 7;

  if (__builtin_expect(wid >= 4, 0)) {
    const int w = wid - 4;
    __builtin_amdgcn_s_setprio(APRIO);
    const u64 epoch = __hip_atomic_load((GAS u64*)(ws + WS_EPOCH), __ATOMIC_RELAXED, __HIP_MEMORY_SCOPE_AGENT);
    const int rt32 = rt * 4 + w;
    unsigned l16 = lane * 16, so[3], kma[3], vcst = r32 == 28 ? 0x3F803F80u : 0u; unsigned scl[3]; u64 vmask[3], pmask[3], qmask[3];
#pragma unroll
    for (int j = 0; j < 3; ++j) {
      const unsigned sl = 3 * g + j;
      unsigned mq = (unsigned)((int)(sl - 16) >> 31), mp = (unsigned)((int)(sl - 32) >> 31);
      asm volatile("" : "+s"(mq), "+s"(mp));
      const unsigned t64 = (sl & 15) * 64 + rt32;
      so[j] = ((WS_QKV + t64 * 2048) & mq) | ((WS_KV + t64 * 4096 + (2048u & ~mp)) & ~mq);
      scl[j] = (__builtin_bit_cast(unsigned, QSCALE) & mq) | (0x3F800000u & ~mq);
      const unsigned mv = 0xF0000000u & ~mp;
      vmask[j] = (u64)mv << 32 | mv; pmask[j] = (u64)mp << 32; qmask[j] = (u64)mq << 32;
      kma[j] = C_KM + j * 4;
      asm volatile("" : "+s"(so[j]), "+s"(scl[j]), "+s"(vmask[j]), "+s"(pmask[j]), "+s"(qmask[j]), "+v"(kma[j]));
    }
    PIN(l16); PIN(vcst);
    f32x16 acc[3];
#pragma unroll
    for (int j = 0; j < 3; ++j) acc[j] = f32x16{};
    {
      const unsigned xa_l = w * 4096 + r32 * 128, xsw = (r32 >> 1) & 7;
      unsigned xa0 = xa_l + (((0 + hh) ^ xsw) << 4), xa1 = xa_l + (((2 + hh) ^ xsw) << 4), xa2 = xa0 ^ 64, xa3 = xa1 ^ 64;
      unsigned wa = XCH_B + (8 * hh + ((lane & 15) >> 2)) * 192 + woff + ((lane >> 4) & 1) * 32 + (lane & 3) * 8;
      unsigned tv, ts, tn, tg, car = C_R, cla = C_L;
      asm volatile("" : "+v"(acc[0]), "+v"(acc[1]), "+v"(acc[2]), "+v"(xa0), "+v"(xa1), "+v"(xa2), "+v"(xa3), "+v"(wa), "+v"(car), "+v"(cla));
      if (has_b) {
        lds_wait_ge(C_UI, 2);
        const int oct = (lane >> 1) & 7;
        s16x8 v[2];
#pragma unroll
        for (int i = 0; i < 2; ++i) {
          const int ch = 2 * (w * 8 + i * 4 + (lane >> 4)) + (lane & 1);
          const unsigned a = L_UIMG + oct * 1024 + (((ch >> 3) ^ oct) << 4) + (ch & 7) * 2;
#pragma unroll
          for (int e = 0; e < 8; ++e) v[i][e] = lds_ld<short>(a + e * 128);
        }
#pragma unroll
        for (int i = 0; i < 2; ++i)
          st128(__builtin_bit_cast(u32x4, v[i]), wsr, ((xcd * 32 + w * 8 + i * 4 + (lane >> 4)) * 32 + (31 - idx)) * 256 + (lane & 15) * 16, WS_UT);
      }
      STAMP(9);
      QKV_LOOP();
    }
    STAMP(2);
#pragma unroll
    for (int j = 0; j < 3; ++j) {
      u32x4 f0, f1;
#pragma unroll
      for (int i = 0; i < 4; ++i) f0[i] = sel(pk_bf16(acc[j][2 * i] * u2f(scl[j]), acc[j][2 * i + 1] * u2f(scl[j])), vcst, vmask[j]);
      st128(f0, wsr, l16, so[j]);
#pragma unroll
      for (int i = 0; i < 4; ++i) f1[i] = sel(pk_bf16(acc[j][8 + 2 * i] * u2f(scl[j]), acc[j][9 + 2 * i] * u2f(scl[j])), vcst, vmask[j]);
      const unsigned n2 = f2u(frag_norm2(f0, f1, hh));
      f1[2] = sel(f1[2], sel(0x3F803F80u, n2, qmask[j]), pmask[j]); f1[3] = sel(f1[3], 0x00003F80u, pmask[j]);
      st128(f1, wsr, l16 + 1024, so[j]);
      const unsigned rm = dpp_max<0x118>(dpp_max<0x114>(dpp_max<0x112>(dpp_max<0x111>(n2))));
      asm volatile("s_mov_b64 exec, %2\n\tds_max_u32 %0, %1\n\ts_mov_b64 exec, -1" :: "v"(kma[j]), "v"(rm), "s"(0x80008000ull) : "memory");
    }
    const u64 tag = MAKE_TAG(epoch);
    STAMP(14);
    const int h = xcd * 2 + (idx >> 4), qt = idx & 15;
    f32x16 o[4];
#pragma unroll
    for (int i = 0; i < 4; ++i) { o[i] = f32x16{}; PIN(o[i]); }
    if (w == 3) lds_wait_ge(C_BTAG, 4);
    drain_vm();
    STAMP(15);
    if (w == 3) {
      lds_wait_ge(C_APUB, 3);
      const u32x4 km = lds_ld<u32x4>(C_KM);
      const u32x4 rec = {(unsigned)tag, ((km[0] + 0xffffu) >> 16) | ((km[1] + 0xffffu) & 0xffff0000u), (unsigned)(tag >> 32), km[2] + 0xffffu};
      if (lane == 0) st128(rec, wsr, (g * 16 + rt) * 128, WS_QFLAG);
    } else {
      lds_arrive_nw(C_APUB);
      if (w == 0) {
        rec_wait(wsr, WS_QFLAG + ((h / 3) * 16 + qt) * 128, tag);
        lds_wait_ge(C_R + 24, 4);
        const char* qs = ws + WS_QKV + (h * 64 + qt * 4) * 2048;
        glds_dev<0>(l16, qs, L_RING); glds_dev<1024>(l16, qs + 1024, L_RING); glds_dev<2048>(l16, qs + 2048, L_RING); glds_dev<3072>(l16, qs + 3072, L_RING);
        glds_dev<4096>(l16, qs + 4096, L_RING); glds_dev<5120>(l16, qs + 5120, L_RING); glds_dev<6144>(l16, qs + 6144, L_RING); glds_dev<7168>(l16, qs + 7168, L_RING);
        drain_vm();
        lds_arrive_nw(C_Q);
      } else if (w == 1 || TWOPOLL) {
        if (w == 2) __builtin_amdgcn_s_sleep(7);
        const u32x4 rec = rec_wait(wsr, WS_QFLAG + ((hh ? (32 + h) / 3 : (16 + h) / 3) * 16 + (lane & 15)) * 128, tag);
        const int j = (16 + h) % 3;
        unsigned v = j == 0 ? rec[1] << 16 : ((j == 1 ? rec[1] : rec[3]) & 0xffff0000u);
        v = dpp_max<0x111>(v); v = dpp_max<0x112>(v); v = dpp_max<0x114>(v); v = dpp_max<0x118>(v);
        lds_st<unsigned>(C_GO, __builtin_amdgcn_readlane(v, 15) + 1u);
      }
    }
    STAMP(3);
    s16x8 qf[4][2];
    const unsigned kbase = WS_KV + (h * 64 + w * 16) * 4096;
#define LDF(soff, voff) __builtin_bit_cast(s16x8, __builtin_amdgcn_raw_buffer_load_b128(wsr, l16 + (voff), (soff), AUX_SC1))
    const unsigned km2 = lds_wait_ge_slow(C_GO, 1);
    STAMP(4);
    u32x16 kv0, kv1, kv2;
    {
      const char* kb = ws + kbase;
      asm volatile("global_load_dwordx4 v[136:139], %3, %4 sc1\n\tglobal_load_dwordx4 v[140:143], %3, %4 offset:1024 sc1\n\tglobal_load_dwordx4 v[144:147], %3, %4 offset:2048 sc1\n\t"
                   "global_load_dwordx4 v[148:151], %3, %4 offset:3072 sc1\n\tglobal_load_dwordx4 v[152:155], %3, %5 sc1\n\tglobal_load_dwordx4 v[156:159], %3, %5 offset:1024 sc1\n\t"
                   "global_load_dwordx4 v[160:163], %3, %5 offset:2048 sc1\n\tglobal_load_dwordx4 v[164:167], %3, %5 offset:3072 sc1\n\tglobal_load_dwordx4 v[168:171], %3, %6 sc1\n\t"
                   "global_load_dwordx4 v[172:175], %3, %6 offset:1024 sc1\n\tglobal_load_dwordx4 v[176:179], %3, %6 offset:2048 sc1\n\tglobal_load_dwordx4 v[180:183], %3, %6 offset:3072 sc1"
                   : "=&{v[136:151]}"(kv0), "=&{v[152:167]}"(kv1), "=&{v[168:183]}"(kv2) : "v"(l16), "s"(kb), "s"(kb + 4096), "s"(kb + 8192) : "memory");
    }
    lds_wait_ge(C_Q, 1);
    STAMP(1);
#pragma unroll
    for (int i = 0; i < 4; ++i) { const unsigned a = L_RING + ((w + 1 + i) & 3) * 2048 + l16; qf[i][0] = lds_ld<s16x8>(a); qf[i][1] = lds_ld<s16x8>(a + 1024); }
    lds_arrive(C_QR);
    {
      const float kmax2 = u2f(km2) * 1.02f;
      unsigned pad2[4], pad3[4]; bool unsafe = false;
#pragma unroll
      for (int i = 0; i < 4; ++i) {
        const float m = __builtin_amdgcn_sqrtf(u2f(__builtin_bit_cast(u32x4, qf[i][1])[2]) * kmax2);
        unsafe |= hh && !(m <= SAFE_BOUND);
        pad2[i] = pk_bf16(-m, 0.f); pad3[i] = 0u;
      }
      if (__builtin_expect(__builtin_amdgcn_ballot_w64(unsafe) != 0ull, 0)) {
        float mx[4];
#pragma unroll
        for (int i = 0; i < 4; ++i) { mx[i] = -INFINITY; if (hh) { u32x4 t_ = __builtin_bit_cast(u32x4, qf[i][1]); t_[2] = 0u; t_[3] = 0u; qf[i][1] = __builtin_bit_cast(s16x8, t_); } }
#pragma unroll 1
        for (int t = 0; t < 16; ++t) {
          const s16x8 k0 = LDF(kbase + t * 4096, 0), k1 = LDF(kbase + t * 4096, 1024);
#pragma unroll
          for (int i = 0; i < 4; ++i) {
            f32x16 s = MFMA32(k0, qf[i][0], f32x16{}); s = MFMA32(k1, qf[i][1], s);
#pragma unroll
            for (int e = 0; e < 16; ++e) mx[i] = __builtin_fmaxf(mx[i], s[e]);
          }
        }
#pragma unroll
        for (int i = 0; i < 4; ++i) {
          const float v = half_swap_max(mx[i]);
          const unsigned k_ = f2u(v) ^ ((unsigned)((int)f2u(v) >> 31) | 0x80000000u);
          __hip_atomic_fetch_max((LAS unsigned*)(uintptr_t)(L_MAX + (((w + 1 + i) & 3) * 32 + r32) * 4), k_, __ATOMIC_RELAXED, __HIP_MEMORY_SCOPE_WORKGROUP);
        }
        role_sync(C_FB, 1);
#pragma unroll
        for (int i = 0; i < 4; ++i) {
          const unsigned k_ = lds_ld<unsigned>(L_MAX + (((w + 1 + i) & 3) * 32 + r32) * 4);
          const float m = u2f(k_ ^ ((unsigned)((int)~k_ >> 31) | 0x80000000u));
          const float m0 = bf_round(-m), r1 = -m - m0, m1 = bf_round(r1);
          pad2[i] = pk_bf16(m0, m1); pad3[i] = pk_bf16(r1 - m1, 0.f);
        }
      }
#pragma unroll
      for (int i = 0; i < 4; ++i) {
        u32x4 t_ = __builtin_bit_cast(u32x4, qf[i][1]);
        t_[2] = hh ? pad2[i] : t_[2]; t_[3] = hh ? pad3[i] : t_[3]; qf[i][1] = __builtin_bit_cast(s16x8, t_);
      }
    }
    unsigned redw = L_B + l16; PIN(redw);
    __builtin_amdgcn_s_setprio(1);
    STAMP(5);
    {
      const u64 kn = (u64)(ws + kbase + 3 * 4096);
      unsigned kl = (unsigned)kn, kh = (unsigned)(kn >> 32), tv, ts, tn, it;
      asm volatile(
        "s_mov_b32 %[it], 3\n\tv_mov_b32 v100, 0\n\tv_mov_b32 v101, 0\n\tv_mov_b32 v102, 0\n\tv_mov_b32 v103, 0\n\t"
        "v_mov_b32 v196, 0\n\tv_mov_b32 v197, 0\n\tv_mov_b32 v198, 0\n\tv_mov_b32 v199, 0\n\t"
        AT_W(10) AT_M(64, "136", "104", "0") AT_M(64, "140", "108", AT_ACC(64)) AT_W(8) "s_nop 7\n\ts_nop 3\n\t.p2align " AT_ALIGN "\n"
        "2:\n\t"
        AT_TILE(136, 184, 152, AT_LD(184), AT_W(8), "", "", "") AT_TILE(152, 136, 168, AT_LD(136), AT_W(8), "", "", "")
        AT_TILE(168, 152, 184, AT_LD(152), AT_W(8), "", "", "") AT_TILE(184, 168, 136, AT_LD(168), AT_W(8), "", "", "s_sub_u32 %[it], %[it], 1\n\t")
        "s_cmp_lg_u32 %[it], 0\n\ts_cbranch_scc1 2b\n\t"
        AT_TILE(136, 184, 152, AT_LD(184), AT_W(8), "", "", "") AT_TILE(152, 136, 168, "", AT_W(4), "", "", "")
        AT_TILE(168, 152, 184, "", AT_W(0), "", "", "")
        LDS_POLL("%[tv]", "%[bf]", "%[ts]", "%[tn]", "4", "")
        AT_TILE(184, 168, 136, "", "", AT_RED("%[r0]", 0), AT_RED("%[r1]", 16), "")
        "s_nop 1\n\t" AT_M(48, "184+12", "100", AT_ACC(48)) AT_RED("%[r2]", 32) "s_nop 7"
        : "+{v[0:15]}"(o[0]), "+{v[16:31]}"(o[1]), "+{v[32:47]}"(o[2]), "+{v[48:63]}"(o[3]), "+{v[136:151]}"(kv0), "+{v[152:167]}"(kv1), "+{v[168:183]}"(kv2),
          [kl] "+{s40}"(kl), [kh] "+{s41}"(kh), [tv] "=&v"(tv), [ts] "=&s"(ts), [tn] "=&s"(tn), [it] "=&s"(it)
        : "{v[104:107]}"(qf[0][0]), "{v[108:111]}"(qf[0][1]), "{v[112:115]}"(qf[1][0]), "{v[116:119]}"(qf[1][1]), "{v[120:123]}"(qf[2][0]), "{v[124:127]}"(qf[2][1]),
          "{v[128:131]}"(qf[3][0]), "{v[132:135]}"(qf[3][1]), [vo] "v"(l16), [bf] "v"(C_BFREE),
          [r0] "v"(redw + (((w + 1) & 3) * 3 + 2) * 4096), [r1] "v"(redw + (((w + 2) & 3) * 3 + 1) * 4096), [r2] "v"(redw + (((w + 3) & 3) * 3) * 4096)
        : "memory", "scc", "v64", "v65", "v66", "v67", "v68", "v69", V10(7), V10(8), V10(9), "v100", "v101", "v102", "v103",
          "v184", "v185", "v186", "v187", "v188", "v189", V10(19));
    }
    STAMP(6);
    role_sync(C_A, 1);
    {
      f32x4 pv[12];
#pragma unroll
      for (int k = 0; k < 12; ++k) pv[k] = lds_ld<f32x4>(redw + w * 12288 + k * 1024);
      SBAR();
      f32x16 a = o[3];
#pragma unroll
      for (int k = 0; k < 12; ++k) { const int q = k & 3; a[4 * q] += pv[k][0]; a[4 * q + 1] += pv[k][1]; a[4 * q + 2] += pv[k][2]; a[4 * q + 3] += pv[k][3]; }
      auto lr = __builtin_amdgcn_permlane32_swap(f2u(a[12]), f2u(a[12]), false, false);
      const float inv = __builtin_amdgcn_rcpf(u2f(lr[1]));
      u32x4 f0, f1;
#pragma unroll
      for (int i = 0; i < 4; ++i) { f0[i] = pk_bf16(a[2 * i] * inv, a[2 * i + 1] * inv); f1[i] = pk_bf16(a[8 + 2 * i] * inv, a[9 + 2 * i] * inv); }
      if (hh) { f1[2] = 0u; f1[3] = 0u; }
      const unsigned so = WS_O + ((qt * 4 + w) * 16 + h) * 2048;
      st128(f0, wsr, l16, so);
      st128(f1, wsr, l16 + 1024, so);
      lds_arrive(C_A);
      drain_vm();
      if (lane == 0) flag_store(ws, WS_OFLAG + ((qt * 4 + w) * 16 + h) * 128, tag);
    }
    STAMP(7);
    const int rs = xcd * 8 + (idx >> 2), cg = idx & 3, n0 = w < 3 ? 32 * w : 80;
    lds_wait_ge(C_A, 2 * 4);
    lds_wait_ge(C_WO, 4);
    flag_wait(ws, WS_OFLAG + (rs * 16 + w * 4 + (lane & 3)) * 128, tag);
    STAMP(8);
    {
      const char* os = ws + WS_O + (rs * 16 + w * 4) * 2048;
      const unsigned od = L_B + w * 8192;
      glds_dev<0>(l16, os, od); glds_dev<1024>(l16, os + 1024, od); glds_dev<2048>(l16, os + 2048, od); glds_dev<3072>(l16, os + 3072, od);
      glds_dev<4096>(l16, os + 4096, od); glds_dev<5120>(l16, os + 5120, od); glds_dev<6144>(l16, os + 6144, od); glds_dev<7168>(l16, os + 7168, od);
    }
    s16x8 wf[32];
    unsigned oa[4];
#pragma unroll
    for (int q = 0; q < 4; ++q) {
      const int gq = (w + q) & 3;
      unsigned wa = L_RING + (gq * 112 + 4 * hh + ((lane & 15) >> 2)) * 224 + n0 * 2 + ((lane >> 4) & 1) * 32 + (lane & 3) * 8;
      unsigned wb2 = wa + (hh ? 16 * 224 : 24 * 224);
      oa[q] = redw + gq * 8192;
      PIN(wa); PIN(wb2); PIN(oa[q]);
#pragma unroll
      for (int hd = 0; hd < 4; ++hd) {
        wf[q * 8 + hd * 2] = cat8(tr_read(wa + hd * 28 * 224), tr_read(wa + hd * 28 * 224 + 8 * 224));
        wf[q * 8 + hd * 2 + 1] = cat8(tr_read(wa + hd * 28 * 224 + 16 * 224), tr_read(wb2 + hd * 28 * 224));
      }
    }
    drain_vm();
    lds_arrive_nw(C_A);
    STAMP(13);
    f32x16 d = f32x16{};
    {
      s16x8 ob[8];
#pragma unroll
      for (int k = 0; k < 8; ++k) ob[k] = lds_ld<s16x8>(oa[0] + k * 1024);
      SBAR();
#pragma unroll
      for (int k = 0; k < 4; ++k) { d = MFMA32(wf[k], ob[k], d); SBAR(); }
      lds_wait_ge(C_A, 3 * 4);
#pragma unroll
      for (int k = 0; k < 4; ++k) ob[k] = lds_ld<s16x8>(oa[1] + k * 1024);
      SBAR();
#pragma unroll
      for (int k = 4; k < 32; ++k) {
        d = MFMA32(wf[k], ob[k & 7], d);
        if (k + 8 < 32) ob[k & 7] = lds_ld<s16x8>(oa[(k + 8) >> 3] + (k & 7) * 1024);
        SBAR();
      }
    }
    {
      char* op = out + (size_t)(rs * 32 * 1792 + (cg * 112 + n0) * 2) + (unsigned)(r32 * 1792 + hh * 16);
#pragma unroll
      for (int k = 0; k < 2; ++k) {
        const unsigned a0 = pk_bf16(d[8 * k], d[8 * k + 1]), a1 = pk_bf16(d[8 * k + 2], d[8 * k + 3]);
        const unsigned b0 = pk_bf16(d[8 * k + 4], d[8 * k + 5]), b1 = pk_bf16(d[8 * k + 6], d[8 * k + 7]);
        auto s0 = __builtin_amdgcn_permlane32_swap(a0, b0, false, false);
        auto s1 = __builtin_amdgcn_permlane32_swap(a1, b1, false, false);
        if (k || w < 3) *(GAS u32x4*)(op + k * 32) = u32x4{s0[0], s1[0], s0[1], s1[1]};
      }
    }
    STAMP(10);
    STAMP_FLUSH();
    asm volatile(".Lpf_end:");
  } else {
    const int wb = wid ^ 2;
    const int c = wb & 1, half = wb >> 1;
    const int d = (has_b ? xcd * 64 : 0) + idx * 2 + c;
    const unsigned po = d * 512 + lane * 4;
    const unsigned cb = L_CV + c * CH_B, cw = C_CH + c * 16;
    const unsigned tb_ = (4 - c) * SLOT_B, lw = tb_ + (lane >> 2) * 1056 + ((lane >> 1) & 1) * 528 + (lane & 1) * 8;
    u64 tag;
#define LOAD_P(P, O) do { P[0] = *(const GAS unsigned*)(logAr + (O) + po); P[1] = *(const GAS unsigned*)(Aim + (O) + po); \
      P[2] = *(const GAS unsigned*)(Cre + (O) + po); P[3] = *(const GAS unsigned*)(Cim + (O) + po); } while (0)
    const unsigned T = L_T + c * T_B;
    unsigned ga[2][11];
    auto gather_addr = [&]() __attribute__((always_inline)) NOPK {
#pragma unroll
      for (int it = 0; it < 2; ++it) {
        const int l0 = 8 * (3 + it * 128 + half * 64 + lane) - 27;
        const float f0 = (float)l0;
#pragma unroll
        for (int k = 0; k < 11; k += 2) {
          const unsigned pk = pk_bf16(f0 + (float)k, f0 + (float)(k + 1));
          asm volatile("v_mad_u32_u16 %0, %1, 2, %2" : "=v"(ga[it][k]) : "v"(pk), "s"(T - 2u * 17024u));
          if (k < 10) asm volatile("v_mad_u32_u16 %0, %1, 2, %2 op_sel:[1,0,0,0]" : "=v"(ga[it][k + 1]) : "v"(pk), "s"(T - 2u * 17024u));
        }
        if (it == 0 && !half) {
#pragma unroll
          for (int k = 0; k < 11; ++k) { const int l = l0 + k; ga[0][k] = l < 128 ? T + 2 * ((k < 3 && l < 0) ? 641 : l) : ga[0][k]; PIN(ga[0][k]); }
        }
      }
    };
    auto lo_vals = [&](float rho, float th, unsigned (&lo)[32]) __attribute__((always_inline)) NOPK {
      const f32x2 z8 = zpow(rho, th, 8.f), z16 = cmul(z8, z8), z24 = cmul(z8, z16), z32 = cmul(z16, z16);
      lo[0] = 0x00003C00u; lo[1] = pk_f16(z8.x, z8.y); lo[2] = pk_f16(z16.x, z16.y); lo[3] = pk_f16(z24.x, z24.y);
      f32x2 e = z32;
#pragma unroll
      for (int a = 1; a < 8; ++a) {
        lo[4 * a] = pk_f16(e.x, e.y);
        if (a < 7) e = cmul(e, z32);
        hmul3(lo[4 * a], lo[1], lo[2], lo[3], lo[4 * a + 1], lo[4 * a + 2], lo[4 * a + 3]);
      }
    };
    auto hi_vals = [&](float rho, float th, f32x2 hA, unsigned (&hi)[21]) __attribute__((always_inline)) NOPK {
      const f32x2 z1 = zpow(rho, -th, 1.f), z256 = zpow(rho, -th, 256.f);
#define F16(v) pk_f16((v).x, (v).y)
      const f32x2 z2 = cmul(z1, z1), z3 = cmul(z1, z2), z4 = cmul(z2, z2), w2 = cmul(z256, z256), w3 = cmul(z256, w2), w4 = cmul(w2, w2);
      const f32x2 h4 = cmul(hA, z4), g4 = cmul(hA, w4);
      const unsigned e1 = F16(z1), e2 = F16(z2), e3 = F16(z3), v1 = F16(z256), v2 = F16(w2), v3 = F16(w3), v4 = F16(w4);
      hi[0] = F16(hA); hi[4] = F16(h4); hi[16] = F16(g4);
      hmul3(hi[0], e1, e2, e3, hi[1], hi[2], hi[3]); hmul3(hi[4], e1, e2, e3, hi[5], hi[6], hi[7]);
      hmul3(hi[0], v1, v2, v3, hi[8], hi[12], hi[14]); hmul3(hi[4], v1, v2, v3, hi[10], hi[13], hi[15]);
      hmul3(hi[16], v1, v2, v3, hi[17], hi[18], hi[19]);
      hmul3x(v1, hi[2], v1, hi[6], v4, hi[16], hi[9], hi[11], hi[20]);
    };
    if (half) {
      unsigned xo[4], wo_[6];
#pragma unroll
      for (int q = 0; q < 4; ++q) xo[q] = (q * 8 + (lane >> 3)) * 896 + (((lane & 7) ^ ((q & 1) * 4 + (lane >> 4))) << 4);
      const char* xb = x + (size_t)(rt * 128 + c * 64) * 896;
      const char* wq = wqkv + g * 168 - woff;
      int kc = ROT ? ((idx & 7) + 2 * (idx >> 3)) % 7 : 0;
      u64 epoch;
      asm volatile("global_load_dwordx2 %0, %1, %2 sc1" : "=v"(epoch) : "v"(0u), "s"(ws + WS_EPOCH) : "memory");
#define X_PIECES(B, D) do { glds<0>(xo[0], B, D); glds<1024>(xo[1], B, D); glds<2048>(xo[2], B, D); glds<3072>(xo[3], B, D); \
        glds<4096>(xo[0], (B) + 32 * 896, D); glds<5120>(xo[1], (B) + 32 * 896, D); glds<6144>(xo[2], (B) + 32 * 896, D); glds<7168>(xo[3], (B) + 32 * 896, D); } while (0)
#define W_PIECES(B, D) do { glds<0>(wo_[0], B, D); glds<1024>(wo_[1], B, D); glds<2048>(wo_[2], B, D); glds<3072>(wo_[3], B, D); glds<4096>(wo_[4], B, D); glds<5120>(wo_[5], B, D); } while (0)
      {
        const char* up = u + (size_t)(idx * 64 + 56 - 32 * c) * 896 + (has_b ? xcd * 128 : 0);
        const unsigned uo = (7 - (lane >> 3)) * 896, k4 = (lane & 7) ^ (4 * c);
        glds<0>(uo + (k4 << 4), up, L_UIMG + c * 4096); glds<1024>(uo + ((k4 ^ 1) << 4), up - 8 * 896, L_UIMG + c * 4096);
        glds<2048>(uo + ((k4 ^ 2) << 4), up - 16 * 896, L_UIMG + c * 4096); glds<3072>(uo + ((k4 ^ 3) << 4), up - 24 * 896, L_UIMG + c * 4096);
      }
      X_PIECES(xb + kc * 128, L_RING + c * 8192);
#pragma unroll
      for (int i = 0; i < 6; ++i) {
        const unsigned p = (6 * c + i) * 64 + lane, row = (p * 43691u) >> 19; unsigned e = p - row * 12; e = e > 10 ? 10 : e;
        wo_[i] = row * 2688 + e * 16;
      }
      W_PIECES(wq + kc * 64 * 2688, L_RING + XCH_B + c * 6144);
      asm volatile("s_waitcnt vmcnt(14)" : "+v"(epoch) :: "memory");
      lds_arrive_nw(C_UI);
      tag = MAKE_TAG(epoch); asm volatile("" :: "s"(tag)); lds_arrive_nw(C_BTAG);
#pragma unroll 1
      for (int n = 1; n < 7; ++n) {
        if (n >= 4) lds_wait_ge(C_R + 4 * (n - 4), 4);
        const unsigned slot = (n & 3) * SLOT_B;
        kc = kc == 6 ? 0 : kc + 1;
        X_PIECES(xb + kc * 128, L_RING + c * 8192 + slot);
        W_PIECES(wq + kc * 64 * 2688, L_RING + XCH_B + c * 6144 + slot);
        asm volatile("s_waitcnt vmcnt(14)" ::: "memory"); lds_arrive_nw(C_L + 4 * (n - 1));
        if (n == 1) STAMP(1);
      }
      const unsigned pdt = *(const GAS unsigned short*)(logdt + d * 2);
      const unsigned pl = *(const GAS unsigned*)(logAr + 256 + po), pa = *(const GAS unsigned*)(Aim + 256 + po);
      asm volatile("s_waitcnt vmcnt(3)" ::: "memory");
      lds_arrive_nw(C_L + 24);
      __builtin_amdgcn_s_setprio(1);
      STAMP(12);
      u32x4 uv[4];
      if (has_b) {
        const float dt = bf_round(fexp(bf_lo(pdt)));
        unsigned lo[32];
        lo_vals(-bf_round(fexp(bf_lo(pl))) * dt * LOG2E, bf_lo(pa) * dt * 0.15915494309189535f, lo);
        SBAR();
        const u64 uflag = __hip_atomic_load((GAS u64*)(ws + WS_UFLAG + (xcd * 32 + r32) * 128), __ATOMIC_RELAXED, __HIP_MEMORY_SCOPE_AGENT);
#pragma unroll
        for (int j = 1; j < 32; ++j) PIN(lo[j]);
        if (__builtin_amdgcn_ballot_w64(uflag != tag) != 0ull) flag_wait(ws, WS_UFLAG + (xcd * 32 + r32) * 128, tag);
        asm volatile("" ::: "memory");
#pragma unroll
        for (int i = 0; i < 4; ++i) uv[i] = __builtin_amdgcn_raw_buffer_load_b128(wsr, i * 2048 + lane * 32 + c * 16, WS_UT + (xcd * 32 + idx) * 8192, AUX_SC1);
        lds_wait_ge(cw + 12, 1);
#pragma unroll
        for (int j = 0; j < 32; ++j) lds_st<unsigned>(lw + 4 + j * 16, lo[j]);
        lds_arrive(cw);
        STAMP(10);
      }
      gather_addr();
      if (has_b) {
        lds_wait_ge(C_R + 24, 4);
        const u32x4 z4 = u32x4{0u, 0u, 0u, 0u};
        if (lane < 56) lds_st<u32x4>(cb + 4 * KC_S + (lane >= 28 ? UC_S + 4096 - 28 * 16 : 4096) + lane * 16, z4);
        if (lane < 12) lds_st<u32x4>(cb + (lane & 3) * KC_S + (lane >> 2) * 16, z4);
        if (lane == 0) lds_st<u32x2>(cb + 4 * KC_S + UC_S + 4088, u32x2{0u, 0u});
#pragma unroll
        for (int i = 0; i < 4; ++i) {
          const unsigned a = cb + 4 * KC_S + (i * 64 + lane) * 16;
          lds_st<u32x4>(a, uv[i]); lds_st<u32x2>(a + UC_S - 8, u32x2{uv[i][0], uv[i][1]}); lds_st<u32x2>(a + UC_S, u32x2{uv[i][2], uv[i][3]});
        }
        STAMP(11);
        lds_wait_ge(cw + 4, 1);
      }
    } else {
      unsigned pdt = *(const GAS unsigned short*)(logdt + d * 2), p0[4], p1[4];
      LOAD_P(p0, 0); LOAD_P(p1, 256);
      const u64 epoch = __hip_atomic_load((GAS u64*)(ws + WS_EPOCH), __ATOMIC_RELAXED, __HIP_MEMORY_SCOPE_AGENT);
      gather_addr();
      asm volatile("" : "+v"(pdt), "+v"(p0[0]), "+v"(p0[2]), "+v"(p0[3]), "+v"(p1[2]), "+v"(p1[3]));
      tag = MAKE_TAG(epoch); asm volatile("" :: "s"(tag)); lds_arrive_nw(C_BTAG);
      __builtin_amdgcn_s_setprio(1);
      STAMP(9);
      if (has_b) {
        const float dt = bf_round(fexp(bf_lo(pdt)));
        unsigned cm = 0;
#pragma unroll
        for (int i = 2; i < 4; ++i) cm = umax(cm, umax(umax(p0[i] << 16 & 0x7fff0000u, p0[i] & 0x7fff0000u), umax(p1[i] << 16 & 0x7fff0000u, p1[i] & 0x7fff0000u)));
        unsigned eb = wave_umax(cm) >> 23; eb = eb < 1u ? 1u : (eb > 253u ? 253u : eb);
        const float csc = u2f((254u - eb) << 23), ks = u2f(eb << 23) * 2.f * dt;
        const unsigned hw = tb_ + TAB_HI + (lane >> 2) * 672 + ((lane >> 1) & 1) * 336 + (lane & 1) * 8;
        unsigned lr = tb_ + hh * 528 + r32 * 16, hr = tb_ + TAB_HI + hh * 336 + r32 * 16;
        PIN(lr); PIN(hr);
        f32x16 kd = f32x16{};
        unsigned lo[3][32], hi[4][21];
        auto vals = [&](auto rc) __attribute__((always_inline)) NOPK {
          constexpr int r = decltype(rc)::value;
          const unsigned (&P)[4] = r < 2 ? p0 : p1;
#define HALF(v) ((r == 0 || r == 2) ? bf_hi(v) : bf_lo(v))
          const float rho = -bf_round(fexp(HALF(P[0]))) * dt * LOG2E, th = HALF(P[1]) * dt * 0.15915494309189535f;
          if constexpr (r < 3) {
            lo_vals(rho, th, lo[r]);
#pragma unroll
            for (int j = 1; j < 32; ++j) PIN(lo[r][j]);
          }
          hi_vals(rho, th, f32x2{HALF(P[2]) * csc, HALF(P[3]) * -csc}, hi[r]);
#pragma unroll
          for (int i = 0; i < 21; ++i) PIN(hi[r][i]);
          SBAR();
        };
        h16x8 fa[8], fb[8];
#define FRAGS(s0) do { _Pragma("unroll") for (int s = 0; s < 8; ++s) { fa[s] = lds_ld<h16x8>(hr + ((s0) + s) * 672); fb[s] = lds_ld<h16x8>(lr + ((s0) + s) * 1056); } SBAR(); } while (0)
#define PRODS() do { _Pragma("unroll") for (int s = 0; s < 8; ++s) kd = MFMA32H(fa[s], fb[s], kd); SBAR(); } while (0)
        vals(IC<0>{}); vals(IC<1>{});
        STAMP(1);
        if (c == 0) {
          lds_wait_ge(C_R + 8, 4);
          if (lane == 0) flag_store(ws, WS_UFLAG + (xcd * 32 + idx) * 128, tag);
        }
        vals(IC<2>{}); vals(IC<3>{});
        STAMP(4);
        lds_wait_ge_slow(C_R + GATE, 4);
        STAMP(11);
#pragma unroll
        for (int j = 0; j < 32; ++j) lds_st<u32x2>(lw + j * 16, u32x2{lo[0][j], lo[1][j]});
#pragma unroll
        for (int i = 0; i < 21; ++i) lds_st<u32x2>(hw + i * 16, u32x2{hi[0][i], hi[1][i]});
        FRAGS(0); PRODS(); FRAGS(8);
        lds_arrive(cw + 12);
        STAMP(12);
#pragma unroll
        for (int s = 0; s < 8; ++s) {
          kd = MFMA32H(fa[s], fb[s], kd); SBAR();
#pragma unroll
          for (int j = 4 * s; j < 4 * s + 4; ++j) lds_st<unsigned>(lw + j * 16, lo[2][j]);
#pragma unroll
          for (int i = (s * 21 / 16) * 2; i < ((s + 1) * 21 / 16) * 2; ++i) lds_st<u32x2>(hw + i * 16, u32x2{hi[2][i], hi[3][i]});
          if (s == 7) lds_st<u32x2>(hw + 20 * 16, u32x2{hi[2][20], hi[3][20]});
          SBAR();
        }
        lds_wait_ge(cw, 1);
        FRAGS(0); PRODS(); FRAGS(8); PRODS();
        STAMP(10);
        const unsigned i1 = T + (hh ? 384 + 2 * r32 : 256 + 4 * r32) * 2;
        const unsigned i2 = T + (hh ? (r32 == 0 ? 640 : 643) : 512 + r32) * 2, i3 = T + (hh ? 641 : 512 + r32) * 2, st3 = hh ? 0 : 64;
        lds_st<u32x2>(T + (8 * r32 + 4 * hh) * 2, u32x2{pk_bf16(kd[0] * ks, kd[1] * ks), pk_bf16(kd[2] * ks, kd[3] * ks)});
        lds_st<unsigned>(i1, pk_bf16(kd[4] * ks, kd[5] * ks)); lds_st<unsigned>(i1 + (hh ? 128 : 4), pk_bf16(kd[6] * ks, kd[7] * ks));
        const unsigned w0 = pk_bf16(kd[8] * ks, kd[9] * ks), w1 = hh ? 0u : pk_bf16(kd[10] * ks, kd[11] * ks);
        lds_st<short>(i2, (short)w0); lds_st<short>(i3 + st3, hh ? (short)0 : (short)(w0 >> 16)); lds_st<short>(i3 + 2 * st3, (short)w1); lds_st<short>(i3 + 3 * st3, (short)(w1 >> 16));
        lds_arrive(cw + 4);
        lds_wait_ge(C_R + 24, 4);
      }
    }
    unsigned wv[7];
#pragma unroll
    for (int i = 0; i < 7; ++i) {
      const unsigned p = i * 64 + lane, row = (p * 37450u) >> 19; wv[i] = row * 896 + (p - row * 14) * 16;
    }
    if (has_b) {
      STAMP(2);
      {
        s16x2 P[2][6];
#pragma unroll
        for (int it = 0; it < 2; ++it)
#pragma unroll
          for (int k = 0; k < 11; ++k) P[it][k >> 1][k & 1] = lds_ld<short>(ga[it][k]);
#pragma unroll
        for (int it = 0; it < 2; ++it) {
          unsigned Q[6], A[5];
#pragma unroll
          for (int k = 0; k < 6; ++k) Q[k] = __builtin_bit_cast(unsigned, P[it][k]);
#pragma unroll
          for (int k = 0; k < 5; ++k) A[k] = __builtin_amdgcn_alignbit(Q[k + 1], Q[k], 16);
          const unsigned a = cb + (3 + it * 128 + half * 64 + lane) * 16;
          lds_st<u32x4>(a, u32x4{Q[0], Q[1], Q[2], Q[3]}); lds_st<u32x4>(a + KC_S, u32x4{A[0], A[1], A[2], A[3]});
          lds_st<u32x4>(a + 2 * KC_S, u32x4{Q[1], Q[2], Q[3], Q[4]}); lds_st<u32x4>(a + 3 * KC_S, u32x4{A[1], A[2], A[3], A[4]});
        }
      }
      lds_arrive(cw + 8);
      asm volatile("" : "+v"(wv[0]), "+v"(wv[1]), "+v"(wv[2]), "+v"(wv[3]), "+v"(wv[4]), "+v"(wv[5]), "+v"(wv[6]));
      lds_wait_ge(cw + 8, 2);
      __builtin_amdgcn_s_setprio(BCONV);
      STAMP(5);
      f32x4 y[4];
#pragma unroll
      for (int i = 0; i < 4; ++i) y[i] = f32x4{0.f, 0.f, 0.f, 0.f};
      const int cc = lane & 15, kq = lane >> 4;
      {
        unsigned ka = cb + (cc & 3) * KC_S + ((cc >> 2) + kq) * 16;
        unsigned ua = cb + 4 * KC_S + (1 - (cc & 1)) * UC_S + (2016 - 32 * (cc >> 1) + 8 * kq) * 2 - 7 * 512;
        PIN(ka); PIN(ua);
        auto conv = [&](auto hc) __attribute__((always_inline)) NOPK {
          constexpr int H = decltype(hc)::value, NF = H ? 6 : 8;
          s16x8 af[2][NF], bf[2][NF];
#pragma unroll
          for (int m = 0; m < NF; ++m) { bf[0][m] = lds_ld<s16x8>(ua + (7 - m) * 512); af[0][m] = lds_ld<s16x8>(ka + m * 512); }
          auto conv_class = [&](auto sc, auto oc) __attribute__((always_inline)) NOPK {
            constexpr int S = decltype(sc)::value, O = decltype(oc)::value;
            int k = 0;
#pragma unroll
            for (int m = 0; m < NF; ++m)
#pragma unroll
              for (int i = 0; i < 4; ++i) {
                const int n = (H ? 2 + i : (i < 2 ? i : 4 + i)) - m;
                if (n >= 0) {
                  y[i] = MFMA16(af[S][m], bf[S][n], y[i]);
                  if (k < 2 * NF) {
                    constexpr int o0[16] = {0, 8, 9, 14, 15, 1, 13, 2, 12, 3, 11, 4, 10, 5, 6, 7}, o1[16] = {0, 10, 11, 12, 13, 1, 9, 2, 8, 3, 4, 5, 0, 0, 0, 0};
                    const int q = H ? o1[k] : o0[k];
                    if (q < 8) af[1 - S][q] = lds_ld<s16x8>(ka + O + q * 512); else bf[1 - S][q - 8] = lds_ld<s16x8>(ua + O + (15 - q) * 512);
                  }
                  SBAR();
                  ++k;
                }
              }
          };
#pragma unroll 1
          for (int it = 0; it < 4; ++it) {
            conv_class(IC<0>{}, IC<64>{});
            conv_class(IC<1>{}, IC<128>{});
            ka += 128; ua += 128;
          }
        };
        if (half) conv(IC<1>{}); else conv(IC<0>{});
      }
      PIN(y[3]); STAMP(13);
      __builtin_amdgcn_s_setprio(2);
#pragma unroll
      for (int i = 0; i < 4; ++i) {
        const int t64 = 4 * (half ? 2 + i : (i < 2 ? i : 4 + i)) + (cc >> 2);
        __builtin_amdgcn_raw_buffer_store_b64(u32x2{pk_bf16(y[i][0], y[i][1]), pk_bf16(y[i][2], y[i][3])}, wsr,
                                              ((xcd * 32 + t64) * 64 + idx * 2 + c) * 128 + (32 * ((cc >> 1) & 1) + 8 * kq + 4 * (cc & 1)) * 2, WS_YT, AUX_SC1);
      }
      lds_arrive(C_BFREE);
      __builtin_amdgcn_s_setprio(0);
      STAMP(15);
    } else {
      lds_arrive(C_BFREE);
      __builtin_amdgcn_s_setprio(0);
    }
    lds_wait_ge_slow(C_QR, 4);
    lds_wait_ge_slow(C_BFREE, 4);
#pragma unroll 1
    for (int i = wb; i < 14; i += 4) {
      const char* sb = wo + i * 32 * 896 + (idx & 3) * 224;
      const unsigned dst = L_RING + i * 7168;
#define WO_PIECE(k) glds<(k) * 1024>(wv[k], sb, dst); asm volatile("s_waitcnt vmcnt(2)" ::: "memory")
      WO_PIECE(0); WO_PIECE(1); WO_PIECE(2);
      if (i < 4 && has_b) {
        if (last_of_four(C_BPUB, lane, 0)) { if (lane == 0) flag_store(ws, WS_YFLAG + (xcd * 32 + idx) * 128, tag); }
        STAMP(6);
      }
      WO_PIECE(3); WO_PIECE(4); WO_PIECE(5); WO_PIECE(6);
    }
    drain_vm();
    lds_arrive(C_WO);
    STAMP(7);
    if (has_b) {
      const int tl = wb * 16 + (lane & 15);
      u32x4 uu[2], ds[2];
#pragma unroll
      for (int i = 0; i < 2; ++i) {
        const int oct = (lane >> 4) + 4 * i;
        uu[i] = *(const GAS u32x4*)(u + (size_t)(idx * 64 * 896 + xcd * 128) + (unsigned)(tl * 896 + oct * 16));
        ds[i] = *(const GAS u32x4*)(Dsk + xcd * 128 + (unsigned)(oct * 16));
      }
      if (wb == 0) flag_wait(ws, WS_YFLAG + (xcd * 32 + r32) * 128, tag);
      role_sync_idle(C_B, 1);
      u32x4 v[2];
#pragma unroll
      for (int i = 0; i < 2; ++i) v[i] = __builtin_amdgcn_raw_buffer_load_b128(wsr, wb * 2048 + i * 1024 + lane * 16, WS_YT + (xcd * 32 + idx) * 8192, AUX_SC1);
#pragma unroll
      for (int i = 0; i < 2; ++i) lds_st<u32x4>(L_YIMG + wb * 2048 + i * 1024 + lane * 16, v[i]);
      role_sync_idle(C_B, 2);
#pragma unroll
      for (int i = 0; i < 2; ++i) {
        const int oct = (lane >> 4) + 4 * i;
        const unsigned a = L_YIMG + (8 * oct + ((lane & 15) >> 2)) * 128 + (wb * 16 + (lane & 3) * 4) * 2;
        const u32x4 yy = __builtin_bit_cast(u32x4, cat8(tr_read(a), tr_read(a + 512)));
        u32x4 r;
#pragma unroll
        for (int k = 0; k < 4; ++k) {
          const unsigned sk = pk_bf16(bf_lo(uu[i][k]) * bf_lo(ds[i][k]), bf_hi(uu[i][k]) * bf_hi(ds[i][k]));
          r[k] = pk_bf16(bf_lo(yy[k]) + bf_lo(sk), bf_hi(yy[k]) + bf_hi(sk));
        }
        *(GAS u32x4*)(out + (size_t)(idx * 64 * 1792 + 896 + xcd * 128) + (unsigned)(tl * 1792 + oct * 16)) = r;
      }
      STAMP(8);
    } else if (b == 7 && wb == 0) {
#pragma unroll 1
      for (int i = 0; i < 4; ++i) rec_wait(wsr, WS_QFLAG + (i * 64 + lane) * 128, tag);
      if (lane == 0) flag_store(ws, WS_EPOCH, tag);
    }
    STAMP_FLUSH();
  }
}

extern "C" void kernel_launch(void* const* d_in, const int* in_sizes, int n_in, void* d_out, int out_size, void* d_ws, size_t ws_size,
                              hipStream_t stream) {
  (void)in_sizes; (void)n_in; (void)out_size; (void)ws_size;
  (void)hipFuncSetAttribute((const void*)fused_mha_s4d, hipFuncAttributeMaxDynamicSharedMemorySize, LDS_BYTES);
  fused_mha_s4d<<<256, 512, LDS_BYTES, stream>>>((const char*)d_in[0], (const char*)d_in[1], (const char*)d_in[2], (const char*)d_in[3],
                                                 (const char*)d_in[4], (const char*)d_in[5], (const char*)d_in[6], (const char*)d_in[7],
                                                 (const char*)d_in[8], (const char*)d_in[9], (char*)d_out,
                                                 (char*)d_ws + (WSEND ? ((ws_size - WS_END) & ~(size_t)0xfffff) : 0));
}
```
